```python
import torch

def _softmax(x, axis=-1):
    return torch.softmax(x, dim=axis)
def _gelu(x):
    return torch.nn.functional.gelu(x, approximate="tanh")
def _silu(x):
    return torch.nn.functional.silu(x)
def _sigmoid(x):
    return torch.sigmoid(x)
def _layernorm(x, axis=-1, eps=1e-5):
    m = x.mean(dim=axis, keepdim=True)
    v = x.var(dim=axis, keepdim=True, unbiased=False)
    return (x - m) / torch.sqrt(v + eps)
def _scatter_add(tgt, idx, src, axis=0):
    return tgt.clone().index_add_(axis, idx, src)
def _segment_sum(src, seg, n):
    out = torch.zeros((n,) + src.shape[1:], dtype=src.dtype)
    return out.index_add_(0, seg, src)
def _pad(x, pw):
    flat = tuple(v for pair in reversed(pw) for v in pair)
    return torch.nn.functional.pad(x, flat)

kernel_name = 'standalone__paged_block_table_kv_radixattention__sampling'

def setup_inputs():

    _w = (lambda a: (lambda t: t.to(torch.bfloat16) if t.is_floating_point() else t)(torch.as_tensor(a)))
    B, H, Hkv, Dh = 8, 32, 8, 128
    Lk = 8192
    bs = 16
    nb = max(2, Lk // bs)
    P = max(1, nb // 2)
    n_phys = P + B * (nb - P)
    shared = torch.arange(P, dtype=torch.int64)
    priv = P + torch.arange(B * (nb - P), dtype=torch.int64).reshape(B, nb - P)
    bt = torch.cat([torch.tile(shared[None, :], (B, 1)), priv], dim=1)
    ctx = nb * bs - (torch.arange(1, B + 1, dtype=torch.int64) * 2654435761) % max(1, (nb * bs - P * bs) // 2)
    return {
        "q": _w((lambda *s: torch.randn(*s, dtype=torch.bfloat16) * 0.8)(B, H, Dh)),
        "k_cache": _w((lambda *s: torch.randn(*s, dtype=torch.bfloat16) * 0.8)(n_phys, bs, Hkv, Dh)),
        "v_cache": _w((lambda *s: torch.randn(*s, dtype=torch.bfloat16) * 0.05)(n_phys, bs, Hkv, Dh)),
        "block_table": _w(bt.to(torch.int64)),
        "ctx_lens": _w(torch.as_tensor(ctx).to(torch.int64)),
    }

def reference(q, k_cache, v_cache, block_table, ctx_lens):
    B, H, Dh = q.shape
    n_phys, bs, Hkv, _ = k_cache.shape
    nb = block_table.shape[1]
    Lk = nb * bs
    rep = H // Hkv
    k = k_cache[block_table].reshape(B, Lk, Hkv, Dh)
    v = v_cache[block_table].reshape(B, Lk, Hkv, Dh)
    k = (lambda x, n, axis: torch.repeat_interleave(x, n, dim=axis))(k, rep, axis=2)
    v = (lambda x, n, axis: torch.repeat_interleave(x, n, dim=axis))(v, rep, axis=2)
    scores = torch.einsum("bhd,bkhd->bhk", q, k) * (Dh ** -0.5)
    pos = torch.arange(Lk)
    valid = pos[None, :] < ctx_lens[:, None]
    scores = torch.where(valid[:, None, :], scores, -1e9)
    p = _softmax(scores, axis=-1)
    return torch.einsum("bhk,bkhd->bhd", p, v)
```

```cpp
#include <hip/hip_runtime.h>
#include <hip/hip_bf16.h>
#include <stdint.h>

#define KV_AUX 0

namespace {

constexpr int NBATCH = 8, NH = 32, NKVH = 8, HD = 128, PAGE = 16, NBLK = 512, LK = NBLK * PAGE;
constexpr int NPART = 32, NWAVE = 8, NTHR = NWAVE * 64, NCOL = 32;
constexpr int TOK_BYTES = NKVH * HD * 2;
constexpr int PAGE_BYTES = PAGE * TOK_BYTES;
constexpr float SC_LOG2 = 0.08838834764831845f * 1.4426950408889634f;

typedef __attribute__((ext_vector_type(8))) __bf16 bf16x8;
typedef __attribute__((ext_vector_type(4))) float f32x4;
typedef __attribute__((ext_vector_type(4))) unsigned u32x4;
typedef __attribute__((ext_vector_type(2))) unsigned u32x2;

constexpr int ROW_F = HD + 4;
constexpr int LDS_O = 0;
constexpr int LDS_ML = LDS_O + NWAVE * NCOL * ROW_F * 4;
constexpr int LDS_CNT = LDS_ML + NWAVE * NCOL * 8;
constexpr int LDS_LIST = LDS_CNT + 256;
constexpr int LDS_Q = LDS_LIST + 128 * 8;
constexpr int LDS_BYTES = LDS_Q + 8192;

constexpr size_t WS_ML = 0;
constexpr size_t WS_PO = (size_t)NKVH * NPART * NCOL * 8;

__device__ __forceinline__ f32x4 mfma16(u32x4 a, u32x4 b, f32x4 c) {
  return __builtin_amdgcn_mfma_f32_16x16x32_bf16(__builtin_bit_cast(bf16x8, a), __builtin_bit_cast(bf16x8, b), c, 0, 0, 0);
}
__device__ __forceinline__ unsigned cvtpk(float lo, float hi) {
  unsigned r;
  asm("v_cvt_pk_bf16_f32 %0, %1, %2" : "=v"(r) : "v"(lo), "v"(hi));
  return r;
}
__device__ __forceinline__ float xmax(float x) {
  auto s = __builtin_amdgcn_permlane16_swap(__float_as_uint(x), __float_as_uint(x), false, false);
  x = fmaxf(__uint_as_float(s[0]), __uint_as_float(s[1]));
  auto t = __builtin_amdgcn_permlane32_swap(__float_as_uint(x), __float_as_uint(x), false, false);
  return fmaxf(__uint_as_float(t[0]), __uint_as_float(t[1]));
}
__device__ __forceinline__ float xsum(float x) {
  auto s = __builtin_amdgcn_permlane16_swap(__float_as_uint(x), __float_as_uint(x), false, false);
  x = __uint_as_float(s[0]) + __uint_as_float(s[1]);
  auto t = __builtin_amdgcn_permlane32_swap(__float_as_uint(x), __float_as_uint(x), false, false);
  return __uint_as_float(t[0]) + __uint_as_float(t[1]);
}

struct Pair {
  u32x4 k[2][4];
  u32x4 v[2][4];
};

__device__ __forceinline__ void load_page(const char* kbase, const char* vbase, int phys, int nbytes, int koff, int voff,
                                          u32x4 (&k)[4], u32x4 (&v)[4]) {
  const size_t off = (size_t)(unsigned)phys * PAGE_BYTES;
  auto rk = __builtin_amdgcn_make_buffer_rsrc((void*)(kbase + off), 0, nbytes, 0x00020000);
  auto rv = __builtin_amdgcn_make_buffer_rsrc((void*)(vbase + off), 0, nbytes, 0x00020000);
#pragma unroll
  for (int i = 0; i < 4; ++i) k[i] = __builtin_amdgcn_raw_buffer_load_b128(rk, koff + 64 * i, 0, KV_AUX);
  __builtin_amdgcn_sched_barrier(0);
#pragma unroll
  for (int j = 0; j < 4; ++j) v[j] = __builtin_amdgcn_raw_buffer_load_b128(rv, voff + TOK_BYTES * j, 0, KV_AUX);
  __builtin_amdgcn_sched_barrier(0);
}

__global__ __launch_bounds__(NTHR, 2) void attn_split(const __hip_bfloat16* __restrict__ q, const char* __restrict__ kc,
                                                      const char* __restrict__ vc, const int* __restrict__ bt,
                                                      const int* __restrict__ lens, float* __restrict__ wml,
                                                      float* __restrict__ wpo) {
  extern __shared__ __attribute__((aligned(16))) char smem[];
  const int tid = threadIdx.x, lane = tid & 63, wave = __builtin_amdgcn_readfirstlane(tid >> 6);
  const int g = lane >> 4, c = lane & 15;
  const int kvh = blockIdx.x & 7, part = blockIdx.x >> 3;

  int y[NBATCH], len[NBATCH];
#pragma unroll
  for (int b = 0; b < NBATCH; ++b) y[b] = bt[b * NBLK + tid];
  unsigned zeroq = 0;
#pragma unroll
  for (int b = 0; b < NBATCH; ++b) {
    int l = lens[b];
    if (l <= 0) { zeroq |= 1u << b; l = LK; }
    len[b] = l > LK ? LK : l;
  }
  u32x4* qlds = (u32x4*)(smem + LDS_Q);
  {
    const int b = 4 * (wave >> 2) + (c >> 2);
    const u32x4 t = *(const u32x4*)((const char*)q + ((b * NH + 4 * kvh + (c & 3)) * HD + 8 * g + 32 * (wave & 3)) * 2);
    qlds[tid] = ((zeroq >> b) & 1) ? u32x4{0, 0, 0, 0} : t;
  }
  int lenv[2];
#pragma unroll
  for (int cg = 0; cg < 2; ++cg) {
    const int cb = c >> 2;
    lenv[cg] = cb == 0 ? len[4 * cg] : cb == 1 ? len[4 * cg + 1] : cb == 2 ? len[4 * cg + 2] : len[4 * cg + 3];
  }
#pragma unroll
  for (int b = 0; b < NBATCH; ++b) y[b] = (tid * PAGE < len[b]) ? y[b] : -1 - b;
  unsigned msk[NBATCH];
#pragma unroll
  for (int b = 0; b < NBATCH; ++b) msk[b] = 1u << b;
#pragma unroll
  for (int a = 0; a < NBATCH; ++a)
#pragma unroll
    for (int b = a + 1; b < NBATCH; ++b) {
      const bool e = y[a] == y[b];
      msk[a] |= e ? (1u << b) : 0u;
      msk[b] |= e ? (1u << a) : 0u;
    }
  int* cnt = (int*)(smem + LDS_CNT);
  u32x2* list = (u32x2*)(smem + LDS_LIST);
  bool lead[NBATCH];
  int pre[NBATCH];
  int mycnt = 0;
#pragma unroll
  for (int b = 0; b < NBATCH; ++b) {
    lead[b] = y[b] >= 0 && (msk[b] & ((1u << b) - 1)) == 0;
    const unsigned long long bal = __ballot(lead[b]);
    pre[b] = __builtin_amdgcn_mbcnt_hi((unsigned)(bal >> 32), __builtin_amdgcn_mbcnt_lo((unsigned)bal, 0));
    const int n = __popcll(bal);
    mycnt = lane == b ? n : mycnt;
  }
  if (lane < NBATCH) cnt[lane * NWAVE + wave] = mycnt;
  __syncthreads();
  int incl = cnt[lane];
#pragma unroll
  for (int d = 1; d < 64; d <<= 1) {
    const int t = __shfl_up(incl, d);
    incl += lane >= d ? t : 0;
  }
  const int excl = incl - cnt[lane];
  const int total = __builtin_amdgcn_readlane(incl, 63);
  const int i0 = (total * part) / NPART, i1 = (total * (part + 1)) / NPART;
  const int nitem = i1 - i0;
#pragma unroll
  for (int b = 0; b < NBATCH; ++b) {
    const int idx = __builtin_amdgcn_readlane(excl, b * NWAVE + wave) + pre[b] - i0;
    if (lead[b] && idx >= 0 && idx < nitem) {
      int mx = 0;
#pragma unroll
      for (int a = 0; a < NBATCH; ++a) mx = ((msk[b] >> a) & 1) ? max(mx, len[a]) : mx;
      const int ntok = min(PAGE, mx - tid * PAGE);
      list[idx] = u32x2{(unsigned)y[b], (unsigned)tid | (msk[b] << 16) | ((unsigned)ntok << 24)};
    }
  }
  __syncthreads();
  u32x2 it = {0u, 0u};
  {
    const int idx = 2 * (wave + NWAVE * (lane >> 1)) + (lane & 1);
    if (idx < nitem) it = list[idx];
  }
  const int npair_all = (nitem + 1) >> 1;
  const int npair = (npair_all - wave + NWAVE - 1) / NWAVE;

  const char* kbase = kc + kvh * (HD * 2);
  const char* vbase = vc + kvh * (HD * 2);
  const int koff = c * TOK_BYTES + g * 16;
  const int voff = 4 * g * TOK_BYTES + c * 16;

  float m[2] = {-INFINITY, -INFINITY}, l[2] = {0.f, 0.f};
  f32x4 acc[2][8];
#pragma unroll
  for (int cg = 0; cg < 2; ++cg)
#pragma unroll
    for (int i = 0; i < 8; ++i) acc[cg][i] = f32x4{0.f, 0.f, 0.f, 0.f};

  auto issue = [&](Pair& p, int k) {
#pragma unroll
    for (int s = 0; s < 2; ++s) {
      const int e = (2 * k + s) & 63;
      const int phys = __builtin_amdgcn_readlane((int)it[0], e);
      const unsigned meta = (unsigned)__builtin_amdgcn_readlane((int)it[1], e);
      const int ntok = meta >> 24;
      const int nbytes = ntok > 0 ? (ntok - 1) * TOK_BYTES + HD * 2 : 0;
      load_page(kbase, vbase, phys, nbytes, koff, voff, p.k[s], p.v[s]);
    }
  };
  auto compute = [&](const Pair& p, int k) {
    const unsigned meta0 = (unsigned)__builtin_amdgcn_readlane((int)it[1], (2 * k) & 63);
    const unsigned meta1 = (unsigned)__builtin_amdgcn_readlane((int)it[1], (2 * k + 1) & 63);
    const unsigned um = ((meta0 | meta1) >> 16) & 0xffu;
    u32x4 va[8];
#pragma unroll
    for (int i = 0; i < 8; ++i) {
      const unsigned sel = (i & 1) ? 0x07060302u : 0x05040100u;
      va[i][0] = __builtin_amdgcn_perm(p.v[0][1][i >> 1], p.v[0][0][i >> 1], sel);
      va[i][1] = __builtin_amdgcn_perm(p.v[0][3][i >> 1], p.v[0][2][i >> 1], sel);
      va[i][2] = __builtin_amdgcn_perm(p.v[1][1][i >> 1], p.v[1][0][i >> 1], sel);
      va[i][3] = __builtin_amdgcn_perm(p.v[1][3][i >> 1], p.v[1][2][i >> 1], sel);
    }
#pragma unroll
    for (int cg = 0; cg < 2; ++cg) {
      if ((um >> (4 * cg)) & 0xfu) {
        f32x4 s0 = {0.f, 0.f, 0.f, 0.f}, s1 = {0.f, 0.f, 0.f, 0.f};
#pragma unroll
        for (int i = 0; i < 4; ++i) {
          const u32x4 qf = qlds[(cg * 4 + i) * 64 + lane];
          s0 = mfma16(p.k[0][i], qf, s0);
          s1 = mfma16(p.k[1][i], qf, s1);
        }
        const int bit = 16 + 4 * cg + (c >> 2);
        const int lim0 = ((meta0 >> bit) & 1) ? lenv[cg] - (int)(meta0 & 0xffffu) * PAGE - 4 * g : 0;
        const int lim1 = ((meta1 >> bit) & 1) ? lenv[cg] - (int)(meta1 & 0xffffu) * PAGE - 4 * g : 0;
        float x[8];
#pragma unroll
        for (int r = 0; r < 4; ++r) {
          x[r] = r < lim0 ? s0[r] * SC_LOG2 : -INFINITY;
          x[4 + r] = r < lim1 ? s1[r] * SC_LOG2 : -INFINITY;
        }
        float mx = fmaxf(fmaxf(fmaxf(x[0], x[1]), fmaxf(x[2], x[3])), fmaxf(fmaxf(x[4], x[5]), fmaxf(x[6], x[7])));
        mx = xmax(mx);
        if (__any(mx > m[cg])) {
          const float mn = fmaxf(m[cg], mx);
          const float alpha = mn == -INFINITY ? 1.f : __builtin_amdgcn_exp2f(m[cg] - mn);
          m[cg] = mn;
          l[cg] *= alpha;
#pragma unroll
          for (int i = 0; i < 8; ++i) acc[cg][i] *= alpha;
        }
        const float ms = m[cg] == -INFINITY ? 0.f : m[cg];
        float ps = 0.f;
#pragma unroll
        for (int r = 0; r < 8; ++r) { x[r] = __builtin_amdgcn_exp2f(x[r] - ms); ps += x[r]; }
        l[cg] += ps;
        const u32x4 pb = {cvtpk(x[0], x[1]), cvtpk(x[2], x[3]), cvtpk(x[4], x[5]), cvtpk(x[6], x[7])};
#pragma unroll
        for (int i = 0; i < 8; ++i) acc[cg][i] = mfma16(va[i], pb, acc[cg][i]);
      }
    }
  };

  Pair pa, pb;
  issue(pa, 0);
  issue(pb, 1);
  for (int k = 0; k < npair; k += 2) {
    compute(pa, k);
    issue(pa, k + 2);
    compute(pb, k + 1);
    issue(pb, k + 3);
  }

  float* lo = (float*)(smem + LDS_O);
  float* lml = (float*)(smem + LDS_ML);
#pragma unroll
  for (int cg = 0; cg < 2; ++cg) {
    l[cg] = xsum(l[cg]);
    if (g == 0) { lml[(wave * NCOL + 16 * cg + c) * 2] = m[cg]; lml[(wave * NCOL + 16 * cg + c) * 2 + 1] = l[cg]; }
    if (m[cg] != -INFINITY) {
      float* row = lo + (wave * NCOL + 16 * cg + c) * ROW_F + 32 * g;
#pragma unroll
      for (int r = 0; r < 4; ++r) {
        *(f32x4*)(row + 8 * r) = f32x4{acc[cg][0][r], acc[cg][1][r], acc[cg][2][r], acc[cg][3][r]};
        *(f32x4*)(row + 8 * r + 4) = f32x4{acc[cg][4][r], acc[cg][5][r], acc[cg][6][r], acc[cg][7][r]};
      }
    }
  }
  __syncthreads();
  const size_t slot0 = (size_t)(kvh * NPART + part) * NCOL;
#pragma unroll
  for (int h = 0; h < 2; ++h) {
    const int col = h * 16 + (tid >> 5), d4 = (tid & 31) * 4;
    float mw[NWAVE], M = -INFINITY;
#pragma unroll
    for (int w = 0; w < NWAVE; ++w) { mw[w] = lml[(w * NCOL + col) * 2]; M = fmaxf(M, mw[w]); }
    float L = 0.f;
    f32x4 o = {0.f, 0.f, 0.f, 0.f};
    if (M != -INFINITY) {
#pragma unroll
      for (int w = 0; w < NWAVE; ++w) {
        if (mw[w] != -INFINITY) {
          const float f = __builtin_amdgcn_exp2f(mw[w] - M);
          L += f * lml[(w * NCOL + col) * 2 + 1];
          o += f * *(const f32x4*)(lo + (w * NCOL + col) * ROW_F + d4);
        }
      }
      *(f32x4*)(wpo + (slot0 + col) * HD + d4) = o;
    }
    if ((tid & 31) == 0) *(float2*)(wml + (slot0 + col) * 2) = make_float2(M, L);
  }
}

__global__ __launch_bounds__(64) void attn_combine(const float* __restrict__ wml, const float* __restrict__ wpo,
                                                   __hip_bfloat16* __restrict__ out) {
  const int row = blockIdx.x, b = row >> 5, h = row & 31, kvh = h >> 2, col = 4 * b + (h & 3);
  const int lane = threadIdx.x;
  const float2 ml = *(const float2*)(wml + ((size_t)(kvh * NPART + (lane & 31)) * NCOL + col) * 2);
  float M = ml.x;
#pragma unroll
  for (int d = 1; d < 32; d <<= 1) M = fmaxf(M, __shfl_xor(M, d));
  const float f = ml.x == -INFINITY ? 0.f : __builtin_amdgcn_exp2f(ml.x - M);
  float L = f * ml.y;
#pragma unroll
  for (int d = 1; d < 32; d <<= 1) L += __shfl_xor(L, d);
  unsigned live = (unsigned)__ballot(ml.x != -INFINITY);
  float o0 = 0.f, o1 = 0.f;
  while (live) {
    float2 a[4];
    float w[4];
#pragma unroll
    for (int u = 0; u < 4; ++u) {
      const bool ok = live != 0;
      const int p = ok ? __builtin_ctz(live) : 0;
      live &= live - 1;
      w[u] = __shfl(f, p);
      a[u] = ok ? *(const float2*)(wpo + ((size_t)(kvh * NPART + p) * NCOL + col) * HD + 2 * lane) : make_float2(0.f, 0.f);
    }
#pragma unroll
    for (int u = 0; u < 4; ++u) { o0 += w[u] * a[u].x; o1 += w[u] * a[u].y; }
  }
  const float inv = 1.f / L;
  *(unsigned*)(out + row * HD + 2 * lane) = cvtpk(o0 * inv, o1 * inv);
}

}

extern "C" void kernel_launch(void* const* d_in, const int* in_sizes, int n_in, void* d_out, int out_size, void* d_ws,
                              size_t ws_size, hipStream_t stream) {
  (void)in_sizes; (void)n_in; (void)out_size; (void)ws_size;
  (void)hipFuncSetAttribute((const void*)attn_split, hipFuncAttributeMaxDynamicSharedMemorySize, LDS_BYTES);
  float* wml = (float*)((char*)d_ws + WS_ML);
  float* wpo = (float*)((char*)d_ws + WS_PO);
  hipLaunchKernelGGL(attn_split, dim3(NKVH * NPART), dim3(NTHR), LDS_BYTES, stream, (const __hip_bfloat16*)d_in[0],
                     (const char*)d_in[1], (const char*)d_in[2], (const int*)d_in[3], (const int*)d_in[4], wml, wpo);
  hipLaunchKernelGGL(attn_combine, dim3(NBATCH * NH), dim3(64), 0, stream, (const float*)wml, (const float*)wpo,
                     (__hip_bfloat16*)d_out);
}
```

```cpp
#define PROBE 0
#define MULT 0ull
#include <hip/hip_runtime.h>
#include <hip/hip_bf16.h>
#include <stdint.h>

#define KV_AUX 2

#define STAMPS
#include <vector>
#include <algorithm>
#include <cstdio>
#define NSTAMP 8
#define STAMP_OFF (64u << 20)
#define STAMP(i) do { if (threadIdx.x == 0) { unsigned long long t_; asm volatile("s_memrealtime %0\n\ts_waitcnt lgkmcnt(0)" : "=s"(t_) :: "memory"); ((unsigned long long*)((char*)wml + STAMP_OFF))[blockIdx.x * NSTAMP + (i)] = t_; } } while (0)
static void stamp_dump(void* ws) {
  std::vector<unsigned long long> h(256 * NSTAMP);
  (void)hipMemcpy(h.data(), (char*)ws + STAMP_OFF, h.size() * 8, hipMemcpyDeviceToHost);
  unsigned long long t0 = ~0ull; for (int b = 0; b < 256; ++b) t0 = std::min(t0, h[b * NSTAMP]);
  for (int i = 0; i < NSTAMP; ++i) { std::vector<double> v; for (int b = 0; b < 256; ++b) v.push_back((double)(long long)(h[b * NSTAMP + i] - t0) * 0.01);
    std::sort(v.begin(), v.end()); printf("stamp %d: min %.2f p10 %.2f med %.2f p90 %.2f max %.2f us\n", i, v[0], v[25], v[128], v[230], v[255]); }
}
#define STAMP_DUMP stamp_dump
namespace {

constexpr int NBATCH = 8, NH = 32, NKVH = 8, HD = 128, PAGE = 16, NBLK = 512, LK = NBLK * PAGE;
constexpr int NPART = 32, NWAVE = 8, NTHR = NWAVE * 64, NCOL = 32;
constexpr int TOK_BYTES = NKVH * HD * 2;
constexpr int PAGE_BYTES = PAGE * TOK_BYTES;
constexpr float SC_LOG2 = 0.08838834764831845f * 1.4426950408889634f;

typedef __attribute__((ext_vector_type(8))) __bf16 bf16x8;
typedef __attribute__((ext_vector_type(4))) float f32x4;
typedef __attribute__((ext_vector_type(4))) unsigned u32x4;
typedef __attribute__((ext_vector_type(2))) unsigned u32x2;

constexpr int ROW_F = HD + 4;
constexpr int LDS_O = 0;
constexpr int LDS_ML = LDS_O + NWAVE * NCOL * ROW_F * 4;
constexpr int LDS_CNT = LDS_ML + NWAVE * NCOL * 8;
constexpr int LDS_LIST = LDS_CNT + 256;
constexpr int LDS_Q = LDS_LIST + 128 * 8;
constexpr int LDS_BYTES = LDS_Q + 8192;

constexpr size_t WS_ML = 0;
constexpr size_t WS_PO = (size_t)NKVH * NPART * NCOL * 8;

__device__ __forceinline__ f32x4 mfma16(u32x4 a, u32x4 b, f32x4 c) {
  return __builtin_amdgcn_mfma_f32_16x16x32_bf16(__builtin_bit_cast(bf16x8, a), __builtin_bit_cast(bf16x8, b), c, 0, 0, 0);
}
__device__ __forceinline__ unsigned cvtpk(float lo, float hi) {
  unsigned r;
  asm("v_cvt_pk_bf16_f32 %0, %1, %2" : "=v"(r) : "v"(lo), "v"(hi));
  return r;
}
__device__ __forceinline__ float xmax(float x) {
  auto s = __builtin_amdgcn_permlane16_swap(__float_as_uint(x), __float_as_uint(x), false, false);
  x = fmaxf(__uint_as_float(s[0]), __uint_as_float(s[1]));
  auto t = __builtin_amdgcn_permlane32_swap(__float_as_uint(x), __float_as_uint(x), false, false);
  return fmaxf(__uint_as_float(t[0]), __uint_as_float(t[1]));
}
__device__ __forceinline__ float xsum(float x) {
  auto s = __builtin_amdgcn_permlane16_swap(__float_as_uint(x), __float_as_uint(x), false, false);
  x = __uint_as_float(s[0]) + __uint_as_float(s[1]);
  auto t = __builtin_amdgcn_permlane32_swap(__float_as_uint(x), __float_as_uint(x), false, false);
  return __uint_as_float(t[0]) + __uint_as_float(t[1]);
}

struct Pair {
  u32x4 k[2][4];
  u32x4 v[2][4];
};

__device__ __forceinline__ void load_page(const char* kbase, const char* vbase, int phys, int nbytes, int koff, int voff,
                                          u32x4 (&k)[4], u32x4 (&v)[4]) {
  const size_t off = (size_t)(unsigned)phys * PAGE_BYTES;
  auto rk = __builtin_amdgcn_make_buffer_rsrc((void*)(kbase + off), 0, nbytes, 0x00020000);
  auto rv = __builtin_amdgcn_make_buffer_rsrc((void*)(vbase + off), 0, nbytes, 0x00020000);
#pragma unroll
  for (int i = 0; i < 4; ++i) k[i] = __builtin_amdgcn_raw_buffer_load_b128(rk, koff + 64 * i, 0, KV_AUX);
  __builtin_amdgcn_sched_barrier(0);
#pragma unroll
  for (int j = 0; j < 4; ++j) v[j] = __builtin_amdgcn_raw_buffer_load_b128(rv, voff + TOK_BYTES * j, 0, KV_AUX);
  __builtin_amdgcn_sched_barrier(0);
}

__global__ __launch_bounds__(NTHR, 2) void attn_split(const __hip_bfloat16* __restrict__ q, const char* __restrict__ kc,
                                                      const char* __restrict__ vc, const int* __restrict__ bt,
                                                      const int* __restrict__ lens, float* __restrict__ wml,
                                                      float* __restrict__ wpo) {
  extern __shared__ __attribute__((aligned(16))) char smem[];
  const int tid = threadIdx.x, lane = tid & 63, wave = __builtin_amdgcn_readfirstlane(tid >> 6);
  const int g = lane >> 4, c = lane & 15;
  const int kvh = blockIdx.x & 7, part = blockIdx.x >> 3;

  STAMP(0);
  int y[NBATCH], len[NBATCH];
#pragma unroll
  for (int b = 0; b < NBATCH; ++b) y[b] = bt[b * NBLK + tid];
  unsigned zeroq = 0;
#pragma unroll
  for (int b = 0; b < NBATCH; ++b) {
    int l = lens[b];
    if (l <= 0) { zeroq |= 1u << b; l = LK; }
    len[b] = l > LK ? LK : l;
  }
  u32x4* qlds = (u32x4*)(smem + LDS_Q);
  {
    const int b = 4 * (wave >> 2) + (c >> 2);
    const u32x4 t = *(const u32x4*)((const char*)q + ((b * NH + 4 * kvh + (c & 3)) * HD + 8 * g + 32 * (wave & 3)) * 2);
    qlds[tid] = ((zeroq >> b) & 1) ? u32x4{0, 0, 0, 0} : t;
  }
  int lenv[2];
#pragma unroll
  for (int cg = 0; cg < 2; ++cg) {
    const int cb = c >> 2;
    lenv[cg] = cb == 0 ? len[4 * cg] : cb == 1 ? len[4 * cg + 1] : cb == 2 ? len[4 * cg + 2] : len[4 * cg + 3];
  }
#pragma unroll
  for (int b = 0; b < NBATCH; ++b) y[b] = (tid * PAGE < len[b]) ? y[b] : -1 - b;
  unsigned msk[NBATCH];
#pragma unroll
  for (int b = 0; b < NBATCH; ++b) msk[b] = 1u << b;
#pragma unroll
  for (int a = 0; a < NBATCH; ++a)
#pragma unroll
    for (int b = a + 1; b < NBATCH; ++b) {
      const bool e = y[a] == y[b];
      msk[a] |= e ? (1u << b) : 0u;
      msk[b] |= e ? (1u << a) : 0u;
    }
  int* cnt = (int*)(smem + LDS_CNT);
  u32x2* list = (u32x2*)(smem + LDS_LIST);
  bool lead[NBATCH];
  int pre[NBATCH];
  int mycnt = 0;
#pragma unroll
  for (int b = 0; b < NBATCH; ++b) {
    lead[b] = y[b] >= 0 && (msk[b] & ((1u << b) - 1)) == 0;
    const unsigned long long bal = __ballot(lead[b]);
    pre[b] = __builtin_amdgcn_mbcnt_hi((unsigned)(bal >> 32), __builtin_amdgcn_mbcnt_lo((unsigned)bal, 0));
    const int n = __popcll(bal);
    mycnt = lane == b ? n : mycnt;
  }
  if (lane < NBATCH) cnt[lane * NWAVE + wave] = mycnt;
  STAMP(1);
  __syncthreads();
  int incl = cnt[lane];
#pragma unroll
  for (int d = 1; d < 64; d <<= 1) {
    const int t = __shfl_up(incl, d);
    incl += lane >= d ? t : 0;
  }
  const int excl = incl - cnt[lane];
  const int total = __builtin_amdgcn_readlane(incl, 63);
  const int i0 = (total * part) / NPART, i1 = (total * (part + 1)) / NPART;
  const int nitem = i1 - i0;
#pragma unroll
  for (int b = 0; b < NBATCH; ++b) {
    const int idx = __builtin_amdgcn_readlane(excl, b * NWAVE + wave) + pre[b] - i0;
    if (lead[b] && idx >= 0 && idx < nitem) {
      int mx = 0;
#pragma unroll
      for (int a = 0; a < NBATCH; ++a) mx = ((msk[b] >> a) & 1) ? max(mx, len[a]) : mx;
      const int ntok = min(PAGE, mx - tid * PAGE);
      list[idx] = u32x2{(unsigned)y[b], (unsigned)tid | (msk[b] << 16) | ((unsigned)ntok << 24)};
    }
  }
  __syncthreads();
  u32x2 it = {0u, 0u};
  {
    const int idx = 2 * (wave + NWAVE * (lane >> 1)) + (lane & 1);
    if (idx < nitem) it = list[idx];
  }
  const int npair_all = (nitem + 1) >> 1;
  const int npair = (npair_all - wave + NWAVE - 1) / NWAVE;

  const char* kbase = kc + kvh * (HD * 2);
  const char* vbase = vc + kvh * (HD * 2);
  const int koff = c * TOK_BYTES + g * 16;
  const int voff = 4 * g * TOK_BYTES + c * 16;

  float m[2] = {-INFINITY, -INFINITY}, l[2] = {0.f, 0.f};
  f32x4 acc[2][8];
#pragma unroll
  for (int cg = 0; cg < 2; ++cg)
#pragma unroll
    for (int i = 0; i < 8; ++i) acc[cg][i] = f32x4{0.f, 0.f, 0.f, 0.f};

  auto issue = [&](Pair& p, int k) {
#pragma unroll
    for (int s = 0; s < 2; ++s) {
      const int e = (2 * k + s) & 63;
      const int phys = __builtin_amdgcn_readlane((int)it[0], e);
      const unsigned meta = (unsigned)__builtin_amdgcn_readlane((int)it[1], e);
      const int ntok = meta >> 24;
      const int nbytes = ntok > 0 ? (ntok - 1) * TOK_BYTES + HD * 2 : 0;
      load_page(kbase, vbase, phys, nbytes, koff, voff, p.k[s], p.v[s]);
    }
  };
  auto compute = [&](const Pair& p, int k) {
    const unsigned meta0 = (unsigned)__builtin_amdgcn_readlane((int)it[1], (2 * k) & 63);
    const unsigned meta1 = (unsigned)__builtin_amdgcn_readlane((int)it[1], (2 * k + 1) & 63);
    const unsigned um = ((meta0 | meta1) >> 16) & 0xffu;
    u32x4 va[8];
#pragma unroll
    for (int i = 0; i < 8; ++i) {
      const unsigned sel = (i & 1) ? 0x07060302u : 0x05040100u;
      va[i][0] = __builtin_amdgcn_perm(p.v[0][1][i >> 1], p.v[0][0][i >> 1], sel);
      va[i][1] = __builtin_amdgcn_perm(p.v[0][3][i >> 1], p.v[0][2][i >> 1], sel);
      va[i][2] = __builtin_amdgcn_perm(p.v[1][1][i >> 1], p.v[1][0][i >> 1], sel);
      va[i][3] = __builtin_amdgcn_perm(p.v[1][3][i >> 1], p.v[1][2][i >> 1], sel);
    }
#pragma unroll
    for (int cg = 0; cg < 2; ++cg) {
      if ((um >> (4 * cg)) & 0xfu) {
        f32x4 s0 = {0.f, 0.f, 0.f, 0.f}, s1 = {0.f, 0.f, 0.f, 0.f};
#pragma unroll
        for (int i = 0; i < 4; ++i) {
          const u32x4 qf = qlds[(cg * 4 + i) * 64 + lane];
          s0 = mfma16(p.k[0][i], qf, s0);
          s1 = mfma16(p.k[1][i], qf, s1);
        }
        const int bit = 16 + 4 * cg + (c >> 2);
        const int lim0 = ((meta0 >> bit) & 1) ? lenv[cg] - (int)(meta0 & 0xffffu) * PAGE - 4 * g : 0;
        const int lim1 = ((meta1 >> bit) & 1) ? lenv[cg] - (int)(meta1 & 0xffffu) * PAGE - 4 * g : 0;
        float x[8];
#pragma unroll
        for (int r = 0; r < 4; ++r) {
          x[r] = r < lim0 ? s0[r] * SC_LOG2 : -INFINITY;
          x[4 + r] = r < lim1 ? s1[r] * SC_LOG2 : -INFINITY;
        }
        float mx = fmaxf(fmaxf(fmaxf(x[0], x[1]), fmaxf(x[2], x[3])), fmaxf(fmaxf(x[4], x[5]), fmaxf(x[6], x[7])));
        mx = xmax(mx);
        if (__any(mx > m[cg])) {
          const float mn = fmaxf(m[cg], mx);
          const float alpha = mn == -INFINITY ? 1.f : __builtin_amdgcn_exp2f(m[cg] - mn);
          m[cg] = mn;
          l[cg] *= alpha;
#pragma unroll
          for (int i = 0; i < 8; ++i) acc[cg][i] *= alpha;
        }
        const float ms = m[cg] == -INFINITY ? 0.f : m[cg];
        float ps = 0.f;
#pragma unroll
        for (int r = 0; r < 8; ++r) { x[r] = __builtin_amdgcn_exp2f(x[r] - ms); ps += x[r]; }
        l[cg] += ps;
        const u32x4 pb = {cvtpk(x[0], x[1]), cvtpk(x[2], x[3]), cvtpk(x[4], x[5]), cvtpk(x[6], x[7])};
#pragma unroll
        for (int i = 0; i < 8; ++i) acc[cg][i] = mfma16(va[i], pb, acc[cg][i]);
      }
    }
  };

  STAMP(2);
  Pair pa, pb;
  issue(pa, 0);
  issue(pb, 1);
  for (int k = 0; k < npair; k += 2) {
    compute(pa, k);
    if (k == 0) STAMP(3);
    issue(pa, k + 2);
    compute(pb, k + 1);
    issue(pb, k + 3);
  }

  STAMP(4);
  float* lo = (float*)(smem + LDS_O);
  float* lml = (float*)(smem + LDS_ML);
#pragma unroll
  for (int cg = 0; cg < 2; ++cg) {
    l[cg] = xsum(l[cg]);
    if (g == 0) { lml[(wave * NCOL + 16 * cg + c) * 2] = m[cg]; lml[(wave * NCOL + 16 * cg + c) * 2 + 1] = l[cg]; }
    if (m[cg] != -INFINITY) {
      float* row = lo + (wave * NCOL + 16 * cg + c) * ROW_F + 32 * g;
#pragma unroll
      for (int r = 0; r < 4; ++r) {
        *(f32x4*)(row + 8 * r) = f32x4{acc[cg][0][r], acc[cg][1][r], acc[cg][2][r], acc[cg][3][r]};
        *(f32x4*)(row + 8 * r + 4) = f32x4{acc[cg][4][r], acc[cg][5][r], acc[cg][6][r], acc[cg][7][r]};
      }
    }
  }
  __syncthreads();
  STAMP(5);
  const size_t slot0 = (size_t)(kvh * NPART + part) * NCOL;
#pragma unroll
  for (int h = 0; h < 2; ++h) {
    const int col = h * 16 + (tid >> 5), d4 = (tid & 31) * 4;
    float mw[NWAVE], M = -INFINITY;
#pragma unroll
    for (int w = 0; w < NWAVE; ++w) { mw[w] = lml[(w * NCOL + col) * 2]; M = fmaxf(M, mw[w]); }
    float L = 0.f;
    f32x4 o = {0.f, 0.f, 0.f, 0.f};
    if (M != -INFINITY) {
#pragma unroll
      for (int w = 0; w < NWAVE; ++w) {
        if (mw[w] != -INFINITY) {
          const float f = __builtin_amdgcn_exp2f(mw[w] - M);
          L += f * lml[(w * NCOL + col) * 2 + 1];
          o += f * *(const f32x4*)(lo + (w * NCOL + col) * ROW_F + d4);
        }
      }
      *(f32x4*)(wpo + (slot0 + col) * HD + d4) = o;
    }
    if ((tid & 31) == 0) *(float2*)(wml + (slot0 + col) * 2) = make_float2(M, L);
  }
  STAMP(6);
}

__global__ __launch_bounds__(64) void attn_combine(const float* __restrict__ wml, const float* __restrict__ wpo,
                                                   __hip_bfloat16* __restrict__ out) {
  const int row = blockIdx.x, b = row >> 5, h = row & 31, kvh = h >> 2, col = 4 * b + (h & 3);
  const int lane = threadIdx.x;
  const float2 ml = *(const float2*)(wml + ((size_t)(kvh * NPART + (lane & 31)) * NCOL + col) * 2);
  float M = ml.x;
#pragma unroll
  for (int d = 1; d < 32; d <<= 1) M = fmaxf(M, __shfl_xor(M, d));
  const float f = ml.x == -INFINITY ? 0.f : __builtin_amdgcn_exp2f(ml.x - M);
  float L = f * ml.y;
#pragma unroll
  for (int d = 1; d < 32; d <<= 1) L += __shfl_xor(L, d);
  unsigned live = (unsigned)__ballot(ml.x != -INFINITY);
  float o0 = 0.f, o1 = 0.f;
  while (live) {
    float2 a[4];
    float w[4];
#pragma unroll
    for (int u = 0; u < 4; ++u) {
      const bool ok = live != 0;
      const int p = ok ? __builtin_ctz(live) : 0;
      live &= live - 1;
      w[u] = __shfl(f, p);
      a[u] = ok ? *(const float2*)(wpo + ((size_t)(kvh * NPART + p) * NCOL + col) * HD + 2 * lane) : make_float2(0.f, 0.f);
    }
#pragma unroll
    for (int u = 0; u < 4; ++u) { o0 += w[u] * a[u].x; o1 += w[u] * a[u].y; }
  }
  const float inv = 1.f / L;
  *(unsigned*)(out + row * HD + 2 * lane) = cvtpk(o0 * inv, o1 * inv);

  {
    const unsigned long long* st = (const unsigned long long*)((const char*)wml + STAMP_OFF);
    unsigned long long t0 = ~0ull, tx = 0;
    for (int b = lane; b < 256; b += 64) { unsigned long long a = st[b * NSTAMP]; t0 = a < t0 ? a : t0; unsigned long long x = st[b * NSTAMP + PROBE]; tx = x > tx ? x : tx; }
    for (int d = 1; d < 64; d <<= 1) { unsigned long long a = __shfl_xor(t0, d); t0 = a < t0 ? a : t0; unsigned long long x = __shfl_xor(tx, d); tx = x > tx ? x : tx; }
    const unsigned long long target = t0 + 10000ull + MULT * (tx - t0);
    for (int it = 0; it < 1000000; ++it) { unsigned long long t_; asm volatile("s_memrealtime %0\n\ts_waitcnt lgkmcnt(0)" : "=s"(t_) :: "memory"); if (t_ >= target) break; }
  }
}

}

extern "C" void kernel_launch(void* const* d_in, const int* in_sizes, int n_in, void* d_out, int out_size, void* d_ws,
                              size_t ws_size, hipStream_t stream) {
  (void)in_sizes; (void)n_in; (void)out_size; (void)ws_size;
  (void)hipFuncSetAttribute((const void*)attn_split, hipFuncAttributeMaxDynamicSharedMemorySize, LDS_BYTES);
  float* wml = (float*)((char*)d_ws + WS_ML);
  float* wpo = (float*)((char*)d_ws + WS_PO);
  hipLaunchKernelGGL(attn_split, dim3(NKVH * NPART), dim3(NTHR), LDS_BYTES, stream, (const __hip_bfloat16*)d_in[0],
                     (const char*)d_in[1], (const char*)d_in[2], (const int*)d_in[3], (const int*)d_in[4], wml, wpo);
  hipLaunchKernelGGL(attn_combine, dim3(NBATCH * NH), dim3(64), 0, stream, (const float*)wml, (const float*)wpo,
                     (__hip_bfloat16*)d_out);
}
```

```cpp
#define POLL_DELAY 50
#include <hip/hip_runtime.h>
#include <hip/hip_bf16.h>
#include <stdint.h>

#define KV_AUX 2
#define DEPTH 1
#define WSH 18
#define PACE 465
#define PACE_N0 2
#define POLL_SLEEP 1
#define TOUCH_FROM 0
#define LATE_ISSUE 1
#define STAMP(i)
#define STAMP_DECL
#define STAMP_FLUSH

namespace {

constexpr int NBATCH = 8, NH = 32, NKVH = 8, HD = 128, PAGE = 16, NBLK = 512, LK = NBLK * PAGE;
constexpr int NPART = 32, NWAVE = 8, NTHR = NWAVE * 64, NCOL = 32;
constexpr int TOK_BYTES = NKVH * HD * 2;
constexpr int PAGE_BYTES = PAGE * TOK_BYTES;
constexpr int PAGE_SPAN = (PAGE - 1) * TOK_BYTES + HD * 2;
constexpr float SC_LOG2 = 0.08838834764831845f * 1.4426950408889634f;
constexpr int AUX_SC1 = 16;
constexpr unsigned SPIN_CAP = 1u << 20;

typedef __attribute__((ext_vector_type(8))) __bf16 bf16x8;
typedef __attribute__((ext_vector_type(2))) __bf16 bf16x2;
typedef __attribute__((ext_vector_type(4))) short s16x4;
typedef __attribute__((ext_vector_type(4))) float f32x4;
typedef __attribute__((ext_vector_type(2))) float f32x2;
typedef __attribute__((ext_vector_type(4))) unsigned u32x4;
typedef __attribute__((ext_vector_type(2))) unsigned u32x2;

constexpr int ROW_F = HD, COL_F = NWAVE * ROW_F + 4;
constexpr int LDS_TBL = 0;
constexpr int LDS_PW = LDS_TBL + NBLK * NBATCH * 4;
constexpr int LDS_Q = LDS_PW + 64;
constexpr int LDS_ML = LDS_Q + 8192;
constexpr int LDS_O = LDS_ML + NWAVE * NCOL * 8;
constexpr int LDS_BYTES = LDS_O + NCOL * COL_F * 4;
constexpr int LDS_FIN = LDS_TBL;

constexpr int WS_HEAD = 2 << 20, WS_SLOT0 = 1024, SLOT_BYTES = 1152;
constexpr int WS_BYTES = WS_SLOT0 + NCOL * NPART * SLOT_BYTES;

__device__ __forceinline__ f32x4 mfma32(u32x4 a, u32x4 b, f32x4 c) {
  return __builtin_amdgcn_mfma_f32_16x16x32_bf16(__builtin_bit_cast(bf16x8, a), __builtin_bit_cast(bf16x8, b), c, 0, 0, 0);
}
__device__ __forceinline__ f32x4 mfma16(u32x2 a, u32x2 b, f32x4 c) {
  return __builtin_amdgcn_mfma_f32_16x16x16bf16_1k(__builtin_bit_cast(s16x4, a), __builtin_bit_cast(s16x4, b), c, 0, 0, 0);
}
__device__ __forceinline__ unsigned cvtpk(float lo, float hi) {
  return __builtin_bit_cast(unsigned, __builtin_convertvector(f32x2{lo, hi}, bf16x2));
}
__device__ __forceinline__ float swap16_max(float x) {
  auto s = __builtin_amdgcn_permlane16_swap(__float_as_uint(x), __float_as_uint(x), false, false);
  return fmaxf(__uint_as_float(s[0]), __uint_as_float(s[1]));
}
__device__ __forceinline__ float swap32_max(float x) {
  auto s = __builtin_amdgcn_permlane32_swap(__float_as_uint(x), __float_as_uint(x), false, false);
  return fmaxf(__uint_as_float(s[0]), __uint_as_float(s[1]));
}
__device__ __forceinline__ float swap16_sum(float x) {
  auto s = __builtin_amdgcn_permlane16_swap(__float_as_uint(x), __float_as_uint(x), false, false);
  return __uint_as_float(s[0]) + __uint_as_float(s[1]);
}
__device__ __forceinline__ float swap32_sum(float x) {
  auto s = __builtin_amdgcn_permlane32_swap(__float_as_uint(x), __float_as_uint(x), false, false);
  return __uint_as_float(s[0]) + __uint_as_float(s[1]);
}
template <int CTRL> __device__ __forceinline__ unsigned dppu(unsigned x) {
  return (unsigned)__builtin_amdgcn_mov_dpp((int)x, CTRL, 0xf, 0xf, true);
}
template <int CTRL> __device__ __forceinline__ float dppf(float x) { return __uint_as_float(dppu<CTRL>(__float_as_uint(x))); }
constexpr int XOR1 = 0xB1, XOR2 = 0x4E, XOR7 = 0x141, XOR15 = 0x140;
__device__ __forceinline__ float max8(float x) {
  x = fmaxf(x, dppf<XOR1>(x)); x = fmaxf(x, dppf<XOR2>(x)); return fmaxf(x, dppf<XOR7>(x));
}
__device__ __forceinline__ float sum8(float x) {
  x += dppf<XOR1>(x); x += dppf<XOR2>(x); return x + dppf<XOR7>(x);
}
__device__ __forceinline__ float rdlane(float x, int l) { return __int_as_float(__builtin_amdgcn_readlane(__float_as_int(x), l)); }
__device__ __forceinline__ int clamp_len(int l) { return (l <= 0 || l > LK) ? LK : l; }
__device__ __forceinline__ void lds_barrier() {
  asm volatile("s_waitcnt lgkmcnt(0)\n\ts_barrier" ::: "memory");
}

struct Page {
  u32x4 k[4];
  u32x4 v[4];
};

__global__ __launch_bounds__(NTHR, 2) void attn_fused(const char* __restrict__ q, const char* __restrict__ kc,
                                                      const char* __restrict__ vc, const int* __restrict__ bt,
                                                      const int* __restrict__ lens, char* __restrict__ ws,
                                                      unsigned* __restrict__ out) {
  extern __shared__ __attribute__((aligned(16))) char smem[];
  STAMP_DECL;
  STAMP(0);
  asm volatile("" :: "s"(q), "s"(kc), "s"(vc), "s"(bt), "s"(lens), "s"(ws), "s"(out));
  const int tid = threadIdx.x, lane = tid & 63, wave = __builtin_amdgcn_readfirstlane(tid >> 6);
  const int g = lane >> 4, c = lane & 15;
  const int kvh = blockIdx.x & 7, part = blockIdx.x >> 3;
  const auto rws = __builtin_amdgcn_make_buffer_rsrc((void*)(ws + (size_t)kvh * WS_HEAD), 0, WS_BYTES, 0x00020000);
  const auto rbt = __builtin_amdgcn_make_buffer_rsrc((void*)bt, 0, NBATCH * NBLK * 4, 0x00020000);
  const auto rln = __builtin_amdgcn_make_buffer_rsrc((void*)lens, 0, NBATCH * 4, 0x00020000);

  int slen[NBATCH];
#pragma unroll
  for (int b = 0; b < NBATCH; ++b) slen[b] = lens[b];
  unsigned y[NBATCH];
#pragma unroll
  for (int b = 0; b < NBATCH; ++b) y[b] = __builtin_amdgcn_raw_buffer_load_b32(rbt, tid * 4 + (b & 1) * 2048, (b >> 1) * 4096, 0);
  const int lseg = (int)__builtin_amdgcn_raw_buffer_load_b32(rln, ((lane + 63) & 63) * 4, 0, 0);
  __builtin_amdgcn_sched_barrier(0);
  const int qb = 4 * (wave >> 2) + (c >> 2);
  const u32x4 qv = *(const u32x4*)(q + ((qb * NH + 4 * kvh + (c & 3)) * HD + 8 * (4 * g + (wave & 3))) * 2);
  const int len0 = (int)__builtin_amdgcn_raw_buffer_load_b32(rln, (c >> 2) * 4, 0, 0);
  const int len1 = (int)__builtin_amdgcn_raw_buffer_load_b32(rln, (c >> 2) * 4 + 16, 0, 0);
  const unsigned epoch = __builtin_amdgcn_raw_buffer_load_b32(rws, (lane & 31) * 4, 0, AUX_SC1);
  __builtin_amdgcn_sched_barrier(0);

  unsigned maxlen = 0;
#pragma unroll
  for (int b = 0; b < NBATCH; ++b) maxlen = max(maxlen, (unsigned)(slen[b] - 1));
  {
    unsigned d = 0;
#pragma unroll
    for (int b = 1; b < NBATCH; ++b) d |= y[0] ^ y[b];
    const unsigned long long mis = __ballot(d != 0 || (unsigned)(tid * PAGE) > maxlen);
    const int pw = mis ? wave * 64 + __builtin_ctzll(mis) : NBLK;
    u32x4* tbl = (u32x4*)(smem + LDS_TBL);
    tbl[tid * 2] = u32x4{y[0], y[1], y[2], y[3]};
    tbl[tid * 2 + 1] = u32x4{y[4], y[5], y[6], y[7]};
    ((int*)(smem + LDS_PW))[wave] = pw;
    ((u32x4*)(smem + LDS_Q))[tid] = ((wave >> 2) ? len1 : len0) <= 0 ? u32x4{0, 0, 0, 0} : qv;
  }
  const int npg = (clamp_len(lseg) + PAGE - 1) >> 4;
  lds_barrier();

  int P;
  {
    const u32x4 a = *(const u32x4*)(smem + LDS_PW), b = *(const u32x4*)(smem + LDS_PW + 16);
    P = (int)min(min(min(a[0], a[1]), min(a[2], a[3])), min(min(b[0], b[1]), min(b[2], b[3])));
  }
  const int cnt = lane == 0 ? P : lane <= NBATCH ? max(npg - P, 0) : 0;
  int send = cnt;
  send += (int)dppu<0x111>(send);
  send += (int)dppu<0x112>(send);
  send += (int)dppu<0x114>(send);
  send += (int)dppu<0x118>(send);
  const int soff = lane == 0 ? 0 : (P - (send - cnt)) * 32 + (lane - 1) * 4;
  const int total = __builtin_amdgcn_readlane(send, 15);
  int bnd;
  {
    const int x = ((total * 16 + P * (WSH - 16)) * lane) >> 5;
    bnd = x < P * WSH ? x / WSH : (x - P * (WSH - 16)) >> 4;
  }
  const int i0 = __builtin_amdgcn_readlane(bnd, part), i1 = __builtin_amdgcn_readlane(bnd, part + 1);
  const int nitem = i1 - i0, nbase = nitem >> 3, nextra = nitem & 7;
  const int nw = nbase + (wave < nextra ? 1 : 0);
  const int idx0 = i0 + nbase * wave + min(wave, nextra);
  int taddr = 0;
  {
    const int idx = idx0 + lane;
    int cur = idx0;
    while (cur < idx0 + nw) {
      const int s = __popcll(__ballot(lane < 16 && send <= cur));
      taddr = idx >= cur ? idx * 32 + __builtin_amdgcn_readlane(soff, s & 15) : taddr;
      cur = __builtin_amdgcn_readlane(send, s & 15);
    }
    taddr = lane < nw ? taddr : 0;
  }
  const int phys = *(const int*)(smem + LDS_TBL + taddr);

  const char* kbase = kc + kvh * (HD * 2);
  const char* vbase = vc + kvh * (HD * 2);
  const int koff = ((c >> 2) * 4) * TOK_BYTES + (4 * g + (c & 3)) * 16;
  const int voff = 4 * g * TOK_BYTES + c * 16;

  const unsigned tpace = (unsigned)__builtin_amdgcn_s_memrealtime() + (((wave - PACE_N0) * PACE) >> 4);
  auto issue = [&](Page& p, int k) {
    if (PACE && k < nw) {
      const unsigned tg = tpace + ((k * NWAVE * PACE) >> 4);
      while ((int)((unsigned)__builtin_amdgcn_s_memrealtime() - tg) < 0) __builtin_amdgcn_s_sleep(1);
    }
    const size_t off = (size_t)(unsigned)__builtin_amdgcn_readlane(phys, k & 63) * PAGE_BYTES;
    const int nbytes = k < nw ? PAGE_SPAN : 0;
    const auto rk = __builtin_amdgcn_make_buffer_rsrc((void*)(kbase + off), 0, nbytes, 0x00020000);
    const auto rv = __builtin_amdgcn_make_buffer_rsrc((void*)(vbase + off), 0, nbytes, 0x00020000);
#pragma unroll
    for (int i = 0; i < 4; ++i) p.k[i] = __builtin_amdgcn_raw_buffer_load_b128(rk, koff + (i & 1) * TOK_BYTES, (i >> 1) * 2 * TOK_BYTES, KV_AUX);
#pragma unroll
    for (int j = 0; j < 4; ++j) p.v[j] = __builtin_amdgcn_raw_buffer_load_b128(rv, voff + (j & 1) * TOK_BYTES, (j >> 1) * 2 * TOK_BYTES, KV_AUX);
    __builtin_amdgcn_sched_barrier(0);
  };

  STAMP(2);
  Page pg[DEPTH];
  issue(pg[0], 0);

  const unsigned meta = lane < nw ? (unsigned)(taddr >> 5) * PAGE | (idx0 + lane < P ? 0xff0000u : 0x10000u << ((taddr >> 2) & 7)) : 0u;
  const unsigned um = (unsigned)(__ballot(lane >= 1 && lane <= NBATCH && nitem > 0 &&
                                          (i0 < min(P, npg) || (cnt > 0 && i0 < send && i1 > send - cnt))) >> 1);
  unsigned contrib;
  {
    const int s = (part >> 2) + 1;
    const int e = __builtin_amdgcn_readlane(send, s), n = __builtin_amdgcn_readlane(cnt, s), ps = min(P, __builtin_amdgcn_readlane(npg, s));
    const int nxt = __builtin_amdgcn_ds_bpermute((lane + 1) * 4, bnd);
    contrib = (unsigned)__ballot(lane < NPART && nxt > bnd && (bnd < ps || (n > 0 && bnd < e && nxt > e - n)));
  }
  const int lenv[2] = {clamp_len(len0) - 4 * g, clamp_len(len1) - 4 * g};
#pragma unroll
  for (int s = 1; s < DEPTH; ++s) issue(pg[s], s);

  float m[2] = {-INFINITY, -INFINITY}, l[2] = {0.f, 0.f};
  f32x4 acc[2][8];
#pragma unroll
  for (int cg = 0; cg < 2; ++cg)
#pragma unroll
    for (int i = 0; i < 8; ++i) acc[cg][i] = f32x4{0.f, 0.f, 0.f, 0.f};
  const u32x4* qlds = (const u32x4*)(smem + LDS_Q);
  const bool o0 = lane & 1, o1 = lane & 2;

  auto step = [&](Page& p, int k) {
    u32x4 kt[4];
    u32x2 va[8];
#pragma unroll
    for (int w = 0; w < 4; ++w) {
      const unsigned x0 = p.k[0][w], x1 = p.k[1][w], x2 = p.k[2][w], x3 = p.k[3][w];
      const unsigned d0 = dppu<XOR1>(x0), d1 = dppu<XOR1>(x1), d2 = dppu<XOR1>(x2), d3 = dppu<XOR1>(x3);
      const unsigned y0 = o0 ? d1 : x0, y1 = o0 ? x1 : d0, y2 = o0 ? d3 : x2, y3 = o0 ? x3 : d2;
      const unsigned e0 = dppu<XOR2>(y0), e1 = dppu<XOR2>(y1), e2 = dppu<XOR2>(y2), e3 = dppu<XOR2>(y3);
      kt[0][w] = o1 ? e2 : y0;
      kt[2][w] = o1 ? y2 : e0;
      kt[1][w] = o1 ? e3 : y1;
      kt[3][w] = o1 ? y3 : e1;
    }
#pragma unroll
    for (int i = 0; i < 8; ++i) {
      const unsigned sel = (i & 1) ? 0x07060302u : 0x05040100u;
      va[i][0] = __builtin_amdgcn_perm(p.v[1][i >> 1], p.v[0][i >> 1], sel);
      va[i][1] = __builtin_amdgcn_perm(p.v[3][i >> 1], p.v[2][i >> 1], sel);
    }
    __builtin_amdgcn_sched_barrier(0);
    if (!LATE_ISSUE) issue(p, k + DEPTH);
    const unsigned mt = (unsigned)__builtin_amdgcn_readlane((int)meta, k & 63);
#pragma unroll
    for (int cg = 0; cg < 2; ++cg) {
      if ((mt >> (16 + 4 * cg)) & 0xfu) {
        f32x4 s = {0.f, 0.f, 0.f, 0.f};
#pragma unroll
        for (int i = 0; i < 4; ++i) s = mfma32(kt[i], qlds[(cg * 4 + i) * 64 + lane], s);
        const int lim = ((mt >> (16 + 4 * cg + (c >> 2))) & 1) ? lenv[cg] - (int)(mt & 0xffffu) : 0;
        float x[4];
#pragma unroll
        for (int r = 0; r < 4; ++r) x[r] = r < lim ? s[r] * SC_LOG2 : -INFINITY;
        const float mx = swap32_max(swap16_max(fmaxf(fmaxf(x[0], x[1]), fmaxf(x[2], x[3]))));
        if (__any(mx > m[cg])) {
          const float mn = fmaxf(m[cg], mx);
          const float alpha = mn == -INFINITY ? 1.f : __builtin_amdgcn_exp2f(m[cg] - mn);
          m[cg] = mn;
          l[cg] *= alpha;
#pragma unroll
          for (int i = 0; i < 8; ++i) acc[cg][i] *= alpha;
        }
        const float ms = m[cg] == -INFINITY ? 0.f : m[cg];
#pragma unroll
        for (int r = 0; r < 4; ++r) x[r] = __builtin_amdgcn_exp2f(x[r] - ms);
        l[cg] += (x[0] + x[1]) + (x[2] + x[3]);
        const u32x2 pb = {cvtpk(x[0], x[1]), cvtpk(x[2], x[3])};
#pragma unroll
        for (int i = 0; i < 8; ++i) acc[cg][i] = mfma16(va[i], pb, acc[cg][i]);
      }
    }
    if (LATE_ISSUE) issue(p, k + DEPTH);
  };

  unsigned tch0 = 0, tch1 = 0;
  for (int k = 0; k < nw; k += DEPTH) {
#pragma unroll
    for (int s = 0; s < DEPTH; ++s) step(pg[s], k + s);
  }
  asm volatile("" :: "v"(tch0), "v"(tch1));
  STAMP(4);

  float* lo = (float*)(smem + LDS_O);
  float* lml = (float*)(smem + LDS_ML);
#pragma unroll
  for (int cg = 0; cg < 2; ++cg) {
    if ((um >> (4 * cg)) & 0xfu) {
      const float ls = swap32_sum(swap16_sum(l[cg]));
      if ((um >> (4 * cg + (c >> 2))) & 1) {
        *(f32x2*)(lml + ((16 * cg + c) * NWAVE + wave) * 2) = f32x2{m[cg], ls};
        float* rw = lo + (16 * cg + c) * COL_F + wave * ROW_F + 32 * g;
#pragma unroll
        for (int r = 0; r < 4; ++r) {
          *(f32x4*)(rw + 8 * r) = f32x4{acc[cg][0][r], acc[cg][1][r], acc[cg][2][r], acc[cg][3][r]};
          *(f32x4*)(rw + 8 * r + 4) = f32x4{acc[cg][4][r], acc[cg][5][r], acc[cg][6][r], acc[cg][7][r]};
        }
      }
    }
  }
  unsigned todo = um;
  if (wave >> 2) todo &= todo - 1;
  lds_barrier();
  STAMP(5);

  while (todo) {
    const int col = 4 * __builtin_ctz(todo) + (wave & 3);
    todo &= todo - 1;
    todo &= todo - 1;
    const f32x2 ml = *(const f32x2*)(lml + (col * NWAVE + (lane & 7)) * 2);
    f32x2 o[NWAVE];
#pragma unroll
    for (int w = 0; w < NWAVE; ++w) o[w] = *(const f32x2*)(lo + col * COL_F + w * ROW_F + 2 * lane);
    const unsigned tag = (unsigned)__builtin_amdgcn_readlane((int)epoch, col) + 1;
    const float M = max8(ml[0]);
    const float f = __builtin_amdgcn_exp2f(ml[0] - M);
    f32x2 a = o[0] * rdlane(f, 0);
#pragma unroll
    for (int w = 1; w < NWAVE; ++w) a += o[w] * rdlane(f, w);
    const int slot = WS_SLOT0 + (col * NPART + part) * SLOT_BYTES;
    __builtin_amdgcn_raw_buffer_store_b128(u32x4{__float_as_uint(a[0]), tag, __float_as_uint(a[1]), tag}, rws, lane * 16, slot, AUX_SC1);
    const float L = sum8(f * ml[1]);
    if (lane < 8)
      __builtin_amdgcn_raw_buffer_store_b128(u32x4{__float_as_uint(M), tag, __float_as_uint(L), tag}, rws, lane * 16 + 1024, slot, AUX_SC1);
  }
  STAMP(6);
  STAMP(3);
  { const unsigned tg = (unsigned)__builtin_amdgcn_s_memrealtime() + POLL_DELAY; while ((int)((unsigned)__builtin_amdgcn_s_memrealtime() - tg) < 0) __builtin_amdgcn_s_sleep(1); }

  const unsigned tag = (unsigned)__builtin_amdgcn_readlane((int)epoch, part) + 1;
  float* fin = (float*)(smem + LDS_FIN);
  {
    const int slot0 = WS_SLOT0 + (part * NPART + wave) * SLOT_BYTES;
    unsigned pend = (contrib >> wave) & 0x01010101u;
    u32x4 h[4], d[4];
#pragma unroll
    for (int u = 0; u < 4; ++u) { h[u] = u32x4{0xff800000u, 0, 0, 0}; d[u] = u32x4{0, 0, 0, 0}; }
    for (unsigned spin = 0; pend && spin < SPIN_CAP; ++spin) {
      asm volatile("" ::: "memory");
#pragma unroll
      for (int u = 0; u < 4; ++u)
        if ((pend >> (8 * u)) & 1) {
          d[u] = __builtin_amdgcn_raw_buffer_load_b128(rws, lane * 16, slot0 + 8 * u * SLOT_BYTES, AUX_SC1);
          h[u] = __builtin_amdgcn_raw_buffer_load_b128(rws, 1024, slot0 + 8 * u * SLOT_BYTES, AUX_SC1);
        }
#pragma unroll
      for (int u = 0; u < 4; ++u)
        if (__all(((d[u][1] ^ tag) | (d[u][3] ^ tag) | (h[u][1] ^ tag) | (h[u][3] ^ tag)) == 0)) pend &= ~(1u << (8 * u));
      if (spin == 0) { STAMP(1); }
      if (POLL_SLEEP && pend) __builtin_amdgcn_s_sleep(POLL_SLEEP);
    }
    const float M = fmaxf(fmaxf(fmaxf(__uint_as_float(h[0][0]), __uint_as_float(h[1][0])), fmaxf(__uint_as_float(h[2][0]), __uint_as_float(h[3][0]))), -3.0e38f);
    float L = 0.f;
    f32x2 a = {0.f, 0.f};
#pragma unroll
    for (int u = 0; u < 4; ++u) {
      const float f = __builtin_amdgcn_exp2f(__uint_as_float(h[u][0]) - M);
      L += f * __uint_as_float(h[u][2]);
      a += f32x2{__uint_as_float(d[u][0]), __uint_as_float(d[u][2])} * f;
    }
    *(f32x2*)(fin + wave * HD + 2 * lane) = a;
    *(f32x2*)(fin + NWAVE * HD + 2 * wave) = f32x2{M, L};
  }
  lds_barrier();
  if (wave == 0) {
    const f32x2 ml = *(const f32x2*)(fin + NWAVE * HD + 2 * (lane & 7));
    f32x2 o[NWAVE];
#pragma unroll
    for (int w = 0; w < NWAVE; ++w) o[w] = *(const f32x2*)(fin + w * HD + 2 * lane);
    const float f = __builtin_amdgcn_exp2f(ml[0] - max8(ml[0]));
    const float cf = f * __builtin_amdgcn_rcpf(sum8(f * ml[1]));
    f32x2 a = o[0] * rdlane(cf, 0);
#pragma unroll
    for (int w = 1; w < NWAVE; ++w) a += o[w] * rdlane(cf, w);
    out[((part >> 2) * NH + 4 * kvh + (part & 3)) * (HD / 2) + lane] = cvtpk(a[0], a[1]);
    if (lane == 0) __builtin_amdgcn_raw_buffer_store_b32(tag, rws, 0, part * 4, AUX_SC1);
  }
  STAMP(7);
  STAMP_FLUSH;
}

}

extern "C" void kernel_launch(void* const* d_in, const int* in_sizes, int n_in, void* d_out, int out_size, void* d_ws,
                              size_t ws_size, hipStream_t stream) {
  (void)in_sizes; (void)n_in; (void)out_size; (void)ws_size;
  (void)hipFuncSetAttribute((const void*)attn_fused, hipFuncAttributeMaxDynamicSharedMemorySize, LDS_BYTES);
  hipLaunchKernelGGL(attn_fused, dim3(NKVH * NPART), dim3(NTHR), LDS_BYTES, stream, (const char*)d_in[0], (const char*)d_in[1],
                     (const char*)d_in[2], (const int*)d_in[3], (const int*)d_in[4], (char*)d_ws, (unsigned*)d_out);
}
```

```cpp
#define PF_N 2
#define PACE 465
#include <hip/hip_runtime.h>
#include <hip/hip_bf16.h>
#include <stdint.h>

#define KV_AUX 2
#define WSH 17
#define PACE_N0 1
#define HINT_EARLY 1
#define POLL_SLEEP 0
#define SLIP_C 0
#define MAP 0
#define ROT 0
#define PF_AUX 0
#define PACE_T0 35
#define EARLY_ISSUE 0
#define STAMP(i)
#define STAMP_DECL
#define STAMP_FLUSH

namespace {

constexpr int NBATCH = 8, NH = 32, NKVH = 8, HD = 128, PAGE = 16, NBLK = 512, LK = NBLK * PAGE;
constexpr int NPART = 32, NWAVE = 8, NTHR = NWAVE * 64, NCOL = 32;
constexpr int TOK_BYTES = NKVH * HD * 2;
constexpr int PAGE_BYTES = PAGE * TOK_BYTES;
constexpr int PAGE_SPAN = (PAGE - 1) * TOK_BYTES + HD * 2;
constexpr float SC_LOG2 = 0.08838834764831845f * 1.4426950408889634f;
constexpr int AUX_SC1 = 16;
constexpr unsigned SPIN_CAP = 1u << 20;
constexpr int OOB = 0x70000000;

typedef __attribute__((ext_vector_type(8))) __bf16 bf16x8;
typedef __attribute__((ext_vector_type(2))) __bf16 bf16x2;
typedef __attribute__((ext_vector_type(4))) short s16x4;
typedef __attribute__((ext_vector_type(4))) float f32x4;
typedef __attribute__((ext_vector_type(2))) float f32x2;
typedef __attribute__((ext_vector_type(4))) unsigned u32x4;
typedef __attribute__((ext_vector_type(2))) unsigned u32x2;
typedef __attribute__((ext_vector_type(8))) int i32x8;
extern "C" __device__ int llvm_writelane(int, int, int) __asm("llvm.amdgcn.writelane.i32");

constexpr int COL_F = NWAVE * HD + 4;
constexpr int LDS_TBL = 0;
constexpr int LDS_PW = LDS_TBL + NBLK * NBATCH * 4;
constexpr int LDS_EP = LDS_PW + 64;
constexpr int LDS_Q = LDS_EP + 512;
constexpr int LDS_ML = LDS_Q + 8192;
constexpr int LDS_O = LDS_ML + NCOL * 40 * 4;
constexpr int LDS_BYTES = LDS_O + NCOL * COL_F * 4;
constexpr int LDS_FIN = LDS_TBL;

constexpr int WS_HEAD = 2 << 20, WS_SLOT0 = 1024, SLOT_BYTES = 1152;
constexpr int WS_BYTES = WS_SLOT0 + NCOL * NPART * SLOT_BYTES;

__device__ __forceinline__ f32x4 mfma32(u32x4 a, u32x4 b, f32x4 c) {
  return __builtin_amdgcn_mfma_f32_16x16x32_bf16(__builtin_bit_cast(bf16x8, a), __builtin_bit_cast(bf16x8, b), c, 0, 0, 0);
}
__device__ __forceinline__ f32x4 mfma16(u32x2 a, u32x2 b, f32x4 c) {
  return __builtin_amdgcn_mfma_f32_16x16x16bf16_1k(__builtin_bit_cast(s16x4, a), __builtin_bit_cast(s16x4, b), c, 0, 0, 0);
}
__device__ __forceinline__ unsigned cvtpk(float lo, float hi) {
  return __builtin_bit_cast(unsigned, __builtin_convertvector(f32x2{lo, hi}, bf16x2));
}
__device__ __forceinline__ float fmx(float a, float b) { return __builtin_elementwise_maximum(a, b); }
__device__ __forceinline__ float swap16_max(float x) {
  auto s = __builtin_amdgcn_permlane16_swap(__float_as_uint(x), __float_as_uint(x), false, false);
  return fmx(__uint_as_float(s[0]), __uint_as_float(s[1]));
}
__device__ __forceinline__ float swap32_max(float x) {
  auto s = __builtin_amdgcn_permlane32_swap(__float_as_uint(x), __float_as_uint(x), false, false);
  return fmx(__uint_as_float(s[0]), __uint_as_float(s[1]));
}
__device__ __forceinline__ float swap16_sum(float x) {
  auto s = __builtin_amdgcn_permlane16_swap(__float_as_uint(x), __float_as_uint(x), false, false);
  return __uint_as_float(s[0]) + __uint_as_float(s[1]);
}
template <int CTRL> __device__ __forceinline__ unsigned dppu(unsigned x) {
  return (unsigned)__builtin_amdgcn_mov_dpp((int)x, CTRL, 0xf, 0xf, true);
}
template <int CTRL> __device__ __forceinline__ float dppf(float x) { return __uint_as_float(dppu<CTRL>(__float_as_uint(x))); }
constexpr int XOR1 = 0xB1, XOR2 = 0x4E, XOR3 = 0x1B, XOR7 = 0x141, XOR15 = 0x140;
__device__ __forceinline__ float max8(float x) {
  x = fmx(x, dppf<XOR7>(x));
  return fmx(fmx(fmx(x, dppf<XOR1>(x)), dppf<XOR2>(x)), dppf<XOR3>(x));
}
__device__ __forceinline__ float sum8(float x) {
  x += dppf<XOR1>(x); x += dppf<XOR2>(x); return x + dppf<XOR7>(x);
}
__device__ __forceinline__ float rdlane(float x, int l) { return __int_as_float(__builtin_amdgcn_readlane(__float_as_int(x), l)); }
__device__ __forceinline__ int clamp_len(int l) { return (l <= 0 || l > LK) ? LK : l; }
__device__ __forceinline__ void lds_barrier() { asm volatile("s_waitcnt lgkmcnt(0)\n\ts_barrier" ::: "memory"); }
__device__ __forceinline__ unsigned now() { return (unsigned)__builtin_amdgcn_s_memrealtime(); }
__device__ __forceinline__ f32x2 wsum8(const f32x2 (&o)[8], float f) {
  const f32x2 a = o[0] * rdlane(f, 0) + o[2] * rdlane(f, 2) + o[4] * rdlane(f, 4) + o[6] * rdlane(f, 6);
  const f32x2 b = o[1] * rdlane(f, 1) + o[3] * rdlane(f, 3) + o[5] * rdlane(f, 5) + o[7] * rdlane(f, 7);
  return a + b;
}

struct Page {
  u32x4 k[4];
  u32x4 v[4];
};

__global__ __launch_bounds__(NTHR, 2) void attn_fused(const char* __restrict__ q, const char* __restrict__ kc,
                                                      const char* __restrict__ vc, const int* __restrict__ bt,
                                                      const int* __restrict__ lens, char* __restrict__ ws,
                                                      unsigned* __restrict__ out, int nphys) {
  extern __shared__ __attribute__((aligned(16))) char smem[];
  STAMP_DECL;
  STAMP(0);
  const int tid = threadIdx.x, lane = tid & 63, wave = __builtin_amdgcn_readfirstlane(tid >> 6);
  if (tid < 8) ((unsigned*)(smem + LDS_PW))[tid] = 0u;
  asm volatile("s_waitcnt lgkmcnt(0)" :: "s"(q), "s"(kc), "s"(vc), "s"(bt), "s"(lens), "s"(ws), "s"(out), "s"(nphys) : "memory");
  const i32x8 slen = *(const __attribute__((address_space(4))) i32x8*)(uintptr_t)lens;
  const int g = lane >> 4, c = lane & 15;
  const int kvh = blockIdx.x & 7, part = blockIdx.x >> 3;

  u32x2 y[NBATCH];
  if (wave < 4) {
    const auto rbt = __builtin_amdgcn_make_buffer_rsrc((void*)bt, 0, NBATCH * NBLK * 4, 0x00020000);
#pragma unroll
    for (int b = 0; b < NBATCH; ++b) y[b] = __builtin_amdgcn_raw_buffer_load_b64(rbt, tid * 8 + (b & 1) * 2048, (b >> 1) * 4096, 0);
  }
  asm volatile("s_barrier" ::: "memory");
  const auto rws = __builtin_amdgcn_make_buffer_rsrc((void*)(ws + (size_t)kvh * WS_HEAD), 0, WS_BYTES, 0x00020000);
  const char* kbase = kc + kvh * (HD * 2);
  const char* vbase = vc + kvh * (HD * 2);
  int koff = ((c >> 2) * 4) * TOK_BYTES + (4 * g + (c & 3)) * 16;
  int voff = 4 * g * TOK_BYTES + c * 16;
  asm volatile("" : "+v"(koff), "+v"(voff), "+s"(kbase), "+s"(vbase));
  if (wave == 4 || wave == 5) {
    const auto rq = __builtin_amdgcn_make_buffer_rsrc((void*)q, 0, NBATCH * NH * HD * 2, 0x00020000);
    u32x4 qs[4];
#pragma unroll
    for (int b = 0; b < 4; ++b) qs[b] = __builtin_amdgcn_raw_buffer_load_b128(rq, lane * 16, (((wave - 4) * 4 + b) * NH + 4 * kvh) * (HD * 2), 0);
    const unsigned e = __builtin_amdgcn_raw_buffer_load_b32(rws, wave == 4 ? (lane & 31) * 4 : WS_SLOT0 + (part * NPART + (lane & 31)) * SLOT_BYTES + 1024, 0, AUX_SC1);
#pragma unroll
    for (int b = 0; b < 4; ++b) *(u32x4*)(smem + LDS_Q + (((wave - 4) * 4 + (c & 3)) * 64 + (c >> 2) * 16 + b * 4 + g) * 16) = qs[b];
    *(unsigned*)(smem + LDS_EP + (wave - 4) * 256 + lane * 4) = e;
    asm volatile("s_waitcnt lgkmcnt(0)" ::: "memory");
    if (lane == 0) __hip_atomic_fetch_add((unsigned*)(smem + LDS_PW + 16), 1u, __ATOMIC_RELAXED, __HIP_MEMORY_SCOPE_WORKGROUP);
  }
  if (PF_N > 0 && wave >= 6) {
    u32x4 warm[PF_N > 0 ? PF_N : 1][4];
#pragma unroll
    for (int u = 0; u < PF_N; ++u) {
      const size_t off = (size_t)(unsigned)(((part * PF_N + u) * 2 + 1) * nphys / (2 * NPART * (PF_N > 0 ? PF_N : 1))) * PAGE_BYTES;
      const auto r = __builtin_amdgcn_make_buffer_rsrc((void*)((wave == 6 ? kbase : vbase) + off), 0, PAGE_SPAN, 0x00020000);
#pragma unroll
      for (int j = 0; j < 4; ++j) warm[u][j] = __builtin_amdgcn_raw_buffer_load_b128(r, voff + (j & 1) * TOK_BYTES, (j >> 1) * 2 * TOK_BYTES, PF_AUX);
    }
#pragma unroll
    for (int u = 0; u < PF_N; ++u) asm volatile("" :: "v"(warm[u][0]), "v"(warm[u][1]), "v"(warm[u][2]), "v"(warm[u][3]));
  }
  int lraw = 1;
#pragma unroll
  for (int b = 0; b < NBATCH; ++b) lraw = llvm_writelane(slen[b], b, lraw);
  const int npg8 = (clamp_len(lraw) + PAGE - 1) >> 4;
  int nmax = max(npg8, (int)dppu<XOR1>(npg8));
  nmax = max(nmax, (int)dppu<XOR2>(nmax));
  nmax = __builtin_amdgcn_readfirstlane(max(nmax, (int)dppu<XOR7>(nmax)));
  int npg = lane < NBATCH ? npg8 : (1 << 20);
  asm volatile("" : "+v"(npg), "+s"(nmax));
  if (wave < 4) {
#pragma unroll
    for (int b = 0; b < NBATCH; ++b) *(u32x2*)(smem + LDS_TBL + b * (NBLK * 4) + tid * 8) = y[b];
    const unsigned long long tnow = __builtin_amdgcn_s_memrealtime();
    __builtin_amdgcn_sched_barrier(0);
    unsigned d0 = y[0][0] ^ y[1][0], d1 = y[0][1] ^ y[1][1];
#pragma unroll
    for (int b = 2; b < NBATCH; ++b) {
      d0 = __builtin_amdgcn_bitop3_b32(d0, y[0][0], y[b][0], 0xF6);
      d1 = __builtin_amdgcn_bitop3_b32(d1, y[0][1], y[b][1], 0xF6);
    }
    unsigned f0, f1;
    asm("s_ff1_i32_b64 %0, %1" : "=s"(f0) : "s"(__builtin_amdgcn_ballot_w64(d0 != 0)));
    asm("s_ff1_i32_b64 %0, %1" : "=s"(f1) : "s"(__builtin_amdgcn_ballot_w64(d1 != 0)));
    asm volatile("" :: "s"(tnow));
    ((unsigned*)(smem + LDS_PW))[8 + wave] = (unsigned)tnow;
    asm volatile("" ::: "memory");
    ((unsigned*)(smem + LDS_PW))[wave] = ((wave * 128) | min(2 * f0, 2 * f1 + 1)) + 1;
  }
  STAMP(1);

  unsigned p1;
  do {
    asm volatile("" ::: "memory");
    const u32x4 a = *(const u32x4*)(smem + LDS_PW);
    p1 = __builtin_amdgcn_readfirstlane(min(min(a[0], a[1]), min(a[2], a[3])));
  } while (!p1);
  asm volatile("" ::: "memory");
  const int P = __builtin_amdgcn_readfirstlane((int)(p1 - 1 < (unsigned)nmax ? p1 - 1 : (unsigned)nmax));
  const int cnt = max(npg - P, 0);
  int send = cnt;
  send += (int)dppu<0x111>(send);
  send += (int)dppu<0x112>(send);
  send += (int)dppu<0x114>(send);
  const int npriv = __builtin_amdgcn_readlane(send, NBATCH - 1);
  int bnd;
  {
    const unsigned x = (unsigned)(npriv * 16 + P * WSH) * (unsigned)lane;
    bnd = max((int)__umulhi(x, (unsigned)((1ull << 27) / WSH)), ((int)(x >> 5) - P * (WSH - 16)) >> 4);
  }
  const int i0 = __builtin_amdgcn_readlane(bnd, part), i1 = __builtin_amdgcn_readlane(bnd, part + 1);
  const int Ps = P;
  const int nitem = i1 - i0, nbase = nitem >> 3, nextra = nitem & 7;
  const int nw = nbase + (wave < nextra ? 1 : 0);
  const int idx0 = i0 + nbase * wave + min(wave, nextra);
  const int soff = lane * NBLK - (send - cnt);
  const int item = idx0 + lane;
  int tix = item;
  {
    const int rel = tix - Ps, rend = idx0 + nw - Ps;
    int add = 0;
    for (int cur = max(idx0 - Ps, 0); cur < rend;) {
      const int s = __popcll(__builtin_amdgcn_ballot_w64(send <= cur));
      const int so = __builtin_amdgcn_readlane(soff, s);
      add = rel >= cur ? so : add;
      cur = __builtin_amdgcn_readlane(send, s);
    }
    tix = (tix + add) & (NBATCH * NBLK - 1);
  }
  const int phys = *(const int*)(smem + LDS_TBL + tix * 4);

  STAMP(2);
  const unsigned tpace = __builtin_amdgcn_readfirstlane(((const unsigned*)(smem + LDS_PW))[8]) + PACE_T0 + (((wave - PACE_N0) * PACE) >> 4);
  Page pg;
  auto issue = [&](int k) {
    const size_t off = (size_t)(unsigned)__builtin_amdgcn_readlane(phys, k) * PAGE_BYTES;
    const auto rk = __builtin_amdgcn_make_buffer_rsrc((void*)(kbase + off), 0, PAGE_SPAN, 0x00020000);
    const auto rv = __builtin_amdgcn_make_buffer_rsrc((void*)(vbase + off), 0, PAGE_SPAN, 0x00020000);
#pragma unroll
    for (int i = 0; i < 4; ++i) pg.k[i] = __builtin_amdgcn_raw_buffer_load_b128(rk, koff + (i & 1) * TOK_BYTES, (i >> 1) * 2 * TOK_BYTES, KV_AUX);
#pragma unroll
    for (int j = 0; j < 4; ++j) pg.v[j] = __builtin_amdgcn_raw_buffer_load_b128(rv, voff + (j & 1) * TOK_BYTES, (j >> 1) * 2 * TOK_BYTES, KV_AUX);
    __builtin_amdgcn_sched_barrier(0);
  };
  unsigned tiss = tpace, tarr = 0, lmin = 1000;
  auto pace = [&](int k) {
    unsigned tg = tpace + ((k * NWAVE * PACE) >> 4);
    if (SLIP_C && k > 0) {
      lmin = min(lmin, tarr - tiss);
      const unsigned t2 = tarr + SLIP_C - lmin;
      tg = (int)(t2 - tg) > 0 ? t2 : tg;
    }
    while ((int)((tiss = now()) - tg) < 0) __builtin_amdgcn_s_sleep(1);
  };
  if (nw > 0) {
    if (wave > PACE_N0) pace(0);
    issue(0);
  }

  while (__hip_atomic_load((unsigned*)(smem + LDS_PW + 16), __ATOMIC_RELAXED, __HIP_MEMORY_SCOPE_WORKGROUP) < 2u) __builtin_amdgcn_s_sleep(1);
  asm volatile("" ::: "memory");
  const unsigned epoch = *(const unsigned*)(smem + LDS_EP + lane * 4);
  const int hoff = WS_SLOT0 + (part * NPART + wave + 8 * (lane & 3)) * SLOT_BYTES + 1024;
  int lenv[2];
  u32x4 qf[2][4];
#pragma unroll
  for (int cg = 0; cg < 2; ++cg) {
    const int l = __builtin_amdgcn_ds_bpermute((4 * cg + (c >> 2)) * 4, lraw);
#pragma unroll
    for (int i = 0; i < 4; ++i) qf[cg][i] = *(const u32x4*)(smem + LDS_Q + ((cg * 4 + i) * 64 + lane) * 16);
    if (l <= 0) {
#pragma unroll
      for (int i = 0; i < 4; ++i) qf[cg][i] = u32x4{0, 0, 0, 0};
    }
    lenv[cg] = clamp_len(l) - 4 * g;
  }
  const unsigned meta = (unsigned)(tix & (NBLK - 1)) * PAGE | (item < Ps ? 0xff0000u : 0x10000u << (tix >> 9));
  const unsigned um = nitem > 0 ? (unsigned)__builtin_amdgcn_ballot_w64(i0 < min(P, npg) || (cnt > 0 && i0 - P < send && i1 - P > send - cnt)) & 0xffu : 0u;
  unsigned contrib;
  {
    const int s = part >> 2;
    const int e = __builtin_amdgcn_readlane(send, s) + Ps, n = __builtin_amdgcn_readlane(cnt, s), ps = min(Ps, __builtin_amdgcn_readlane(npg, s));
    const int nxt = __builtin_amdgcn_ds_bpermute((lane + 1) * 4, bnd);
    contrib = (unsigned)__builtin_amdgcn_ballot_w64(nxt > bnd && (bnd < ps || (n > 0 && bnd < e && nxt > e - n)));
  }

  float m[2] = {-INFINITY, -INFINITY}, l[2] = {0.f, 0.f};
  f32x4 acc[2][8];
#pragma unroll
  for (int cg = 0; cg < 2; ++cg)
#pragma unroll
    for (int i = 0; i < 8; ++i) acc[cg][i] = f32x4{0.f, 0.f, 0.f, 0.f};
  const bool o0 = lane & 1, o1 = lane & 2;

  for (int k = 0; k < nw;) {
    u32x4 kt[4];
    u32x2 va[8];
#pragma unroll
    for (int w = 0; w < 4; ++w) {
      const unsigned x0 = pg.k[0][w], x1 = pg.k[1][w], x2 = pg.k[2][w], x3 = pg.k[3][w];
      const unsigned d0 = dppu<XOR1>(x0), d1 = dppu<XOR1>(x1), d2 = dppu<XOR1>(x2), d3 = dppu<XOR1>(x3);
      const unsigned y0 = o0 ? d1 : x0, y1 = o0 ? x1 : d0, y2 = o0 ? d3 : x2, y3 = o0 ? x3 : d2;
      const unsigned e0 = dppu<XOR2>(y0), e1 = dppu<XOR2>(y1), e2 = dppu<XOR2>(y2), e3 = dppu<XOR2>(y3);
      kt[0][w] = o1 ? e2 : y0;
      kt[2][w] = o1 ? y2 : e0;
      kt[1][w] = o1 ? e3 : y1;
      kt[3][w] = o1 ? y3 : e1;
    }
#pragma unroll
    for (int i = 0; i < 8; ++i) {
      const unsigned sel = (i & 1) ? 0x07060302u : 0x05040100u;
      va[i][0] = __builtin_amdgcn_perm(pg.v[1][i >> 1], pg.v[0][i >> 1], sel);
      va[i][1] = __builtin_amdgcn_perm(pg.v[3][i >> 1], pg.v[2][i >> 1], sel);
    }
    if (SLIP_C) { __builtin_amdgcn_sched_barrier(0); tarr = now(); }
    const unsigned mt = (unsigned)__builtin_amdgcn_readlane((int)meta, k);
    ++k;
    bool issued = false;
    if (EARLY_ISSUE) {
      __builtin_amdgcn_sched_barrier(0);
      if (k < nw && (int)(now() - (tpace + ((k * NWAVE * PACE) >> 4))) >= 0) { issue(k); issued = true; }
    }
#pragma unroll
    for (int cg = 0; cg < 2; ++cg) {
      if ((mt >> (16 + 4 * cg)) & 0xfu) {
        f32x4 s = {0.f, 0.f, 0.f, 0.f};
#pragma unroll
        for (int i = 0; i < 4; ++i) s = mfma32(kt[i], qf[cg][i], s);
        const int lim = ((mt >> (16 + 4 * cg + (c >> 2))) & 1) ? lenv[cg] - (int)(mt & 0xffffu) : 0;
        float x[4];
#pragma unroll
        for (int r = 0; r < 4; ++r) x[r] = r < lim ? s[r] * SC_LOG2 : -INFINITY;
        const float mx = swap32_max(swap16_max(fmx(fmx(x[0], x[1]), fmx(x[2], x[3]))));
        if (__builtin_amdgcn_ballot_w64(mx > m[cg])) {
          const float mn = fmx(m[cg], mx);
          const float alpha = mn == -INFINITY ? 1.f : __builtin_amdgcn_exp2f(m[cg] - mn);
          m[cg] = mn;
          l[cg] *= alpha;
#pragma unroll
          for (int i = 0; i < 8; ++i) acc[cg][i] *= alpha;
        }
        const float ms = m[cg] == -INFINITY ? 0.f : m[cg];
#pragma unroll
        for (int r = 0; r < 4; ++r) x[r] = __builtin_amdgcn_exp2f(x[r] - ms);
        l[cg] += (x[0] + x[1]) + (x[2] + x[3]);
        const u32x2 pb = {cvtpk(x[0], x[1]), cvtpk(x[2], x[3])};
#pragma unroll
        for (int i = 0; i < 8; ++i) acc[cg][i] = mfma16(va[i], pb, acc[cg][i]);
      }
    }
    if (k < nw && !issued) {
      pace(k);
      issue(k);
    }
  }
  STAMP(4);

  float* lo = (float*)(smem + LDS_O);
  float* lml = (float*)(smem + LDS_ML);
#pragma unroll
  for (int cg = 0; cg < 2; ++cg) {
    if ((um >> (4 * cg + (c >> 2))) & 1) {
      lml[(16 * cg + c) * 40 + 32 + wave] = m[cg];
      lml[(16 * cg + c) * 40 + g * NWAVE + wave] = l[cg];
      float* rw = lo + (16 * cg + c) * COL_F + wave * HD + 4 * g;
#pragma unroll
      for (int i = 0; i < 8; ++i) *(f32x4*)(rw + 16 * i) = acc[cg][i];
    }
  }
  unsigned todo = um;
  if (wave >> 2) todo &= todo - 1;
  const unsigned tag = (unsigned)__builtin_amdgcn_readlane((int)epoch, part) + 1;
  const unsigned mine = contrib & (0x01010101u << wave);
  const bool hon = (mine >> (wave + 8 * (lane & 3))) & 1;
  int hpoll = hon ? hoff : OOB;
  unsigned htag = hon ? tag : 0u;
  int doff[4];
  unsigned etag[4];
#pragma unroll
  for (int u = 0; u < 4; ++u) {
    doff[u] = __builtin_amdgcn_readlane(hpoll, u) - 1024 + lane * 16;
    etag[u] = __builtin_amdgcn_readlane(htag, u);
  }
  asm volatile("" : "+v"(hpoll), "+v"(htag), "+v"(doff[0]), "+v"(doff[1]), "+v"(doff[2]), "+v"(doff[3]));
  lds_barrier();
  STAMP(5);

  while (todo) {
    const int col = 4 * __builtin_ctz(todo) + (wave & 3);
    todo &= todo - 1;
    todo &= todo - 1;
    const f32x2 ml = {lml[col * 40 + 32 + (lane & 7)], lml[col * 40 + (lane & 31)]};
    f32x2 o[NWAVE];
#pragma unroll
    for (int w = 0; w < NWAVE; ++w) o[w] = *(const f32x2*)(lo + col * COL_F + w * HD + 2 * lane);
    __builtin_amdgcn_sched_barrier(0);
    const unsigned ct = (unsigned)__builtin_amdgcn_readlane((int)epoch, col) + 1;
    const int slot = WS_SLOT0 + (col * NPART + part) * SLOT_BYTES;
    const float M = max8(ml[0]);
    const float f = __builtin_amdgcn_exp2f(ml[0] - M);
    float L = sum8(f * ml[1]);
    L = swap16_sum(L + dppf<XOR15>(L));
    if (HINT_EARLY) {
      __builtin_amdgcn_raw_buffer_store_b128(u32x4{__float_as_uint(M), ct, __float_as_uint(L), ct}, rws, slot + (lane < 8 ? lane * 16 + 1024 : OOB), 0, AUX_SC1);
      __builtin_amdgcn_sched_barrier(0);
    }
    const f32x2 a = wsum8(o, f);
    __builtin_amdgcn_raw_buffer_store_b128(u32x4{__float_as_uint(a[0]), ct, __float_as_uint(a[1]), ct}, rws, slot + lane * 16, 0, AUX_SC1);
    if (!HINT_EARLY)
      __builtin_amdgcn_raw_buffer_store_b128(u32x4{__float_as_uint(M), ct, __float_as_uint(L), ct}, rws, slot + (lane < 8 ? lane * 16 + 1024 : OOB), 0, AUX_SC1);
  }
  STAMP(6);

  float* fin = (float*)(smem + LDS_FIN);
  {
    float M = -3.0e38f, L = 0.f;
    f32x2 a = {0.f, 0.f};
    if (mine) {
      unsigned spin = 0;
      u32x4 h;
#pragma unroll 1
      for (; spin < SPIN_CAP; ++spin) {
        asm volatile("" ::: "memory");
        h = __builtin_amdgcn_raw_buffer_load_b128(rws, hpoll, 0, AUX_SC1);
        if (!__builtin_amdgcn_ballot_w64(__builtin_amdgcn_bitop3_b32(h[1], h[3], htag, 0x7E) != 0)) break;
        if (POLL_SLEEP) __builtin_amdgcn_s_sleep(POLL_SLEEP);
      }
      STAMP(3);
      u32x4 d[4];
#pragma unroll
      for (int u = 0; u < 4; ++u) d[u] = __builtin_amdgcn_raw_buffer_load_b128(rws, doff[u], 0, AUX_SC1);
      __builtin_amdgcn_sched_barrier(0);
      const float mu = hon ? __uint_as_float(h[0]) : -INFINITY;
      M = fmx(fmx(fmx(mu, dppf<XOR1>(mu)), dppf<XOR2>(mu)), dppf<XOR3>(mu));
      const float f = __builtin_amdgcn_exp2f(mu - M);
      L = f * __uint_as_float(h[2]);
      L += dppf<XOR1>(L);
      L += dppf<XOR2>(L);
      float fu[4];
#pragma unroll
      for (int u = 0; u < 4; ++u) fu[u] = rdlane(f, u);
      __builtin_amdgcn_sched_barrier(0);
#pragma unroll 1
      for (; spin < SPIN_CAP; ++spin) {
        const unsigned bad = (__builtin_amdgcn_bitop3_b32(d[0][1], d[0][3], etag[0], 0x7E) | __builtin_amdgcn_bitop3_b32(d[1][1], d[1][3], etag[1], 0x7E)) |
                             (__builtin_amdgcn_bitop3_b32(d[2][1], d[2][3], etag[2], 0x7E) | __builtin_amdgcn_bitop3_b32(d[3][1], d[3][3], etag[3], 0x7E));
        if (!__builtin_amdgcn_ballot_w64(bad != 0)) break;
        asm volatile("" ::: "memory");
#pragma unroll
        for (int u = 0; u < 4; ++u) d[u] = __builtin_amdgcn_raw_buffer_load_b128(rws, doff[u], 0, AUX_SC1);
      }
      a[0] = (fu[0] * __uint_as_float(d[0][0]) + fu[1] * __uint_as_float(d[1][0])) + (fu[2] * __uint_as_float(d[2][0]) + fu[3] * __uint_as_float(d[3][0]));
      a[1] = (fu[0] * __uint_as_float(d[0][2]) + fu[1] * __uint_as_float(d[1][2])) + (fu[2] * __uint_as_float(d[2][2]) + fu[3] * __uint_as_float(d[3][2]));
    }
    *(f32x2*)(fin + wave * HD + 2 * lane) = a;
    *(f32x2*)(fin + NWAVE * HD + 2 * wave) = f32x2{M, L};
  }
  lds_barrier();
  if (wave == 0) {
    const f32x2 ml = *(const f32x2*)(fin + NWAVE * HD + 2 * (lane & 7));
    f32x2 o[NWAVE];
    const int pos = ((2 * lane) & 7) * 16 + (lane >> 4) * 4 + ((lane >> 2) & 3);
#pragma unroll
    for (int w = 0; w < NWAVE; ++w) o[w] = f32x2{fin[w * HD + pos], fin[w * HD + pos + 16]};
    __builtin_amdgcn_sched_barrier(0);
    const float f = __builtin_amdgcn_exp2f(ml[0] - max8(ml[0]));
    const f32x2 a = wsum8(o, f) * __builtin_amdgcn_rcpf(sum8(f * ml[1]));
    out[((part >> 2) * NH + 4 * kvh + (part & 3)) * (HD / 2) + lane] = cvtpk(a[0], a[1]);
    __builtin_amdgcn_raw_buffer_store_b32(tag, rws, lane ? OOB : 0, part * 4, AUX_SC1);
  }
  STAMP(7);
  STAMP_FLUSH;
}

}

extern "C" void kernel_launch(void* const* d_in, const int* in_sizes, int n_in, void* d_out, int out_size, void* d_ws,
                              size_t ws_size, hipStream_t stream) {
  (void)in_sizes; (void)n_in; (void)out_size; (void)ws_size;
  (void)hipFuncSetAttribute((const void*)attn_fused, hipFuncAttributeMaxDynamicSharedMemorySize, LDS_BYTES);
  hipLaunchKernelGGL(attn_fused, dim3(NKVH * NPART), dim3(NTHR), LDS_BYTES, stream, (const char*)d_in[0], (const char*)d_in[1],
                     (const char*)d_in[2], (const int*)d_in[3], (const int*)d_in[4], (char*)d_ws, (unsigned*)d_out, in_sizes[1] / (PAGE_BYTES / 2));
}
```

```cpp
#define PACE 470
#define PF_N 1
#include <hip/hip_runtime.h>
#include <hip/hip_bf16.h>
#include <stdint.h>

#define KV_AUX 2
#define WSH 17
#define PACE_N0 1
#define HINT_EARLY 1
#define POLL_SLEEP 0
#define SLIP_C 0
#define MAP 0
#define ROT 0
#define PF_AUX 0
#define PACE_T0 0
#define MRG0 1
#define LEAD 140
#define LEAD2 35
#define OUT_TOUCH 0
#define OUT_AUX 16
#define PRIO 1
#define STAMP(i)
#define STAMP_DECL
#define STAMP_FLUSH

namespace {

constexpr int NBATCH = 8, NH = 32, NKVH = 8, HD = 128, PAGE = 16, NBLK = 512, LK = NBLK * PAGE;
constexpr int NPART = 32, NWAVE = 8, NTHR = NWAVE * 64, NCOL = 32;
constexpr int TOK_BYTES = NKVH * HD * 2;
constexpr int PAGE_BYTES = PAGE * TOK_BYTES;
constexpr int PAGE_SPAN = (PAGE - 1) * TOK_BYTES + HD * 2;
constexpr float SC_LOG2 = 0.08838834764831845f * 1.4426950408889634f;
constexpr int AUX_SC1 = 16;
constexpr unsigned SPIN_CAP = 1u << 20;
constexpr int OOB = 0x70000000;
constexpr int GS = 12;

typedef __attribute__((ext_vector_type(8))) __bf16 bf16x8;
typedef __attribute__((ext_vector_type(2))) __bf16 bf16x2;
typedef __attribute__((ext_vector_type(4))) short s16x4;
typedef __attribute__((ext_vector_type(4))) float f32x4;
typedef __attribute__((ext_vector_type(2))) float f32x2;
typedef __attribute__((ext_vector_type(4))) unsigned u32x4;
typedef __attribute__((ext_vector_type(2))) unsigned u32x2;
typedef __attribute__((ext_vector_type(8))) int i32x8;
extern "C" __device__ int llvm_writelane(int, int, int) __asm("llvm.amdgcn.writelane.i32");

constexpr int COL_F = NWAVE * HD + 4;
constexpr int LDS_TBL = 0;
constexpr int LDS_PW = LDS_TBL + NBLK * NBATCH * 4;
constexpr int LDS_EP = LDS_PW + 64;
constexpr int LDS_Q = LDS_EP + 512;
constexpr int LDS_ML = LDS_Q + 8192;
constexpr int LDS_O = LDS_ML + NCOL * 40 * 4;
constexpr int LDS_BYTES = LDS_O + NCOL * COL_F * 4;
constexpr int LDS_FIN = LDS_TBL;

constexpr int WS_HEAD = 2 << 20, WS_SLOT0 = 1024, SLOT_BYTES = 1152;
constexpr int WS_BYTES = WS_SLOT0 + NCOL * NPART * SLOT_BYTES;

__device__ __forceinline__ f32x4 mfma32(u32x4 a, u32x4 b, f32x4 c) {
  return __builtin_amdgcn_mfma_f32_16x16x32_bf16(__builtin_bit_cast(bf16x8, a), __builtin_bit_cast(bf16x8, b), c, 0, 0, 0);
}
__device__ __forceinline__ f32x4 mfma16(u32x2 a, u32x2 b, f32x4 c) {
  return __builtin_amdgcn_mfma_f32_16x16x16bf16_1k(__builtin_bit_cast(s16x4, a), __builtin_bit_cast(s16x4, b), c, 0, 0, 0);
}
__device__ __forceinline__ unsigned cvtpk(float lo, float hi) {
  return __builtin_bit_cast(unsigned, __builtin_convertvector(f32x2{lo, hi}, bf16x2));
}
__device__ __forceinline__ float fmx(float a, float b) { return __builtin_elementwise_maximum(a, b); }
__device__ __forceinline__ float swap16_max(float x) {
  auto s = __builtin_amdgcn_permlane16_swap(__float_as_uint(x), __float_as_uint(x), false, false);
  return fmx(__uint_as_float(s[0]), __uint_as_float(s[1]));
}
__device__ __forceinline__ float swap32_max(float x) {
  auto s = __builtin_amdgcn_permlane32_swap(__float_as_uint(x), __float_as_uint(x), false, false);
  return fmx(__uint_as_float(s[0]), __uint_as_float(s[1]));
}
__device__ __forceinline__ float swap16_sum(float x) {
  auto s = __builtin_amdgcn_permlane16_swap(__float_as_uint(x), __float_as_uint(x), false, false);
  return __uint_as_float(s[0]) + __uint_as_float(s[1]);
}
template <int CTRL> __device__ __forceinline__ unsigned dppu(unsigned x) {
  return (unsigned)__builtin_amdgcn_mov_dpp((int)x, CTRL, 0xf, 0xf, true);
}
template <int CTRL> __device__ __forceinline__ float dppf(float x) { return __uint_as_float(dppu<CTRL>(__float_as_uint(x))); }
constexpr int XOR1 = 0xB1, XOR2 = 0x4E, XOR3 = 0x1B, XOR7 = 0x141, XOR15 = 0x140;
__device__ __forceinline__ float max8(float x) {
  x = fmx(x, dppf<XOR7>(x));
  return fmx(fmx(fmx(x, dppf<XOR1>(x)), dppf<XOR2>(x)), dppf<XOR3>(x));
}
__device__ __forceinline__ float sum8(float x) {
  x += dppf<XOR1>(x); x += dppf<XOR2>(x); return x + dppf<XOR7>(x);
}
__device__ __forceinline__ float rdlane(float x, int l) { return __int_as_float(__builtin_amdgcn_readlane(__float_as_int(x), l)); }
__device__ __forceinline__ int clamp_len(int l) { return (l <= 0 || l > LK) ? LK : l; }
__device__ __forceinline__ void lds_barrier() { asm volatile("s_waitcnt lgkmcnt(0)\n\ts_barrier" ::: "memory"); }
__device__ __forceinline__ unsigned now() { return (unsigned)__builtin_amdgcn_s_memrealtime(); }
__device__ __forceinline__ f32x2 wsum8(const f32x2 (&o)[8], float f, int l0) {
  float s[8];
#pragma unroll
  for (int w = 0; w < 8; ++w) s[w] = rdlane(f, l0 + w);
  asm volatile("" : "+s"(s[0]), "+s"(s[1]), "+s"(s[2]), "+s"(s[3]), "+s"(s[4]), "+s"(s[5]), "+s"(s[6]), "+s"(s[7]));
  const f32x2 a = o[0] * s[0] + o[2] * s[2] + o[4] * s[4] + o[6] * s[6];
  const f32x2 b = o[1] * s[1] + o[3] * s[3] + o[5] * s[5] + o[7] * s[7];
  return a + b;
}

#define CND4(D, A, B, P)                                                                        \
  "v_cndmask_b32_dpp %[" D "0], %[" A "0], %[" B "0], vcc " P " row_mask:0xf bank_mask:0xf\n\t" \
  "v_cndmask_b32_dpp %[" D "1], %[" A "1], %[" B "1], vcc " P " row_mask:0xf bank_mask:0xf\n\t" \
  "v_cndmask_b32_dpp %[" D "2], %[" A "2], %[" B "2], vcc " P " row_mask:0xf bank_mask:0xf\n\t" \
  "v_cndmask_b32_dpp %[" D "3], %[" A "3], %[" B "3], vcc " P " row_mask:0xf bank_mask:0xf\n\t"
#define SWAP_STAGE(MASK, P)                                                                                              \
  asm("s_mov_b32 vcc_lo, " MASK "\n\ts_mov_b32 vcc_hi, " MASK "\n\t" CND4("x", "b", "a", P) "s_not_b64 vcc, vcc\n\t" CND4("y", "a", "b", P) \
      "s_nop 1"                                                                                                          \
      : [x0] "=&v"(x[0]), [x1] "=&v"(x[1]), [x2] "=&v"(x[2]), [x3] "=&v"(x[3]), [y0] "=&v"(y[0]), [y1] "=&v"(y[1]),      \
        [y2] "=&v"(y[2]), [y3] "=&v"(y[3])                                                                               \
      : [a0] "v"(a[0]), [a1] "v"(a[1]), [a2] "v"(a[2]), [a3] "v"(a[3]), [b0] "v"(b[0]), [b1] "v"(b[1]), [b2] "v"(b[2]),  \
        [b3] "v"(b[3])                                                                                                   \
      : "vcc", "scc")
__device__ __forceinline__ void swap1(const u32x4& a, const u32x4& b, u32x4& ox, u32x4& oy) {
  unsigned x[4], y[4];
  SWAP_STAGE("0x55555555", "quad_perm:[1,0,3,2]");
  ox = u32x4{x[0], x[1], x[2], x[3]};
  oy = u32x4{y[0], y[1], y[2], y[3]};
}
__device__ __forceinline__ void swap2(const u32x4& a, const u32x4& b, u32x4& ox, u32x4& oy) {
  unsigned x[4], y[4];
  SWAP_STAGE("0x33333333", "quad_perm:[2,3,0,1]");
  ox = u32x4{x[0], x[1], x[2], x[3]};
  oy = u32x4{y[0], y[1], y[2], y[3]};
}
__device__ __forceinline__ void quad_transpose(const u32x4 (&in)[4], u32x4 (&out)[4]) {
  u32x4 t[4];
  swap1(in[0], in[1], t[0], t[1]);
  swap1(in[2], in[3], t[2], t[3]);
  swap2(t[0], t[2], out[0], out[2]);
  swap2(t[1], t[3], out[1], out[3]);
}

struct Page {
  u32x4 k[4];
  u32x4 v[4];
};

__global__ __launch_bounds__(NTHR, 2) void attn_fused(const char* __restrict__ q, const char* __restrict__ kc,
                                                      const char* __restrict__ vc, const int* __restrict__ bt,
                                                      const int* __restrict__ lens, char* __restrict__ ws,
                                                      __hip_bfloat16* __restrict__ out, int nphys) {
  extern __shared__ __attribute__((aligned(16))) char smem[];
  STAMP_DECL;
  STAMP(0);
  const int tid = threadIdx.x, lane = tid & 63, wave = __builtin_amdgcn_readfirstlane(tid >> 6);
  if (tid < 16) ((unsigned*)(smem + LDS_PW))[tid] = 0u;
  asm volatile("s_waitcnt lgkmcnt(0)" :: "s"(q), "s"(kc), "s"(vc), "s"(bt), "s"(lens), "s"(ws), "s"(out), "s"(nphys) : "memory");
  const i32x8 slen = *(const __attribute__((address_space(4))) i32x8*)(uintptr_t)lens;
  const int g = lane >> 4, c = lane & 15;
  const int kvh = blockIdx.x & 7, part = blockIdx.x >> 3;

  u32x2 y[NBATCH];
  const bool btw = (unsigned)(wave - 2) < 4u;
  const int bcol = (tid - 128) * 8;
  if (btw) {
    const auto rbt = __builtin_amdgcn_make_buffer_rsrc((void*)bt, 0, NBATCH * NBLK * 4, 0x00020000);
#pragma unroll
    for (int b = 0; b < NBATCH; ++b) y[b] = __builtin_amdgcn_raw_buffer_load_b64(rbt, bcol + (b & 1) * 2048, (b >> 1) * 4096, 0);
  }
  asm volatile("s_barrier" ::: "memory");
  const auto rws = __builtin_amdgcn_make_buffer_rsrc((void*)(ws + (size_t)kvh * WS_HEAD), 0, WS_BYTES, 0x00020000);
  const auto rout = __builtin_amdgcn_make_buffer_rsrc((void*)out, 0, NBATCH * NH * HD * 2, 0x00020000);
  const char* kbase = kc + kvh * (HD * 2);
  const char* vbase = vc + kvh * (HD * 2);
  int koff = ((c >> 2) * 4) * TOK_BYTES + (4 * g + (c & 3)) * 16;
  int voff = 4 * g * TOK_BYTES + c * 16;
  asm volatile("" : "+v"(koff), "+v"(voff), "+s"(kbase), "+s"(vbase));
  if (btw) {
#pragma unroll
    for (int b = 0; b < NBATCH; ++b) *(u32x2*)(smem + LDS_TBL + b * (NBLK * 4) + bcol) = y[b];
    unsigned d0 = y[0][0] ^ y[1][0], d1 = y[0][1] ^ y[1][1];
#pragma unroll
    for (int b = 2; b < NBATCH; ++b) {
      d0 = __builtin_amdgcn_bitop3_b32(d0, y[0][0], y[b][0], 0xF6);
      d1 = __builtin_amdgcn_bitop3_b32(d1, y[0][1], y[b][1], 0xF6);
    }
    unsigned f0, f1;
    asm("s_ff1_i32_b64 %0, %1" : "=s"(f0) : "s"(__builtin_amdgcn_ballot_w64(d0 != 0)));
    asm("s_ff1_i32_b64 %0, %1" : "=s"(f1) : "s"(__builtin_amdgcn_ballot_w64(d1 != 0)));
    asm volatile("" ::: "memory");
    ((unsigned*)(smem + LDS_PW))[wave - 2] = (((wave - 2) * 128) | min(2 * f0, 2 * f1 + 1)) + 1;
  }
  if (wave == 6) {
    const auto rq = __builtin_amdgcn_make_buffer_rsrc((void*)(q + 4 * kvh * (HD * 2)), 0, NBATCH * NH * HD * 2 - 4 * kvh * (HD * 2), 0x00020000);
    u32x4 qs[NBATCH];
#pragma unroll
    for (int b = 0; b < NBATCH; ++b) qs[b] = __builtin_amdgcn_raw_buffer_load_b128(rq, lane * 16, b * (NH * HD * 2), 0);
    const unsigned e = __builtin_amdgcn_raw_buffer_load_b32(rws, lane < 32 ? lane * 4 : WS_SLOT0 + (part * NPART + lane - 32) * SLOT_BYTES + 1024, 0, AUX_SC1);
    const unsigned ot = OUT_TOUCH ? __builtin_amdgcn_raw_buffer_load_b32(rout, ((part >> 2) * NH + 4 * kvh + (part & 3)) * (HD * 2) + lane * 4, 0, OUT_TOUCH - 1) : 0u;
#pragma unroll
    for (int b = 0; b < NBATCH; ++b) *(u32x4*)(smem + LDS_Q + (((b >> 2) * 4 + (c & 3)) * 64 + (c >> 2) * 16 + (b & 3) * 4 + g) * 16) = qs[b];
    *(unsigned*)(smem + LDS_EP + lane * 4) = e;
    asm volatile("" ::: "memory");
    ((unsigned*)(smem + LDS_PW))[4] = 1u;
    asm volatile("" :: "v"(ot));
  }
  if (PF_N > 0 && wave == 7) {
    u32x4 warm[PF_N > 0 ? 2 * PF_N : 1][4];
#pragma unroll
    for (int u = 0; u < 2 * PF_N; ++u) {
      const size_t off = (size_t)(unsigned)(((part * PF_N + (u >> 1)) * 2 + 1) * nphys / (2 * NPART * (PF_N > 0 ? PF_N : 1))) * PAGE_BYTES;
      const auto r = __builtin_amdgcn_make_buffer_rsrc((void*)(((u & 1) ? vbase : kbase) + off), 0, PAGE_SPAN, 0x00020000);
#pragma unroll
      for (int j = 0; j < 4; ++j) warm[u][j] = __builtin_amdgcn_raw_buffer_load_b128(r, voff + (j & 1) * TOK_BYTES, (j >> 1) * 2 * TOK_BYTES, PF_AUX);
    }
#pragma unroll
    for (int u = 0; u < 2 * PF_N; ++u) asm volatile("" :: "v"(warm[u][0]), "v"(warm[u][1]), "v"(warm[u][2]), "v"(warm[u][3]));
  }
  __builtin_amdgcn_s_waitcnt(0x0F70);
  int lraw = 1;
#pragma unroll
  for (int b = 0; b < NBATCH; ++b) lraw = llvm_writelane(slen[b], b, lraw);
  const int npg8 = (clamp_len(lraw) + PAGE - 1) >> 4;
  int nmax = max(npg8, (int)dppu<XOR1>(npg8));
  nmax = max(nmax, (int)dppu<XOR2>(nmax));
  nmax = __builtin_amdgcn_readfirstlane(max(nmax, (int)dppu<XOR7>(nmax)));
  int npg = lane < NBATCH ? npg8 : (1 << 20);
  asm volatile("" : "+v"(npg), "+s"(nmax));
  STAMP(1);

  unsigned p1;
  do {
    asm volatile("" ::: "memory");
    const u32x4 a = *(const u32x4*)(smem + LDS_PW);
    p1 = __builtin_amdgcn_readfirstlane(min(min(a[0], a[1]), min(a[2], a[3])));
  } while (!p1);
  asm volatile("" ::: "memory");
  const int P = __builtin_amdgcn_readfirstlane((int)(p1 - 1 < (unsigned)nmax ? p1 - 1 : (unsigned)nmax));
  const int cnt = max(npg - P, 0);
  int send = cnt;
  send += (int)dppu<0x111>(send);
  send += (int)dppu<0x112>(send);
  send += (int)dppu<0x114>(send);
  const int npriv = __builtin_amdgcn_readlane(send, NBATCH - 1);
  int bnd;
  {
    const unsigned x = (unsigned)(npriv * 16 + P * WSH) * (unsigned)lane;
    bnd = max((int)__umulhi(x, (unsigned)((1ull << 27) / WSH)), ((int)(x >> 5) - P * (WSH - 16)) >> 4);
  }
  const int i0 = __builtin_amdgcn_readlane(bnd, part), i1 = __builtin_amdgcn_readlane(bnd, part + 1);
  const int Ps = P;
  const int nitem = i1 - i0, nbase = nitem >> 3, nextra = nitem & 7;
  const int nw = nbase + (wave < nextra ? 1 : 0);
  const int idx0 = i0 + nbase * wave + min(wave, nextra);
  const int soff = lane * NBLK - (send - cnt);
  const int item = idx0 + lane;
  int tix = item;
  {
    const int rel = tix - Ps, rend = idx0 + nw - Ps;
    int add = 0;
    for (int cur = max(idx0 - Ps, 0); cur < rend;) {
      const int s = __popcll(__builtin_amdgcn_ballot_w64(send <= cur));
      const int so = __builtin_amdgcn_readlane(soff, s);
      add = rel >= cur ? so : add;
      cur = __builtin_amdgcn_readlane(send, s);
    }
    tix = (tix + add) & (NBATCH * NBLK - 1);
  }
  const unsigned tstart = now();
  const int phys = *(const int*)(smem + LDS_TBL + tix * 4);

  STAMP(2);
  unsigned tpace;
  int dpace;
  Page pg;
  auto issue = [&](int k) {
    const size_t off = (size_t)(unsigned)__builtin_amdgcn_readlane(phys, k) * PAGE_BYTES;
    const auto rk = __builtin_amdgcn_make_buffer_rsrc((void*)(kbase + off), 0, PAGE_SPAN, 0x00020000);
    const auto rv = __builtin_amdgcn_make_buffer_rsrc((void*)(vbase + off), 0, PAGE_SPAN, 0x00020000);
#pragma unroll
    for (int i = 0; i < 4; ++i) pg.k[i] = __builtin_amdgcn_raw_buffer_load_b128(rk, koff + (i & 1) * TOK_BYTES, (i >> 1) * 2 * TOK_BYTES, KV_AUX);
#pragma unroll
    for (int j = 0; j < 4; ++j) pg.v[j] = __builtin_amdgcn_raw_buffer_load_b128(rv, voff + (j & 1) * TOK_BYTES, (j >> 1) * 2 * TOK_BYTES, KV_AUX);
    __builtin_amdgcn_sched_barrier(0);
  };
  auto pace = [&](int k) {
    const unsigned tg = tpace + ((k * dpace) >> 5);
    while ((int)(now() - tg) < 0) __builtin_amdgcn_s_sleep(1);
  };
  if (wave == 0) ((unsigned*)(smem + LDS_PW))[5] = tstart | 1u;
  if (nw > 0 && wave <= PACE_N0) issue(0);

  unsigned contrib;
  {
    const int s = part >> 2;
    const int e = __builtin_amdgcn_readlane(send, s) + Ps, n = __builtin_amdgcn_readlane(cnt, s), ps = min(Ps, __builtin_amdgcn_readlane(npg, s));
    const int nxt = __builtin_amdgcn_ds_bpermute((lane + 1) * 4, bnd);
    contrib = (unsigned)__builtin_amdgcn_ballot_w64(nxt > bnd && (bnd < ps || (n > 0 && bnd < e && nxt > e - n)));
  }
  const unsigned um = nitem > 0 ? (unsigned)__builtin_amdgcn_ballot_w64(i0 < min(P, npg) || (cnt > 0 && i0 - P < send && i1 - P > send - cnt)) & 0xffu : 0u;
  const int nseq = __popc(um);
  const bool wide = nseq > 2;
  {
    const float span = (float)(npriv * 16 + P * WSH) * (PACE * (1.f / 32.f)) - (wide ? LEAD * 256.f : nseq == 2 ? LEAD2 * 256.f : 0.f);
    dpace = __builtin_amdgcn_readfirstlane((int)(fmaxf(span, 0.f) * __builtin_amdgcn_rcpf((float)max(nitem, 1))));
  }
  do {
    asm volatile("" ::: "memory");
    tpace = __builtin_amdgcn_readfirstlane(((const unsigned*)(smem + LDS_PW))[5]);
  } while (!tpace);
  tpace += ((wave - PACE_N0) * dpace >> 8) + PACE_T0;
  if (nw > 0 && wave > PACE_N0) {
    pace(0);
    issue(0);
  }

  while (__hip_atomic_load((unsigned*)(smem + LDS_PW + 16), __ATOMIC_RELAXED, __HIP_MEMORY_SCOPE_WORKGROUP) == 0u) __builtin_amdgcn_s_sleep(1);
  asm volatile("" ::: "memory");
  const unsigned epoch = *(const unsigned*)(smem + LDS_EP + lane * 4);
  int lenv[2];
  u32x4 qf[2][4];
#pragma unroll
  for (int cg = 0; cg < 2; ++cg) {
    const int l = __builtin_amdgcn_ds_bpermute((4 * cg + (c >> 2)) * 4, lraw);
#pragma unroll
    for (int i = 0; i < 4; ++i) qf[cg][i] = *(const u32x4*)(smem + LDS_Q + ((cg * 4 + i) * 64 + lane) * 16);
    if (l <= 0) {
#pragma unroll
      for (int i = 0; i < 4; ++i) qf[cg][i] = u32x4{0, 0, 0, 0};
    }
    lenv[cg] = clamp_len(l) - 4 * g;
  }
  const unsigned meta = (unsigned)(tix & (NBLK - 1)) * PAGE | (item < Ps ? 0xff0000u : 0x10000u << (tix >> 9));
  const int rk = (wave - nextra) & 7;
  int ncol = wide ? (nseq + 1 - (rk >> 2)) >> 1 : (unsigned)(rk - MRG0) < 4u ? nseq : 0;
  int mrd, lrd, pcol, phdr;
  unsigned ctv;
  {
    const int u = (lane >> 3) & 3;
    const int rank = ((um >> (lane & 7)) & 1) ? __popc(um & ((1u << (lane & 7)) - 1)) : -1;
    const unsigned long long hit = __builtin_amdgcn_ballot_w64(rank == (wide ? 2 * u + (rk >> 2) : u));
    const unsigned mine8 = (unsigned)(hit >> (8 * u)) & 0xffu;
    const bool on = mine8 != 0 && lane < 32;
    const int col = 4 * __builtin_ctz(mine8 | 0x80u) + (rk & 3);
    mrd = LDS_ML + (col * 40 + 32 + (lane & 7)) * 4;
    lrd = LDS_ML + (col * 40 + (lane & 7) * 4) * 4;
    pcol = col;
    const int slot = WS_SLOT0 + (col * NPART + part) * SLOT_BYTES;
    phdr = on ? slot + 1024 + (lane & 7) * 16 : OOB;
    ctv = (unsigned)__builtin_amdgcn_ds_bpermute(col * 4, (int)epoch) + 1;
  }
  int phdr0 = wide || lane >= 8 ? OOB : phdr;
  int ord = LDS_O + (__builtin_amdgcn_readfirstlane(pcol) * COL_F + 2 * lane) * 4;
  int prow = WS_SLOT0 + (__builtin_amdgcn_readfirstlane(pcol) * NPART + part) * SLOT_BYTES + lane * 16;
  unsigned ct = __builtin_amdgcn_readfirstlane(ctv);
  asm volatile("" : "+v"(mrd), "+v"(lrd), "+v"(ord), "+v"(prow), "+v"(ct), "+v"(phdr0), "+v"(phdr), "+v"(ctv), "+v"(pcol), "+s"(ncol));
  int wm[2], wl[2], wo[2];
#pragma unroll
  for (int cg = 0; cg < 2; ++cg) {
    wm[cg] = LDS_ML + ((16 * cg + c) * 40 + 32 + rk) * 4;
    wl[cg] = LDS_ML + ((16 * cg + c) * 40 + rk * 4 + g) * 4;
    wo[cg] = ((um >> (4 * cg + (c >> 2))) & 1) ? LDS_O + ((16 * cg + c) * COL_F + rk * HD + 4 * g) * 4 : -1;
  }
  asm volatile("" : "+v"(wm[0]), "+v"(wl[0]), "+v"(wo[0]), "+v"(wo[1]));

  float m[2] = {-INFINITY, -INFINITY}, ms[2] = {0.f, 0.f}, l[2] = {0.f, 0.f};
  f32x4 acc[2][8];
#pragma unroll
  for (int cg = 0; cg < 2; ++cg)
#pragma unroll
    for (int i = 0; i < 8; ++i) acc[cg][i] = f32x4{0.f, 0.f, 0.f, 0.f};
  if (PRIO) __builtin_amdgcn_s_setprio(2);

  auto put_stats = [&]() {
#pragma unroll
    for (int cg = 0; cg < 2; ++cg) {
      *(float*)(smem + wm[0] + cg * (16 * 160)) = m[cg] * SC_LOG2;
      *(float*)(smem + wl[0] + cg * (16 * 160)) = l[cg];
    }
    asm volatile("" ::: "memory");
    *(unsigned char*)(smem + LDS_PW + 24 + rk) = 1;
  };
  if (nw == 0) put_stats();
  for (int k = 0; k < nw;) {
    u32x4 kt[4];
    u32x2 va[8];
    quad_transpose(pg.k, kt);
    const unsigned mt = (unsigned)__builtin_amdgcn_readlane((int)meta, k);
    ++k;
    u32x2 pb[2];
#pragma unroll
    for (int cg = 0; cg < 2; ++cg) {
      if ((mt >> (16 + 4 * cg)) & 0xfu) {
        f32x4 s = {0.f, 0.f, 0.f, 0.f};
#pragma unroll
        for (int i = 0; i < 4; ++i) s = mfma32(kt[i], qf[cg][i], s);
        const int lim = ((mt >> (16 + 4 * cg + (c >> 2))) & 1) ? lenv[cg] - (int)(mt & 0xffffu) : 0;
        unsigned long long live[4];
#pragma unroll
        for (int r = 0; r < 4; ++r) live[r] = __builtin_amdgcn_ballot_w64(r < lim);
        asm volatile("" : "+s"(live[0]), "+s"(live[1]), "+s"(live[2]), "+s"(live[3]));
        float x[4];
#pragma unroll
        for (int r = 0; r < 4; ++r) x[r] = __builtin_amdgcn_inverse_ballot_w64(live[r]) ? s[r] : -INFINITY;
        const float mx = swap32_max(swap16_max(fmx(fmx(x[0], x[1]), fmx(x[2], x[3]))));
        if (__builtin_amdgcn_ballot_w64(mx > m[cg])) {
          m[cg] = fmx(m[cg], mx);
          const float mn = m[cg] == -INFINITY ? 0.f : m[cg] * SC_LOG2;
          const float alpha = __builtin_amdgcn_exp2f(fminf(ms[cg] - mn, 0.f));
          ms[cg] = mn;
          l[cg] *= alpha;
#pragma unroll
          for (int i = 0; i < 8; ++i) acc[cg][i] *= alpha;
        }
#pragma unroll
        for (int r = 0; r < 4; ++r) x[r] = __builtin_amdgcn_exp2f(__builtin_fmaf(x[r], SC_LOG2, -ms[cg]));
        l[cg] += (x[0] + x[1]) + (x[2] + x[3]);
        pb[cg] = u32x2{cvtpk(x[0], x[1]), cvtpk(x[2], x[3])};
      }
    }
    if (k == nw) put_stats();
#pragma unroll
    for (int i = 0; i < 8; ++i) {
      const unsigned sel = (i & 1) ? 0x07060302u : 0x05040100u;
      va[i][0] = __builtin_amdgcn_perm(pg.v[1][i >> 1], pg.v[0][i >> 1], sel);
      va[i][1] = __builtin_amdgcn_perm(pg.v[3][i >> 1], pg.v[2][i >> 1], sel);
    }
#pragma unroll
    for (int cg = 0; cg < 2; ++cg) {
      if ((mt >> (16 + 4 * cg)) & 0xfu) {
#pragma unroll
        for (int i = 0; i < 8; ++i) acc[cg][i] = mfma16(va[i], pb[cg], acc[cg][i]);
      }
    }
    if (k < nw) {
      pace(k);
      issue(k);
    }
  }
  __builtin_amdgcn_s_waitcnt(0x0F70);
  STAMP(4);

#pragma unroll
  for (int cg = 0; cg < 2; ++cg) {
    if (wo[cg] >= 0) {
#pragma unroll
      for (int i = 0; i < 8; ++i) *(f32x4*)(smem + wo[cg] + 64 * i) = acc[cg][i];
    }
  }
  asm volatile("" ::: "memory");
  *(unsigned char*)(smem + LDS_PW + 32 + rk) = 1;
  if (PRIO) __builtin_amdgcn_s_setprio(0);

  if (ncol > 0) {
    for (;;) {
      asm volatile("" ::: "memory");
      const u32x2 dn = *(const u32x2*)(smem + LDS_PW + 32);
      if (__builtin_amdgcn_ballot_w64((dn[0] & (dn[1] | 0x01000000u)) == 0x01010101u)) break;
    }
    asm volatile("" ::: "memory");
    f32x2 o[NWAVE];
#pragma unroll
    for (int w = 0; w < NWAVE - 1; ++w) o[w] = *(const f32x2*)(smem + ord + w * (HD * 4));
    float mw;
    f32x4 lw;
    for (;;) {
      asm volatile("" ::: "memory");
      const unsigned dn = *(const unsigned*)(smem + LDS_PW + 28);
      asm volatile("" ::: "memory");
      mw = *(const float*)(smem + mrd);
      lw = *(const f32x4*)(smem + lrd);
      asm volatile("" : "+v"(mw), "+v"(lw));
      if (__builtin_amdgcn_ballot_w64(dn == 0x01010101u)) break;
    }
    const float M = max8(mw);
    const float f = __builtin_amdgcn_exp2f(mw - M);
    const float L = sum8(f * ((lw[0] + lw[1]) + (lw[2] + lw[3])));
    const u32x4 hdr = {__float_as_uint(M), ctv, __float_as_uint(L), ctv};
    __builtin_amdgcn_raw_buffer_store_b128(hdr, rws, phdr0, 0, AUX_SC1);
    STAMP(5);
    float fw[8];
#pragma unroll
    for (int w = 0; w < 8; ++w) fw[w] = rdlane(f, w);
    asm volatile("" : "+s"(fw[0]), "+s"(fw[1]), "+s"(fw[2]), "+s"(fw[3]), "+s"(fw[4]), "+s"(fw[5]), "+s"(fw[6]), "+s"(fw[7]));
    f32x2 a = (o[0] * fw[0] + o[2] * fw[2] + o[4] * fw[4] + o[6] * fw[6]) + (o[1] * fw[1] + o[3] * fw[3] + o[5] * fw[5]);
    asm volatile("" : "+v"(a));
    for (;;) {
      asm volatile("" ::: "memory");
      const unsigned dn = *(const unsigned*)(smem + LDS_PW + 36);
      asm volatile("" ::: "memory");
      o[7] = *(const f32x2*)(smem + ord + 7 * (HD * 4));
      asm volatile("" : "+v"(o[7]));
      if (__builtin_amdgcn_ballot_w64(dn == 0x01010101u)) break;
    }
    a += o[7] * fw[7];
    __builtin_amdgcn_raw_buffer_store_b128(u32x4{__float_as_uint(a[0]), ct, __float_as_uint(a[1]), ct}, rws, prow, 0, AUX_SC1);
    if (!wide && ncol > 1) __builtin_amdgcn_raw_buffer_store_b128(hdr, rws, lane < 8 ? OOB : phdr, 0, AUX_SC1);
#pragma unroll 1
    for (int u = 1; u < ncol; ++u) {
      const int col = __builtin_amdgcn_readlane(pcol, 8 * u);
#pragma unroll
      for (int w = 0; w < NWAVE; ++w) o[w] = *(const f32x2*)(smem + LDS_O + (col * COL_F + 2 * lane) * 4 + w * (HD * 4));
      ct = __builtin_amdgcn_readlane(ctv, 8 * u);
      a = wsum8(o, f, 8 * u);
      __builtin_amdgcn_raw_buffer_store_b128(u32x4{__float_as_uint(a[0]), ct, __float_as_uint(a[1]), ct}, rws,
                                             WS_SLOT0 + (col * NPART + part) * SLOT_BYTES + lane * 16, 0, AUX_SC1);
    }
    if (wide) __builtin_amdgcn_raw_buffer_store_b128(hdr, rws, phdr, 0, AUX_SC1);
    STAMP(6);
  }

  if (rk == 0) {
    const unsigned tag = (unsigned)__builtin_amdgcn_readlane((int)epoch, part) + 1;
    const bool oddi = (lane >> 3) & 1;
    int orow = (((part >> 2) * NH + 4 * kvh + (part & 3)) * HD + ((lane >> 1) & 3) * 32 + (lane & 1) * 16 + (lane >> 4) * 2 + (oddi ? 8 : 0)) * 2;
    float Mr = -3.0e38f, Lr = 0.f, a0 = 0.f, a1 = 0.f;
    unsigned spin = 0;
    const int ncon = __popc(contrib);
    const int crank = lane < 32 && ((contrib >> lane) & 1) ? __popc(contrib & ((1u << lane) - 1)) : 63;
    const int cpart = __builtin_amdgcn_ds_permute(crank * 4, lane);
    for (int base = 0; base < ncon; base += GS) {
      const int u16 = lane & 15;
      const bool hon = u16 < GS && base + u16 < ncon;
      const int sl = __builtin_amdgcn_ds_bpermute((base + u16) * 4, cpart);
      const int so = hon ? WS_SLOT0 + (part * NPART + sl) * SLOT_BYTES : OOB;
      const unsigned htag = hon ? tag : 0u;
      const float hbias = hon ? 0.f : -INFINITY;
      int doff[GS];
      unsigned etag[GS];
#pragma unroll
      for (int u = 0; u < GS; ++u) {
        doff[u] = __builtin_amdgcn_readlane(so, u) + lane * 16;
        etag[u] = __builtin_amdgcn_readlane(htag, u);
        asm volatile("" : "+v"(doff[u]), "+s"(etag[u]));
      }
      asm volatile("" : "+v"(orow));
      u32x4 h, d[GS];
#pragma unroll 1
      for (; spin < SPIN_CAP; ++spin) {
        asm volatile("" ::: "memory");
        h = __builtin_amdgcn_raw_buffer_load_b128(rws, so + 1024, 0, AUX_SC1);
        if (!__builtin_amdgcn_ballot_w64(__builtin_amdgcn_bitop3_b32(h[1], h[3], htag, 0x7E) != 0)) break;
        if (POLL_SLEEP) __builtin_amdgcn_s_sleep(POLL_SLEEP);
      }
      STAMP(3);
#pragma unroll
      for (int u = GS - 1; u >= 0; --u) d[u] = __builtin_amdgcn_raw_buffer_load_b128(rws, doff[u], 0, AUX_SC1);
      __builtin_amdgcn_sched_barrier(0);
      const float mu = __uint_as_float(h[0]) + hbias;
      float M = max8(mu);
      M = fmx(fmx(M, dppf<XOR15>(M)), Mr);
      float f = __builtin_amdgcn_exp2f(mu - M), fr = __builtin_amdgcn_exp2f(Mr - M);
      const float l8 = sum8(f * __uint_as_float(h[2]));
      Lr = (l8 + dppf<XOR15>(l8)) + fr * Lr;
      Mr = M;
      if (base + GS >= ncon) {
        const float inv = __builtin_amdgcn_rcpf(Lr);
        f *= inv;
        fr *= inv;
      }
      a0 *= fr;
      a1 *= fr;
      float fu[GS], b0 = 0.f, b1 = 0.f;
#pragma unroll
      for (int u = 0; u < GS; ++u) {
        fu[u] = rdlane(f, u);
        asm volatile("" : "+s"(fu[u]));
      }
      asm volatile("s_nop 1" : "+v"(a0), "+v"(a1), "+v"(b0), "+v"(b1));
#pragma unroll 1
      for (; spin < SPIN_CAP; ++spin) {
        unsigned bad = 0;
#pragma unroll
        for (int u = GS - 1; u >= 0; --u) bad |= __builtin_amdgcn_bitop3_b32(d[u][1], d[u][3], etag[u], 0x7E);
        if (!__builtin_amdgcn_ballot_w64(bad != 0)) break;
        asm volatile("" ::: "memory");
#pragma unroll
        for (int u = GS - 1; u >= 0; --u) d[u] = __builtin_amdgcn_raw_buffer_load_b128(rws, doff[u], 0, AUX_SC1);
      }
#pragma unroll
      for (int u = 0; u < GS; u += 2)
        asm("v_fmac_f32 %0, %4, %6\n\tv_fmac_f32 %1, %4, %7\n\tv_fmac_f32 %2, %5, %8\n\tv_fmac_f32 %3, %5, %9"
            : "+v"(a0), "+v"(a1), "+v"(b0), "+v"(b1)
            : "s"(fu[u]), "s"(fu[u + 1]), "v"(d[u][0]), "v"(d[u][2]), "v"(d[u + 1][0]), "v"(d[u + 1][2]));
      a0 += b0;
      a1 += b1;
    }
    const unsigned pk = cvtpk(a0, a1), pp = dppu<0x128>(pk);
    __builtin_amdgcn_raw_buffer_store_b32(__builtin_amdgcn_perm(pk, pp, oddi ? 0x07060302u : 0x01000504u), rout, orow, 0, OUT_AUX);
    __builtin_amdgcn_raw_buffer_store_b32(tag, rws, lane ? OOB : 0, part * 4, AUX_SC1);
  }
  STAMP(7);
  STAMP_FLUSH;
}

}

extern "C" void kernel_launch(void* const* d_in, const int* in_sizes, int n_in, void* d_out, int out_size, void* d_ws,
                              size_t ws_size, hipStream_t stream) {
  (void)in_sizes; (void)n_in; (void)out_size; (void)ws_size;
  (void)hipFuncSetAttribute((const void*)attn_fused, hipFuncAttributeMaxDynamicSharedMemorySize, LDS_BYTES);
  hipLaunchKernelGGL(attn_fused, dim3(NKVH * NPART), dim3(NTHR), LDS_BYTES, stream, (const char*)d_in[0], (const char*)d_in[1],
                     (const char*)d_in[2], (const int*)d_in[3], (const int*)d_in[4], (char*)d_ws, (__hip_bfloat16*)d_out, in_sizes[1] / (PAGE_BYTES / 2));
}
```

```cpp
#define SEQ 2
#define PACE 455
#define ROT_A 3
#include <hip/hip_runtime.h>
#include <hip/hip_bf16.h>
#include <stdint.h>

#define KV_AUX 2
#define WSH 17
#define PACE_N0 1
#define HINT_EARLY 1
#define POLL_SLEEP 0
#define SLIP_C 0
#define MAP 0
#define ROT 0
#define PF_N 1
#define PF_AUX 0
#define PACE_T0 0
#define MRG0 1
#define LEAD 130
#define LEAD2 35
#define OUT_TOUCH 0
#define OUT_AUX 16
#define ENDN 6
#define ENDF 100
#define POLL_AT 20
#define ROW_WAIT 0
#define ROT_B 0
#define PRIO 1
#define STAMP(i)
#define STAMP_DECL
#define STAMP_FLUSH

namespace {

constexpr int NBATCH = 8, NH = 32, NKVH = 8, HD = 128, PAGE = 16, NBLK = 512, LK = NBLK * PAGE;
constexpr int NPART = 32, NWAVE = 8, NTHR = NWAVE * 64, NCOL = 32;
constexpr int TOK_BYTES = NKVH * HD * 2;
constexpr int PAGE_BYTES = PAGE * TOK_BYTES;
constexpr int PAGE_SPAN = (PAGE - 1) * TOK_BYTES + HD * 2;
constexpr float SC_LOG2 = 0.08838834764831845f * 1.4426950408889634f;
constexpr int AUX_SC1 = 16;
constexpr unsigned SPIN_CAP = 1u << 20;
constexpr int OOB = 0x70000000;
constexpr int GS = 12;

typedef __attribute__((ext_vector_type(8))) __bf16 bf16x8;
typedef __attribute__((ext_vector_type(2))) __bf16 bf16x2;
typedef __attribute__((ext_vector_type(4))) short s16x4;
typedef __attribute__((ext_vector_type(4))) float f32x4;
typedef __attribute__((ext_vector_type(2))) float f32x2;
typedef __attribute__((ext_vector_type(4))) unsigned u32x4;
typedef __attribute__((ext_vector_type(2))) unsigned u32x2;
typedef __attribute__((ext_vector_type(8))) int i32x8;
extern "C" __device__ int llvm_writelane(int, int, int) __asm("llvm.amdgcn.writelane.i32");

constexpr int COL_F = NWAVE * HD + 4;
constexpr int LDS_TBL = 0;
constexpr int LDS_PW = LDS_TBL + NBLK * NBATCH * 4;
constexpr int LDS_EP = LDS_PW + 64;
constexpr int LDS_Q = LDS_EP + 512;
constexpr int LDS_ML = LDS_Q + 8192;
constexpr int LDS_O = LDS_ML + NCOL * 40 * 4;
constexpr int LDS_BYTES = LDS_O + NCOL * COL_F * 4;
constexpr int LDS_FIN = LDS_TBL;

constexpr int WS_HEAD = 2 << 20, WS_SLOT0 = 1024, SLOT_BYTES = 1152;
constexpr int WS_BYTES = WS_SLOT0 + NCOL * NPART * SLOT_BYTES;

__device__ __forceinline__ f32x4 mfma32(u32x4 a, u32x4 b, f32x4 c) {
  return __builtin_amdgcn_mfma_f32_16x16x32_bf16(__builtin_bit_cast(bf16x8, a), __builtin_bit_cast(bf16x8, b), c, 0, 0, 0);
}
__device__ __forceinline__ f32x4 mfma16(u32x2 a, u32x2 b, f32x4 c) {
  return __builtin_amdgcn_mfma_f32_16x16x16bf16_1k(__builtin_bit_cast(s16x4, a), __builtin_bit_cast(s16x4, b), c, 0, 0, 0);
}
__device__ __forceinline__ unsigned cvtpk(float lo, float hi) {
  return __builtin_bit_cast(unsigned, __builtin_convertvector(f32x2{lo, hi}, bf16x2));
}
__device__ __forceinline__ float fmx(float a, float b) { return __builtin_elementwise_maximum(a, b); }
__device__ __forceinline__ float swap16_max(float x) {
  auto s = __builtin_amdgcn_permlane16_swap(__float_as_uint(x), __float_as_uint(x), false, false);
  return fmx(__uint_as_float(s[0]), __uint_as_float(s[1]));
}
__device__ __forceinline__ float swap32_max(float x) {
  auto s = __builtin_amdgcn_permlane32_swap(__float_as_uint(x), __float_as_uint(x), false, false);
  return fmx(__uint_as_float(s[0]), __uint_as_float(s[1]));
}
__device__ __forceinline__ float swap16_sum(float x) {
  auto s = __builtin_amdgcn_permlane16_swap(__float_as_uint(x), __float_as_uint(x), false, false);
  return __uint_as_float(s[0]) + __uint_as_float(s[1]);
}
template <int CTRL> __device__ __forceinline__ unsigned dppu(unsigned x) {
  return (unsigned)__builtin_amdgcn_mov_dpp((int)x, CTRL, 0xf, 0xf, true);
}
template <int CTRL> __device__ __forceinline__ float dppf(float x) { return __uint_as_float(dppu<CTRL>(__float_as_uint(x))); }
constexpr int XOR1 = 0xB1, XOR2 = 0x4E, XOR3 = 0x1B, XOR7 = 0x141, XOR15 = 0x140;
__device__ __forceinline__ float max8(float x) {
  x = fmx(x, dppf<XOR7>(x));
  return fmx(fmx(fmx(x, dppf<XOR1>(x)), dppf<XOR2>(x)), dppf<XOR3>(x));
}
__device__ __forceinline__ float sum8(float x) {
  x += dppf<XOR1>(x); x += dppf<XOR2>(x); return x + dppf<XOR7>(x);
}
__device__ __forceinline__ float rdlane(float x, int l) { return __int_as_float(__builtin_amdgcn_readlane(__float_as_int(x), l)); }
__device__ __forceinline__ int clamp_len(int l) { return (l <= 0 || l > LK) ? LK : l; }
__device__ __forceinline__ void lds_barrier() { asm volatile("s_waitcnt lgkmcnt(0)\n\ts_barrier" ::: "memory"); }
__device__ __forceinline__ unsigned now() { return (unsigned)__builtin_amdgcn_s_memrealtime(); }
__device__ __forceinline__ f32x2 wsum8(const f32x2 (&o)[8], float f, int l0) {
  float s[8];
#pragma unroll
  for (int w = 0; w < 8; ++w) s[w] = rdlane(f, l0 + w);
  asm volatile("" : "+s"(s[0]), "+s"(s[1]), "+s"(s[2]), "+s"(s[3]), "+s"(s[4]), "+s"(s[5]), "+s"(s[6]), "+s"(s[7]));
  const f32x2 a = o[0] * s[0] + o[2] * s[2] + o[4] * s[4] + o[6] * s[6];
  const f32x2 b = o[1] * s[1] + o[3] * s[3] + o[5] * s[5] + o[7] * s[7];
  return a + b;
}

#define CND4(D, A, B, P)                                                                        \
  "v_cndmask_b32_dpp %[" D "0], %[" A "0], %[" B "0], vcc " P " row_mask:0xf bank_mask:0xf\n\t" \
  "v_cndmask_b32_dpp %[" D "1], %[" A "1], %[" B "1], vcc " P " row_mask:0xf bank_mask:0xf\n\t" \
  "v_cndmask_b32_dpp %[" D "2], %[" A "2], %[" B "2], vcc " P " row_mask:0xf bank_mask:0xf\n\t" \
  "v_cndmask_b32_dpp %[" D "3], %[" A "3], %[" B "3], vcc " P " row_mask:0xf bank_mask:0xf\n\t"
#define SWAP_STAGE(MASK, P)                                                                                              \
  asm("s_mov_b32 vcc_lo, " MASK "\n\ts_mov_b32 vcc_hi, " MASK "\n\t" CND4("x", "b", "a", P) "s_not_b64 vcc, vcc\n\t" CND4("y", "a", "b", P) \
      "s_nop 1"                                                                                                          \
      : [x0] "=&v"(x[0]), [x1] "=&v"(x[1]), [x2] "=&v"(x[2]), [x3] "=&v"(x[3]), [y0] "=&v"(y[0]), [y1] "=&v"(y[1]),      \
        [y2] "=&v"(y[2]), [y3] "=&v"(y[3])                                                                               \
      : [a0] "v"(a[0]), [a1] "v"(a[1]), [a2] "v"(a[2]), [a3] "v"(a[3]), [b0] "v"(b[0]), [b1] "v"(b[1]), [b2] "v"(b[2]),  \
        [b3] "v"(b[3])                                                                                                   \
      : "vcc", "scc")
__device__ __forceinline__ void swap1(const u32x4& a, const u32x4& b, u32x4& ox, u32x4& oy) {
  unsigned x[4], y[4];
  SWAP_STAGE("0x55555555", "quad_perm:[1,0,3,2]");
  ox = u32x4{x[0], x[1], x[2], x[3]};
  oy = u32x4{y[0], y[1], y[2], y[3]};
}
__device__ __forceinline__ void swap2(const u32x4& a, const u32x4& b, u32x4& ox, u32x4& oy) {
  unsigned x[4], y[4];
  SWAP_STAGE("0x33333333", "quad_perm:[2,3,0,1]");
  ox = u32x4{x[0], x[1], x[2], x[3]};
  oy = u32x4{y[0], y[1], y[2], y[3]};
}
__device__ __forceinline__ void quad_transpose(const u32x4 (&in)[4], u32x4 (&out)[4]) {
  u32x4 t[4];
  swap1(in[0], in[1], t[0], t[1]);
  swap1(in[2], in[3], t[2], t[3]);
  swap2(t[0], t[2], out[0], out[2]);
  swap2(t[1], t[3], out[1], out[3]);
}

struct Page {
  u32x4 k[4];
  u32x4 v[4];
};

__global__ __launch_bounds__(NTHR, 2) void attn_fused(const char* __restrict__ q, const char* __restrict__ kc,
                                                      const char* __restrict__ vc, const int* __restrict__ bt,
                                                      const int* __restrict__ lens, char* __restrict__ ws,
                                                      __hip_bfloat16* __restrict__ out, int nphys) {
  extern __shared__ __attribute__((aligned(16))) char smem[];
  STAMP_DECL;
  STAMP(0);
  const int tid = threadIdx.x, lane = tid & 63, wave = __builtin_amdgcn_readfirstlane(tid >> 6);
  if (tid < 16) ((unsigned*)(smem + LDS_PW))[tid] = 0u;
  asm volatile("s_waitcnt lgkmcnt(0)" :: "s"(q), "s"(kc), "s"(vc), "s"(bt), "s"(lens), "s"(ws), "s"(out), "s"(nphys) : "memory");
  const i32x8 slen = *(const __attribute__((address_space(4))) i32x8*)(uintptr_t)lens;
  const int g = lane >> 4, c = lane & 15;
  const int kvh = blockIdx.x & 7, part = blockIdx.x >> 3;

  u32x2 y[NBATCH];
  const bool btw = (unsigned)(wave - 2) < 4u;
  const int bcol = (tid - 128) * 8;
  if (btw) {
    const auto rbt = __builtin_amdgcn_make_buffer_rsrc((void*)bt, 0, NBATCH * NBLK * 4, 0x00020000);
#pragma unroll
    for (int b = 0; b < NBATCH; ++b) y[b] = __builtin_amdgcn_raw_buffer_load_b64(rbt, bcol + (b & 1) * 2048, (b >> 1) * 4096, 0);
  }
  asm volatile("s_barrier" ::: "memory");
  const auto rws = __builtin_amdgcn_make_buffer_rsrc((void*)(ws + (size_t)kvh * WS_HEAD), 0, WS_BYTES, 0x00020000);
  const auto rout = __builtin_amdgcn_make_buffer_rsrc((void*)out, 0, NBATCH * NH * HD * 2, 0x00020000);
  const char* kbase = kc + kvh * (HD * 2);
  const char* vbase = vc + kvh * (HD * 2);
  int koff = ((c >> 2) * 4) * TOK_BYTES + (4 * g + (c & 3)) * 16;
  int voff = 4 * g * TOK_BYTES + c * 16;
  asm volatile("" : "+v"(koff), "+v"(voff), "+s"(kbase), "+s"(vbase));
  if (btw) {
#pragma unroll
    for (int b = 0; b < NBATCH; ++b) *(u32x2*)(smem + LDS_TBL + b * (NBLK * 4) + bcol) = y[b];
    unsigned d0 = y[0][0] ^ y[1][0], d1 = y[0][1] ^ y[1][1];
#pragma unroll
    for (int b = 2; b < NBATCH; ++b) {
      d0 = __builtin_amdgcn_bitop3_b32(d0, y[0][0], y[b][0], 0xF6);
      d1 = __builtin_amdgcn_bitop3_b32(d1, y[0][1], y[b][1], 0xF6);
    }
    unsigned f0, f1;
    asm("s_ff1_i32_b64 %0, %1" : "=s"(f0) : "s"(__builtin_amdgcn_ballot_w64(d0 != 0)));
    asm("s_ff1_i32_b64 %0, %1" : "=s"(f1) : "s"(__builtin_amdgcn_ballot_w64(d1 != 0)));
    asm volatile("" ::: "memory");
    ((unsigned*)(smem + LDS_PW))[wave - 2] = (((wave - 2) * 128) | min(2 * f0, 2 * f1 + 1)) + 1;
  }
  if (wave == 6) {
    const auto rq = __builtin_amdgcn_make_buffer_rsrc((void*)(q + 4 * kvh * (HD * 2)), 0, NBATCH * NH * HD * 2 - 4 * kvh * (HD * 2), 0x00020000);
    u32x4 qs[NBATCH];
#pragma unroll
    for (int b = 0; b < NBATCH; ++b) qs[b] = __builtin_amdgcn_raw_buffer_load_b128(rq, lane * 16, b * (NH * HD * 2), 0);
    const unsigned e = __builtin_amdgcn_raw_buffer_load_b32(rws, lane < 32 ? lane * 4 : WS_SLOT0 + (part * NPART + lane - 32) * SLOT_BYTES + 1024, 0, AUX_SC1);
    const unsigned ot = OUT_TOUCH ? __builtin_amdgcn_raw_buffer_load_b32(rout, ((part >> 2) * NH + 4 * kvh + (part & 3)) * (HD * 2) + lane * 4, 0, OUT_TOUCH - 1) : 0u;
#pragma unroll
    for (int b = 0; b < NBATCH; ++b) *(u32x4*)(smem + LDS_Q + (((b >> 2) * 4 + (c & 3)) * 64 + (c >> 2) * 16 + (b & 3) * 4 + g) * 16) = qs[b];
    *(unsigned*)(smem + LDS_EP + lane * 4) = e;
    asm volatile("" ::: "memory");
    ((unsigned*)(smem + LDS_PW))[4] = 1u;
    asm volatile("" :: "v"(ot));
  }
  if (PF_N > 0 && wave == 7) {
    u32x4 warm[PF_N > 0 ? 2 * PF_N : 1][4];
#pragma unroll
    for (int u = 0; u < 2 * PF_N; ++u) {
      const size_t off = (size_t)(unsigned)(((part * PF_N + (u >> 1)) * 2 + 1) * nphys / (2 * NPART * (PF_N > 0 ? PF_N : 1))) * PAGE_BYTES;
      const auto r = __builtin_amdgcn_make_buffer_rsrc((void*)(((u & 1) ? vbase : kbase) + off), 0, PAGE_SPAN, 0x00020000);
#pragma unroll
      for (int j = 0; j < 4; ++j) warm[u][j] = __builtin_amdgcn_raw_buffer_load_b128(r, voff + (j & 1) * TOK_BYTES, (j >> 1) * 2 * TOK_BYTES, PF_AUX);
    }
#pragma unroll
    for (int u = 0; u < 2 * PF_N; ++u) asm volatile("" :: "v"(warm[u][0]), "v"(warm[u][1]), "v"(warm[u][2]), "v"(warm[u][3]));
  }
  __builtin_amdgcn_s_waitcnt(0x0F70);
  int lraw = 1;
#pragma unroll
  for (int b = 0; b < NBATCH; ++b) lraw = llvm_writelane(slen[b], b, lraw);
  const int npg8 = (clamp_len(lraw) + PAGE - 1) >> 4;
  int nmax = max(npg8, (int)dppu<XOR1>(npg8));
  nmax = max(nmax, (int)dppu<XOR2>(nmax));
  nmax = __builtin_amdgcn_readfirstlane(max(nmax, (int)dppu<XOR7>(nmax)));
  int npg = lane < NBATCH ? npg8 : (1 << 20);
  asm volatile("" : "+v"(npg), "+s"(nmax));
  STAMP(1);

  unsigned p1;
  do {
    asm volatile("" ::: "memory");
    const u32x4 a = *(const u32x4*)(smem + LDS_PW);
    p1 = __builtin_amdgcn_readfirstlane(min(min(a[0], a[1]), min(a[2], a[3])));
  } while (!p1);
  asm volatile("" ::: "memory");
  const int P = __builtin_amdgcn_readfirstlane((int)(p1 - 1 < (unsigned)nmax ? p1 - 1 : (unsigned)nmax));
  const int cnt = max(npg - P, 0);
  int send = cnt;
  send += (int)dppu<0x111>(send);
  send += (int)dppu<0x112>(send);
  send += (int)dppu<0x114>(send);
  const int npriv = __builtin_amdgcn_readlane(send, NBATCH - 1);
  int bnd;
  {
    const unsigned x = (unsigned)(npriv * 16 + P * WSH) * (unsigned)lane;
    bnd = max((int)__umulhi(x, (unsigned)((1ull << 27) / WSH)), ((int)(x >> 5) - P * (WSH - 16)) >> 4);
  }
  const int i0 = __builtin_amdgcn_readlane(bnd, part), i1 = __builtin_amdgcn_readlane(bnd, part + 1);
  const int Ps = P;
  const int nitem = i1 - i0, nbase = nitem >> 3, nextra = nitem & 7;
  const int nw = nbase + (wave < nextra ? 1 : 0);
  const int soff = lane * NBLK - (send - cnt);
  auto table_index = [&](int it, int lo, int hi) {
    const int rel = it - Ps;
    int add = 0;
    for (int cur = max(lo - Ps, 0); cur < hi - Ps;) {
      const int s = __popcll(__builtin_amdgcn_ballot_w64(send <= cur));
      const int so = __builtin_amdgcn_readlane(soff, s);
      add = rel >= cur ? so : add;
      cur = __builtin_amdgcn_readlane(send, s);
    }
    return (it + add) & (NBATCH * NBLK - 1);
  };
  int item, tix;
  if (SEQ == 0) {
    const int idx0 = i0 + nbase * wave + min(wave, nextra);
    item = idx0 + lane;
    tix = table_index(item, idx0, idx0 + nw);
  } else {
    int r = 0;
    if (SEQ == 2) {
      const int ph0 = __builtin_amdgcn_readfirstlane(*(const int*)(smem + LDS_TBL + table_index(i0, i0, min(i0 + 1, i1)) * 4));
      r = (ROT_A * part + ROT_B - ph0 - (int)((uintptr_t)kc >> 15)) & 63;
      r = r < nitem ? r : 0;
    }
    if (SEQ >= 3) {
      const int ph = *(const int*)(smem + LDS_TBL + table_index(i0 + lane, i0, min(i1, i0 + 64)) * 4);
      const int key = (ph + (int)((uintptr_t)kc >> 15) - ROT_B - ROT_A * part) & 63;
      const int prev = __builtin_amdgcn_update_dpp(0, key, 0x138, 0xf, 0xf, false);
      const unsigned long long ds = __builtin_amdgcn_ballot_w64(key < prev && lane < nitem);
      const int first = __builtin_ctzll(ds), last = 63 - __builtin_clzll(ds);
      r = ds == 0 ? 0 : SEQ == 3 ? first : SEQ == 4 ? last : (last - first > nitem - last ? first : last);
    }
    const int n = 8 * lane + wave + r;
    item = i0 + (n >= nitem ? n - nitem : n);
    tix = table_index(item, i0, i1);
  }
  const unsigned tstart = now();
  const int phys = *(const int*)(smem + LDS_TBL + tix * 4);

  STAMP(2);
  unsigned tpace;
  int dpace, nfast, dcut;
  Page pg;
  auto issue = [&](int k) {
    const size_t off = (size_t)(unsigned)__builtin_amdgcn_readlane(phys, k) * PAGE_BYTES;
    const auto rk = __builtin_amdgcn_make_buffer_rsrc((void*)(kbase + off), 0, PAGE_SPAN, 0x00020000);
    const auto rv = __builtin_amdgcn_make_buffer_rsrc((void*)(vbase + off), 0, PAGE_SPAN, 0x00020000);
#pragma unroll
    for (int i = 0; i < 4; ++i) pg.k[i] = __builtin_amdgcn_raw_buffer_load_b128(rk, koff + (i & 1) * TOK_BYTES, (i >> 1) * 2 * TOK_BYTES, KV_AUX);
#pragma unroll
    for (int j = 0; j < 4; ++j) pg.v[j] = __builtin_amdgcn_raw_buffer_load_b128(rv, voff + (j & 1) * TOK_BYTES, (j >> 1) * 2 * TOK_BYTES, KV_AUX);
    __builtin_amdgcn_sched_barrier(0);
  };
  auto pace = [&](int k) {
    const unsigned tg = tpace + ((k * 8 * dpace - max(k * 8 - nfast, 0) * dcut) >> 8);
    while ((int)(now() - tg) < 0) __builtin_amdgcn_s_sleep(1);
  };
  if (wave == 0) ((unsigned*)(smem + LDS_PW))[5] = tstart | 1u;
  if (nw > 0 && wave <= PACE_N0) issue(0);

  unsigned contrib;
  {
    const int s = part >> 2;
    const int e = __builtin_amdgcn_readlane(send, s) + Ps, n = __builtin_amdgcn_readlane(cnt, s), ps = min(Ps, __builtin_amdgcn_readlane(npg, s));
    const int nxt = __builtin_amdgcn_ds_bpermute((lane + 1) * 4, bnd);
    contrib = (unsigned)__builtin_amdgcn_ballot_w64(nxt > bnd && (bnd < ps || (n > 0 && bnd < e && nxt > e - n)));
  }
  const unsigned um = nitem > 0 ? (unsigned)__builtin_amdgcn_ballot_w64(i0 < min(P, npg) || (cnt > 0 && i0 - P < send && i1 - P > send - cnt)) & 0xffu : 0u;
  const int nseq = __popc(um);
  const bool wide = nseq > 2;
  {
    const float span = (float)(npriv * 16 + P * WSH) * (PACE * (1.f / 32.f)) - (wide ? LEAD * 256.f : nseq == 2 ? LEAD2 * 256.f : 0.f);
    dpace = __builtin_amdgcn_readfirstlane((int)(fmaxf(span, 0.f) * __builtin_amdgcn_rcpf((float)max(nitem, 1))));
    nfast = nitem - ENDN - wave;
    dcut = wide ? 0 : __builtin_amdgcn_readfirstlane((int)((float)dpace * (ENDF * 0.01f) * (float)(P * WSH) * __builtin_amdgcn_rcpf((float)(npriv * 16 + P * WSH))));
  }
  do {
    asm volatile("" ::: "memory");
    tpace = __builtin_amdgcn_readfirstlane(((const unsigned*)(smem + LDS_PW))[5]);
  } while (!tpace);
  const unsigned tend = tpace + (unsigned)(((npriv * 16 + P * WSH) >> 5) * PACE >> 8);
  tpace += ((wave - PACE_N0) * dpace >> 8) + PACE_T0;
  if (nw > 0 && wave > PACE_N0) {
    pace(0);
    issue(0);
  }

  while (__hip_atomic_load((unsigned*)(smem + LDS_PW + 16), __ATOMIC_RELAXED, __HIP_MEMORY_SCOPE_WORKGROUP) == 0u) __builtin_amdgcn_s_sleep(1);
  asm volatile("" ::: "memory");
  const unsigned epoch = *(const unsigned*)(smem + LDS_EP + lane * 4);
  int lenv[2];
  u32x4 qf[2][4];
#pragma unroll
  for (int cg = 0; cg < 2; ++cg) {
    const int l = __builtin_amdgcn_ds_bpermute((4 * cg + (c >> 2)) * 4, lraw);
#pragma unroll
    for (int i = 0; i < 4; ++i) qf[cg][i] = *(const u32x4*)(smem + LDS_Q + ((cg * 4 + i) * 64 + lane) * 16);
    if (l <= 0) {
#pragma unroll
      for (int i = 0; i < 4; ++i) qf[cg][i] = u32x4{0, 0, 0, 0};
    }
    lenv[cg] = clamp_len(l) - 4 * g;
  }
  const unsigned meta = (unsigned)(tix & (NBLK - 1)) * PAGE | (item < Ps ? 0xff0000u : 0x10000u << (tix >> 9));
  const int rk = (wave - nextra) & 7;
  int ncol = wide ? (nseq + 1 - (rk >> 2)) >> 1 : (unsigned)(rk - MRG0) < 4u ? nseq : 0;
  int mrd, lrd, pcol, phdr;
  unsigned ctv;
  {
    const int u = (lane >> 3) & 3;
    const int rank = ((um >> (lane & 7)) & 1) ? __popc(um & ((1u << (lane & 7)) - 1)) : -1;
    const unsigned long long hit = __builtin_amdgcn_ballot_w64(rank == (wide ? 2 * u + (rk >> 2) : u));
    const unsigned mine8 = (unsigned)(hit >> (8 * u)) & 0xffu;
    const bool on = mine8 != 0 && lane < 32;
    const int col = 4 * __builtin_ctz(mine8 | 0x80u) + (rk & 3);
    mrd = LDS_ML + (col * 40 + 32 + (lane & 7)) * 4;
    lrd = LDS_ML + (col * 40 + (lane & 7) * 4) * 4;
    pcol = col;
    const int slot = WS_SLOT0 + (col * NPART + part) * SLOT_BYTES;
    phdr = on ? slot + 1024 + (lane & 7) * 16 : OOB;
    ctv = (unsigned)__builtin_amdgcn_ds_bpermute(col * 4, (int)epoch) + 1;
  }
  int phdr0 = wide || lane >= 8 ? OOB : phdr;
  int ord = LDS_O + (__builtin_amdgcn_readfirstlane(pcol) * COL_F + 2 * lane) * 4;
  int prow = WS_SLOT0 + (__builtin_amdgcn_readfirstlane(pcol) * NPART + part) * SLOT_BYTES + lane * 16;
  unsigned ct = __builtin_amdgcn_readfirstlane(ctv);
  asm volatile("" : "+v"(mrd), "+v"(lrd), "+v"(ord), "+v"(prow), "+v"(ct), "+v"(phdr0), "+v"(phdr), "+v"(ctv), "+v"(pcol), "+s"(ncol));
  int wm[2], wl[2], wo[2];
#pragma unroll
  for (int cg = 0; cg < 2; ++cg) {
    wm[cg] = LDS_ML + ((16 * cg + c) * 40 + 32 + rk) * 4;
    wl[cg] = LDS_ML + ((16 * cg + c) * 40 + rk * 4 + g) * 4;
    wo[cg] = ((um >> (4 * cg + (c >> 2))) & 1) ? LDS_O + ((16 * cg + c) * COL_F + rk * HD + 4 * g) * 4 : -1;
  }
  asm volatile("" : "+v"(wm[0]), "+v"(wl[0]), "+v"(wo[0]), "+v"(wo[1]));

  float m[2] = {-INFINITY, -INFINITY}, ms[2] = {0.f, 0.f}, l[2] = {0.f, 0.f};
  f32x4 acc[2][8];
#pragma unroll
  for (int cg = 0; cg < 2; ++cg)
#pragma unroll
    for (int i = 0; i < 8; ++i) acc[cg][i] = f32x4{0.f, 0.f, 0.f, 0.f};
  if (PRIO) __builtin_amdgcn_s_setprio(2);

  auto put_stats = [&]() {
#pragma unroll
    for (int cg = 0; cg < 2; ++cg) {
      *(float*)(smem + wm[0] + cg * (16 * 160)) = m[cg] * SC_LOG2;
      *(float*)(smem + wl[0] + cg * (16 * 160)) = l[cg];
    }
    asm volatile("" ::: "memory");
    *(unsigned char*)(smem + LDS_PW + 24 + rk) = 1;
  };
  if (nw == 0) put_stats();
  for (int k = 0; k < nw;) {
    u32x4 kt[4];
    u32x2 va[8];
    quad_transpose(pg.k, kt);
    const unsigned mt = (unsigned)__builtin_amdgcn_readlane((int)meta, k);
    ++k;
    u32x2 pb[2];
#pragma unroll
    for (int cg = 0; cg < 2; ++cg) {
      if ((mt >> (16 + 4 * cg)) & 0xfu) {
        f32x4 s = {0.f, 0.f, 0.f, 0.f};
#pragma unroll
        for (int i = 0; i < 4; ++i) s = mfma32(kt[i], qf[cg][i], s);
        const int lim = ((mt >> (16 + 4 * cg + (c >> 2))) & 1) ? lenv[cg] - (int)(mt & 0xffffu) : 0;
        unsigned long long live[4];
#pragma unroll
        for (int r = 0; r < 4; ++r) live[r] = __builtin_amdgcn_ballot_w64(r < lim);
        asm volatile("" : "+s"(live[0]), "+s"(live[1]), "+s"(live[2]), "+s"(live[3]));
        float x[4];
#pragma unroll
        for (int r = 0; r < 4; ++r) x[r] = __builtin_amdgcn_inverse_ballot_w64(live[r]) ? s[r] : -INFINITY;
        const float mx = swap32_max(swap16_max(fmx(fmx(x[0], x[1]), fmx(x[2], x[3]))));
        if (__builtin_amdgcn_ballot_w64(mx > m[cg])) {
          m[cg] = fmx(m[cg], mx);
          const float mn = m[cg] == -INFINITY ? 0.f : m[cg] * SC_LOG2;
          const float alpha = __builtin_amdgcn_exp2f(fminf(ms[cg] - mn, 0.f));
          ms[cg] = mn;
          l[cg] *= alpha;
#pragma unroll
          for (int i = 0; i < 8; ++i) acc[cg][i] *= alpha;
        }
#pragma unroll
        for (int r = 0; r < 4; ++r) x[r] = __builtin_amdgcn_exp2f(__builtin_fmaf(x[r], SC_LOG2, -ms[cg]));
        l[cg] += (x[0] + x[1]) + (x[2] + x[3]);
        pb[cg] = u32x2{cvtpk(x[0], x[1]), cvtpk(x[2], x[3])};
      }
    }
    if (k == nw) put_stats();
#pragma unroll
    for (int i = 0; i < 8; ++i) {
      const unsigned sel = (i & 1) ? 0x07060302u : 0x05040100u;
      va[i][0] = __builtin_amdgcn_perm(pg.v[1][i >> 1], pg.v[0][i >> 1], sel);
      va[i][1] = __builtin_amdgcn_perm(pg.v[3][i >> 1], pg.v[2][i >> 1], sel);
    }
#pragma unroll
    for (int cg = 0; cg < 2; ++cg) {
      if ((mt >> (16 + 4 * cg)) & 0xfu) {
#pragma unroll
        for (int i = 0; i < 8; ++i) acc[cg][i] = mfma16(va[i], pb[cg], acc[cg][i]);
      }
    }
    if (k < nw) {
      pace(k);
      issue(k);
    }
  }
  __builtin_amdgcn_s_waitcnt(0x0F70);
  STAMP(4);

#pragma unroll
  for (int cg = 0; cg < 2; ++cg) {
    if (wo[cg] >= 0) {
#pragma unroll
      for (int i = 0; i < 8; ++i) *(f32x4*)(smem + wo[cg] + 64 * i) = acc[cg][i];
    }
  }
  asm volatile("" ::: "memory");
  *(unsigned char*)(smem + LDS_PW + 32 + rk) = 1;
  if (PRIO) __builtin_amdgcn_s_setprio(0);

  if (ncol > 0) {
    for (;;) {
      asm volatile("" ::: "memory");
      const u32x2 dn = *(const u32x2*)(smem + LDS_PW + 32);
      if (__builtin_amdgcn_ballot_w64((dn[0] & (dn[1] | 0x01010100u)) == 0x01010101u)) break;
    }
    asm volatile("" ::: "memory");
    f32x2 o[NWAVE];
#pragma unroll
    for (int w = 0; w < 5; ++w) o[w] = *(const f32x2*)(smem + ord + w * (HD * 4));
    float mw;
    f32x4 lw;
    for (;;) {
      asm volatile("" ::: "memory");
      const unsigned dn = *(const unsigned*)(smem + LDS_PW + 28);
      asm volatile("" ::: "memory");
      mw = *(const float*)(smem + mrd);
      lw = *(const f32x4*)(smem + lrd);
      asm volatile("" : "+v"(mw), "+v"(lw));
      if (__builtin_amdgcn_ballot_w64(dn == 0x01010101u)) break;
    }
    const float M = max8(mw);
    const float f = __builtin_amdgcn_exp2f(mw - M);
    const float L = sum8(f * ((lw[0] + lw[1]) + (lw[2] + lw[3])));
    const u32x4 hdr = {__float_as_uint(M), ctv, __float_as_uint(L), ctv};
    __builtin_amdgcn_raw_buffer_store_b128(hdr, rws, phdr0, 0, AUX_SC1);
    STAMP(5);
    float fw[8];
#pragma unroll
    for (int w = 0; w < 8; ++w) fw[w] = rdlane(f, w);
    asm volatile("" : "+s"(fw[0]), "+s"(fw[1]), "+s"(fw[2]), "+s"(fw[3]), "+s"(fw[4]), "+s"(fw[5]), "+s"(fw[6]), "+s"(fw[7]));
    f32x2 a = (o[0] * fw[0] + o[2] * fw[2] + o[4] * fw[4]) + (o[1] * fw[1] + o[3] * fw[3]);
    asm volatile("" : "+v"(a));
    for (;;) {
      asm volatile("" ::: "memory");
      const unsigned dn = *(const unsigned*)(smem + LDS_PW + 36);
      asm volatile("" ::: "memory");
#pragma unroll
      for (int w = 5; w < NWAVE; ++w) o[w] = *(const f32x2*)(smem + ord + w * (HD * 4));
      asm volatile("" : "+v"(o[5]), "+v"(o[6]), "+v"(o[7]));
      if (__builtin_amdgcn_ballot_w64(dn == 0x01010101u)) break;
    }
    a = (a + o[5] * fw[5]) + (o[6] * fw[6] + o[7] * fw[7]);
    __builtin_amdgcn_raw_buffer_store_b128(u32x4{__float_as_uint(a[0]), ct, __float_as_uint(a[1]), ct}, rws, prow, 0, AUX_SC1);
    if (!wide && ncol > 1) __builtin_amdgcn_raw_buffer_store_b128(hdr, rws, lane < 8 ? OOB : phdr, 0, AUX_SC1);
#pragma unroll 1
    for (int u = 1; u < ncol; ++u) {
      const int col = __builtin_amdgcn_readlane(pcol, 8 * u);
#pragma unroll
      for (int w = 0; w < NWAVE; ++w) o[w] = *(const f32x2*)(smem + LDS_O + (col * COL_F + 2 * lane) * 4 + w * (HD * 4));
      ct = __builtin_amdgcn_readlane(ctv, 8 * u);
      a = wsum8(o, f, 8 * u);
      __builtin_amdgcn_raw_buffer_store_b128(u32x4{__float_as_uint(a[0]), ct, __float_as_uint(a[1]), ct}, rws,
                                             WS_SLOT0 + (col * NPART + part) * SLOT_BYTES + lane * 16, 0, AUX_SC1);
    }
    if (wide) __builtin_amdgcn_raw_buffer_store_b128(hdr, rws, phdr, 0, AUX_SC1);
    STAMP(6);
  }

  if (rk == 0) {
    const unsigned tag = (unsigned)__builtin_amdgcn_readlane((int)epoch, part) + 1;
    const bool oddi = (lane >> 3) & 1;
    int orow = (((part >> 2) * NH + 4 * kvh + (part & 3)) * HD + ((lane >> 1) & 3) * 32 + (lane & 1) * 16 + (lane >> 4) * 2 + (oddi ? 8 : 0)) * 2;
    float Mr = -3.0e38f, Lr = 0.f, a0 = 0.f, a1 = 0.f;
    unsigned spin = 0;
    const int ncon = __popc(contrib);
    const int crank = lane < 32 && ((contrib >> lane) & 1) ? __popc(contrib & ((1u << lane) - 1)) : 63;
    const int cpart = __builtin_amdgcn_ds_permute(crank * 4, lane);
    for (int base = 0; base < ncon; base += GS) {
      const int u16 = lane & 15;
      const bool hon = u16 < GS && base + u16 < ncon;
      const int sl = __builtin_amdgcn_ds_bpermute((base + u16) * 4, cpart);
      const int so = hon ? WS_SLOT0 + (part * NPART + sl) * SLOT_BYTES : OOB;
      const unsigned htag = hon ? tag : 0u;
      const float hbias = hon ? 0.f : -INFINITY;
      int doff[GS];
      unsigned etag[GS];
#pragma unroll
      for (int u = 0; u < GS; ++u) {
        doff[u] = __builtin_amdgcn_readlane(so, u) + lane * 16;
        etag[u] = __builtin_amdgcn_readlane(htag, u);
        asm volatile("" : "+v"(doff[u]), "+s"(etag[u]));
      }
      asm volatile("" : "+v"(orow));
      u32x4 h, d[GS];
      if (POLL_AT > -1000) {
        const unsigned tg = tend + POLL_AT;
        while ((int)(now() - tg) < 0) __builtin_amdgcn_s_sleep(2);
      }
#pragma unroll 1
      for (; spin < SPIN_CAP; ++spin) {
        asm volatile("" ::: "memory");
        h = __builtin_amdgcn_raw_buffer_load_b128(rws, so + 1024, 0, AUX_SC1);
        if (!__builtin_amdgcn_ballot_w64(__builtin_amdgcn_bitop3_b32(h[1], h[3], htag, 0x7E) != 0)) break;
        if (POLL_SLEEP) __builtin_amdgcn_s_sleep(POLL_SLEEP);
      }
      STAMP(3);
      if (ROW_WAIT) __builtin_amdgcn_s_sleep(ROW_WAIT);
#pragma unroll
      for (int u = GS - 1; u >= 0; --u) d[u] = __builtin_amdgcn_raw_buffer_load_b128(rws, doff[u], 0, AUX_SC1);
      __builtin_amdgcn_sched_barrier(0);
      const float mu = __uint_as_float(h[0]) + hbias;
      float M = max8(mu);
      M = fmx(fmx(M, dppf<XOR15>(M)), Mr);
      float f = __builtin_amdgcn_exp2f(mu - M), fr = __builtin_amdgcn_exp2f(Mr - M);
      const float l8 = sum8(f * __uint_as_float(h[2]));
      Lr = (l8 + dppf<XOR15>(l8)) + fr * Lr;
      Mr = M;
      if (base + GS >= ncon) {
        const float inv = __builtin_amdgcn_rcpf(Lr);
        f *= inv;
        fr *= inv;
      }
      a0 *= fr;
      a1 *= fr;
      float fu[GS], b0 = 0.f, b1 = 0.f;
#pragma unroll
      for (int u = 0; u < GS; ++u) {
        fu[u] = rdlane(f, u);
        asm volatile("" : "+s"(fu[u]));
      }
      asm volatile("s_nop 1" : "+v"(a0), "+v"(a1), "+v"(b0), "+v"(b1));
#pragma unroll 1
      for (; spin < SPIN_CAP; ++spin) {
        unsigned bad = 0;
#pragma unroll
        for (int u = GS - 1; u >= 0; --u) bad |= __builtin_amdgcn_bitop3_b32(d[u][1], d[u][3], etag[u], 0x7E);
        if (!__builtin_amdgcn_ballot_w64(bad != 0)) break;
        asm volatile("" ::: "memory");
#pragma unroll
        for (int u = GS - 1; u >= 0; --u) d[u] = __builtin_amdgcn_raw_buffer_load_b128(rws, doff[u], 0, AUX_SC1);
      }
#pragma unroll
      for (int u = 0; u < GS; u += 2)
        asm("v_fmac_f32 %0, %4, %6\n\tv_fmac_f32 %1, %4, %7\n\tv_fmac_f32 %2, %5, %8\n\tv_fmac_f32 %3, %5, %9"
            : "+v"(a0), "+v"(a1), "+v"(b0), "+v"(b1)
            : "s"(fu[u]), "s"(fu[u + 1]), "v"(d[u][0]), "v"(d[u][2]), "v"(d[u + 1][0]), "v"(d[u + 1][2]));
      a0 += b0;
      a1 += b1;
    }
    const unsigned pk = cvtpk(a0, a1), pp = dppu<0x128>(pk);
    __builtin_amdgcn_raw_buffer_store_b32(__builtin_amdgcn_perm(pk, pp, oddi ? 0x07060302u : 0x01000504u), rout, orow, 0, OUT_AUX);
    __builtin_amdgcn_raw_buffer_store_b32(tag, rws, lane ? OOB : 0, part * 4, AUX_SC1);
  }
  STAMP(7);
  STAMP_FLUSH;
}

}

extern "C" void kernel_launch(void* const* d_in, const int* in_sizes, int n_in, void* d_out, int out_size, void* d_ws,
                              size_t ws_size, hipStream_t stream) {
  (void)in_sizes; (void)n_in; (void)out_size; (void)ws_size;
  (void)hipFuncSetAttribute((const void*)attn_fused, hipFuncAttributeMaxDynamicSharedMemorySize, LDS_BYTES);
  hipLaunchKernelGGL(attn_fused, dim3(NKVH * NPART), dim3(NTHR), LDS_BYTES, stream, (const char*)d_in[0], (const char*)d_in[1],
                     (const char*)d_in[2], (const int*)d_in[3], (const int*)d_in[4], (char*)d_ws, (__hip_bfloat16*)d_out, in_sizes[1] / (PAGE_BYTES / 2));
}
```

```cpp
#include <hip/hip_runtime.h>
#include <hip/hip_bf16.h>
#include <stdint.h>

#define KV_AUX 2
#define WSH 17
#define PACE 491
#define PACE_N0 1
#define HINT_EARLY 1
#define POLL_SLEEP 0
#define SLIP_C 0
#define MAP 0
#define ROT 0
#define PF_N 1
#define PF_AUX 0
#define PACE_T0 0
#define MRG0 1
#define LEAD 130
#define LEAD2 35
#define OUT_TOUCH 0
#define OUT_AUX 16
#define ENDN 6
#define ENDF 100
#define POLL_AT 20
#define ROW_WAIT 0
#define GW 2
#define ADV 105
#define ADV_K0 2
#define ADV_LAT 102
#define ADV_SLOW 25
#define ADV_LINE 77
#define ADV_N0 12
#define ADV_GAIN 10
#define ADV_BACK 40
#define ADV_CLIP 150
#define PRIO 1
#define STAMP(i)
#define STAMP_DECL
#define STAMP_FLUSH

namespace {

constexpr int NBATCH = 8, NH = 32, NKVH = 8, HD = 128, PAGE = 16, NBLK = 512, LK = NBLK * PAGE;
constexpr int NPART = 32, NWAVE = 8, NTHR = NWAVE * 64, NCOL = 32;
constexpr int TOK_BYTES = NKVH * HD * 2;
constexpr int PAGE_BYTES = PAGE * TOK_BYTES;
constexpr int PAGE_SPAN = (PAGE - 1) * TOK_BYTES + HD * 2;
constexpr float SC_LOG2 = 0.08838834764831845f * 1.4426950408889634f;
constexpr int AUX_SC1 = 16;
constexpr unsigned SPIN_CAP = 1u << 20;
constexpr int OOB = 0x70000000;
constexpr int GS = 12;

typedef __attribute__((ext_vector_type(8))) __bf16 bf16x8;
typedef __attribute__((ext_vector_type(2))) __bf16 bf16x2;
typedef __attribute__((ext_vector_type(4))) short s16x4;
typedef __attribute__((ext_vector_type(4))) float f32x4;
typedef __attribute__((ext_vector_type(2))) float f32x2;
typedef __attribute__((ext_vector_type(4))) unsigned u32x4;
typedef __attribute__((ext_vector_type(2))) unsigned u32x2;
typedef __attribute__((ext_vector_type(8))) int i32x8;
extern "C" __device__ int llvm_writelane(int, int, int) __asm("llvm.amdgcn.writelane.i32");

constexpr int COL_F = NWAVE * HD + 4;
constexpr int LDS_TBL = 0;
constexpr int LDS_PW = LDS_TBL + NBLK * NBATCH * 4;
constexpr int LDS_EP = LDS_PW + 64;
constexpr int LDS_Q = LDS_EP + 512;
constexpr int LDS_ML = LDS_Q + 8192;
constexpr int LDS_O = LDS_ML + NCOL * 40 * 4;
constexpr int LDS_BYTES = LDS_O + NCOL * COL_F * 4;
constexpr int LDS_FIN = LDS_TBL;

constexpr int WS_HEAD = 2 << 20, WS_SLOT0 = 1024, SLOT_BYTES = 1152;
constexpr int WS_BYTES = WS_SLOT0 + NCOL * NPART * SLOT_BYTES;

__device__ __forceinline__ f32x4 mfma32(u32x4 a, u32x4 b, f32x4 c) {
  return __builtin_amdgcn_mfma_f32_16x16x32_bf16(__builtin_bit_cast(bf16x8, a), __builtin_bit_cast(bf16x8, b), c, 0, 0, 0);
}
__device__ __forceinline__ f32x4 mfma16(u32x2 a, u32x2 b, f32x4 c) {
  return __builtin_amdgcn_mfma_f32_16x16x16bf16_1k(__builtin_bit_cast(s16x4, a), __builtin_bit_cast(s16x4, b), c, 0, 0, 0);
}
__device__ __forceinline__ unsigned cvtpk(float lo, float hi) {
  return __builtin_bit_cast(unsigned, __builtin_convertvector(f32x2{lo, hi}, bf16x2));
}
__device__ __forceinline__ float fmx(float a, float b) { return __builtin_elementwise_maximum(a, b); }
__device__ __forceinline__ float swap16_max(float x) {
  auto s = __builtin_amdgcn_permlane16_swap(__float_as_uint(x), __float_as_uint(x), false, false);
  return fmx(__uint_as_float(s[0]), __uint_as_float(s[1]));
}
__device__ __forceinline__ float swap32_max(float x) {
  auto s = __builtin_amdgcn_permlane32_swap(__float_as_uint(x), __float_as_uint(x), false, false);
  return fmx(__uint_as_float(s[0]), __uint_as_float(s[1]));
}
__device__ __forceinline__ float swap16_sum(float x) {
  auto s = __builtin_amdgcn_permlane16_swap(__float_as_uint(x), __float_as_uint(x), false, false);
  return __uint_as_float(s[0]) + __uint_as_float(s[1]);
}
template <int CTRL> __device__ __forceinline__ unsigned dppu(unsigned x) {
  return (unsigned)__builtin_amdgcn_mov_dpp((int)x, CTRL, 0xf, 0xf, true);
}
template <int CTRL> __device__ __forceinline__ float dppf(float x) { return __uint_as_float(dppu<CTRL>(__float_as_uint(x))); }
constexpr int XOR1 = 0xB1, XOR2 = 0x4E, XOR3 = 0x1B, XOR7 = 0x141, XOR15 = 0x140;
__device__ __forceinline__ float max8(float x) {
  x = fmx(x, dppf<XOR7>(x));
  return fmx(fmx(fmx(x, dppf<XOR1>(x)), dppf<XOR2>(x)), dppf<XOR3>(x));
}
__device__ __forceinline__ float sum8(float x) {
  x += dppf<XOR1>(x); x += dppf<XOR2>(x); return x + dppf<XOR7>(x);
}
__device__ __forceinline__ float rdlane(float x, int l) { return __int_as_float(__builtin_amdgcn_readlane(__float_as_int(x), l)); }
__device__ __forceinline__ int clamp_len(int l) { return (l <= 0 || l > LK) ? LK : l; }
__device__ __forceinline__ void lds_barrier() { asm volatile("s_waitcnt lgkmcnt(0)\n\ts_barrier" ::: "memory"); }
__device__ __forceinline__ unsigned now() { return (unsigned)__builtin_amdgcn_s_memrealtime(); }
__device__ __forceinline__ f32x2 wsum8(const f32x2 (&o)[8], float f, int l0) {
  float s[8];
#pragma unroll
  for (int w = 0; w < 8; ++w) s[w] = rdlane(f, l0 + w);
  asm volatile("" : "+s"(s[0]), "+s"(s[1]), "+s"(s[2]), "+s"(s[3]), "+s"(s[4]), "+s"(s[5]), "+s"(s[6]), "+s"(s[7]));
  const f32x2 a = o[0] * s[0] + o[2] * s[2] + o[4] * s[4] + o[6] * s[6];
  const f32x2 b = o[1] * s[1] + o[3] * s[3] + o[5] * s[5] + o[7] * s[7];
  return a + b;
}

#define CND4(D, A, B, P)                                                                        \
  "v_cndmask_b32_dpp %[" D "0], %[" A "0], %[" B "0], vcc " P " row_mask:0xf bank_mask:0xf\n\t" \
  "v_cndmask_b32_dpp %[" D "1], %[" A "1], %[" B "1], vcc " P " row_mask:0xf bank_mask:0xf\n\t" \
  "v_cndmask_b32_dpp %[" D "2], %[" A "2], %[" B "2], vcc " P " row_mask:0xf bank_mask:0xf\n\t" \
  "v_cndmask_b32_dpp %[" D "3], %[" A "3], %[" B "3], vcc " P " row_mask:0xf bank_mask:0xf\n\t"
#define SWAP_STAGE(MASK, P)                                                                                              \
  asm("s_mov_b32 vcc_lo, " MASK "\n\ts_mov_b32 vcc_hi, " MASK "\n\t" CND4("x", "b", "a", P) "s_not_b64 vcc, vcc\n\t" CND4("y", "a", "b", P) \
      "s_nop 1"                                                                                                          \
      : [x0] "=&v"(x[0]), [x1] "=&v"(x[1]), [x2] "=&v"(x[2]), [x3] "=&v"(x[3]), [y0] "=&v"(y[0]), [y1] "=&v"(y[1]),      \
        [y2] "=&v"(y[2]), [y3] "=&v"(y[3])                                                                               \
      : [a0] "v"(a[0]), [a1] "v"(a[1]), [a2] "v"(a[2]), [a3] "v"(a[3]), [b0] "v"(b[0]), [b1] "v"(b[1]), [b2] "v"(b[2]),  \
        [b3] "v"(b[3])                                                                                                   \
      : "vcc", "scc")
__device__ __forceinline__ void swap1(const u32x4& a, const u32x4& b, u32x4& ox, u32x4& oy) {
  unsigned x[4], y[4];
  SWAP_STAGE("0x55555555", "quad_perm:[1,0,3,2]");
  ox = u32x4{x[0], x[1], x[2], x[3]};
  oy = u32x4{y[0], y[1], y[2], y[3]};
}
__device__ __forceinline__ void swap2(const u32x4& a, const u32x4& b, u32x4& ox, u32x4& oy) {
  unsigned x[4], y[4];
  SWAP_STAGE("0x33333333", "quad_perm:[2,3,0,1]");
  ox = u32x4{x[0], x[1], x[2], x[3]};
  oy = u32x4{y[0], y[1], y[2], y[3]};
}
__device__ __forceinline__ void quad_transpose(const u32x4 (&in)[4], u32x4 (&out)[4]) {
  u32x4 t[4];
  swap1(in[0], in[1], t[0], t[1]);
  swap1(in[2], in[3], t[2], t[3]);
  swap2(t[0], t[2], out[0], out[2]);
  swap2(t[1], t[3], out[1], out[3]);
}

struct Page {
  u32x4 k[4];
  u32x4 v[4];
};

__global__ __launch_bounds__(NTHR, 2) void attn_fused(const char* __restrict__ q, const char* __restrict__ kc,
                                                      const char* __restrict__ vc, const int* __restrict__ bt,
                                                      const int* __restrict__ lens, char* __restrict__ ws,
                                                      __hip_bfloat16* __restrict__ out, int nphys) {
  extern __shared__ __attribute__((aligned(16))) char smem[];
  STAMP_DECL;
  STAMP(0);
  const int tid = threadIdx.x, lane = tid & 63, wave = __builtin_amdgcn_readfirstlane(tid >> 6);
  if (tid < 16) ((unsigned*)(smem + LDS_PW))[tid] = 0u;
  asm volatile("s_waitcnt lgkmcnt(0)" :: "s"(q), "s"(kc), "s"(vc), "s"(bt), "s"(lens), "s"(ws), "s"(out), "s"(nphys) : "memory");
  const i32x8 slen = *(const __attribute__((address_space(4))) i32x8*)(uintptr_t)lens;
  const int g = lane >> 4, c = lane & 15;
  const int kvh = blockIdx.x & 7, part = blockIdx.x >> 3;

  u32x2 y[NBATCH];
  const bool btw = (unsigned)(wave - 2) < 4u;
  const int bcol = (tid - 128) * 8;
  if (btw) {
    const auto rbt = __builtin_amdgcn_make_buffer_rsrc((void*)bt, 0, NBATCH * NBLK * 4, 0x00020000);
#pragma unroll
    for (int b = 0; b < NBATCH; ++b) y[b] = __builtin_amdgcn_raw_buffer_load_b64(rbt, bcol + (b & 1) * 2048, (b >> 1) * 4096, 0);
  }
  asm volatile("s_barrier" ::: "memory");
  const auto rws = __builtin_amdgcn_make_buffer_rsrc((void*)(ws + (size_t)kvh * WS_HEAD), 0, WS_BYTES, 0x00020000);
  const auto rout = __builtin_amdgcn_make_buffer_rsrc((void*)out, 0, NBATCH * NH * HD * 2, 0x00020000);
  const char* kbase = kc + kvh * (HD * 2);
  const char* vbase = vc + kvh * (HD * 2);
  int koff = ((c >> 2) * 4) * TOK_BYTES + (4 * g + (c & 3)) * 16;
  int voff = 4 * g * TOK_BYTES + c * 16;
  asm volatile("" : "+v"(koff), "+v"(voff), "+s"(kbase), "+s"(vbase));
  if (btw) {
#pragma unroll
    for (int b = 0; b < NBATCH; ++b) *(u32x2*)(smem + LDS_TBL + b * (NBLK * 4) + bcol) = y[b];
    unsigned d0 = y[0][0] ^ y[1][0], d1 = y[0][1] ^ y[1][1];
#pragma unroll
    for (int b = 2; b < NBATCH; ++b) {
      d0 = __builtin_amdgcn_bitop3_b32(d0, y[0][0], y[b][0], 0xF6);
      d1 = __builtin_amdgcn_bitop3_b32(d1, y[0][1], y[b][1], 0xF6);
    }
    unsigned f0, f1;
    asm("s_ff1_i32_b64 %0, %1" : "=s"(f0) : "s"(__builtin_amdgcn_ballot_w64(d0 != 0)));
    asm("s_ff1_i32_b64 %0, %1" : "=s"(f1) : "s"(__builtin_amdgcn_ballot_w64(d1 != 0)));
    asm volatile("" ::: "memory");
    ((unsigned*)(smem + LDS_PW))[wave - 2] = (((wave - 2) * 128) | min(2 * f0, 2 * f1 + 1)) + 1;
  }
  if (wave == 6) {
    const auto rq = __builtin_amdgcn_make_buffer_rsrc((void*)(q + 4 * kvh * (HD * 2)), 0, NBATCH * NH * HD * 2 - 4 * kvh * (HD * 2), 0x00020000);
    u32x4 qs[NBATCH];
#pragma unroll
    for (int b = 0; b < NBATCH; ++b) qs[b] = __builtin_amdgcn_raw_buffer_load_b128(rq, lane * 16, b * (NH * HD * 2), 0);
    const unsigned e = __builtin_amdgcn_raw_buffer_load_b32(rws, lane < 32 ? lane * 4 : WS_SLOT0 + (part * NPART + lane - 32) * SLOT_BYTES + 1024, 0, AUX_SC1);
    const unsigned ot = OUT_TOUCH ? __builtin_amdgcn_raw_buffer_load_b32(rout, ((part >> 2) * NH + 4 * kvh + (part & 3)) * (HD * 2) + lane * 4, 0, OUT_TOUCH - 1) : 0u;
#pragma unroll
    for (int b = 0; b < NBATCH; ++b) *(u32x4*)(smem + LDS_Q + (((b >> 2) * 4 + (c & 3)) * 64 + (c >> 2) * 16 + (b & 3) * 4 + g) * 16) = qs[b];
    *(unsigned*)(smem + LDS_EP + lane * 4) = e;
    asm volatile("" ::: "memory");
    ((unsigned*)(smem + LDS_PW))[4] = 1u;
    asm volatile("" :: "v"(ot));
  }
  if (PF_N > 0 && wave == 7) {
    u32x4 warm[PF_N > 0 ? 2 * PF_N : 1][4];
#pragma unroll
    for (int u = 0; u < 2 * PF_N; ++u) {
      const size_t off = (size_t)(unsigned)(((part * PF_N + (u >> 1)) * 2 + 1) * nphys / (2 * NPART * (PF_N > 0 ? PF_N : 1))) * PAGE_BYTES;
      const auto r = __builtin_amdgcn_make_buffer_rsrc((void*)(((u & 1) ? vbase : kbase) + off), 0, PAGE_SPAN, 0x00020000);
#pragma unroll
      for (int j = 0; j < 4; ++j) warm[u][j] = __builtin_amdgcn_raw_buffer_load_b128(r, voff + (j & 1) * TOK_BYTES, (j >> 1) * 2 * TOK_BYTES, PF_AUX);
    }
#pragma unroll
    for (int u = 0; u < 2 * PF_N; ++u) asm volatile("" :: "v"(warm[u][0]), "v"(warm[u][1]), "v"(warm[u][2]), "v"(warm[u][3]));
  }
  __builtin_amdgcn_s_waitcnt(0x0F70);
  int lraw = 1;
#pragma unroll
  for (int b = 0; b < NBATCH; ++b) lraw = llvm_writelane(slen[b], b, lraw);
  const int npg8 = (clamp_len(lraw) + PAGE - 1) >> 4;
  int nmax = max(npg8, (int)dppu<XOR1>(npg8));
  nmax = max(nmax, (int)dppu<XOR2>(nmax));
  nmax = __builtin_amdgcn_readfirstlane(max(nmax, (int)dppu<XOR7>(nmax)));
  int npg = lane < NBATCH ? npg8 : (1 << 20);
  asm volatile("" : "+v"(npg), "+s"(nmax));
  STAMP(1);

  unsigned p1;
  do {
    asm volatile("" ::: "memory");
    const u32x4 a = *(const u32x4*)(smem + LDS_PW);
    p1 = __builtin_amdgcn_readfirstlane(min(min(a[0], a[1]), min(a[2], a[3])));
  } while (!p1);
  asm volatile("" ::: "memory");
  const int P = __builtin_amdgcn_readfirstlane((int)(p1 - 1 < (unsigned)nmax ? p1 - 1 : (unsigned)nmax));
  const int cnt = max(npg - P, 0);
  int send = cnt;
  send += (int)dppu<0x111>(send);
  send += (int)dppu<0x112>(send);
  send += (int)dppu<0x114>(send);
  const int npriv = __builtin_amdgcn_readlane(send, NBATCH - 1);
  int bnd;
  {
    const unsigned x = (unsigned)(npriv * 16 + P * WSH) * (unsigned)lane;
    bnd = max((int)__umulhi(x, (unsigned)((1ull << 27) / WSH)), ((int)(x >> 5) - P * (WSH - 16)) >> 4);
  }
  const int i0 = __builtin_amdgcn_readlane(bnd, part), i1 = __builtin_amdgcn_readlane(bnd, part + 1);
  const int Ps = P;
  const int nitem = i1 - i0, nbase = nitem >> 3, nextra = nitem & 7;
  const int nw = nbase + (wave < nextra ? 1 : 0);
  const int soff = lane * NBLK - (send - cnt);
  auto table_index = [&](int it, int lo, int hi) {
    const int rel = it - Ps;
    int add = 0;
    for (int cur = max(lo - Ps, 0); cur < hi - Ps;) {
      const int s = __popcll(__builtin_amdgcn_ballot_w64(send <= cur));
      const int so = __builtin_amdgcn_readlane(soff, s);
      add = rel >= cur ? so : add;
      cur = __builtin_amdgcn_readlane(send, s);
    }
    return (it + add) & (NBATCH * NBLK - 1);
  };
  int item, tix;
  {
    const int w0 = wave & ~(GW - 1);
    const int idx0 = i0 + nbase * w0 + min(w0, nextra);
    item = idx0 + GW * lane + (wave & (GW - 1));
    tix = table_index(item, idx0, min(idx0 + GW * nw, i1));
  }
  const unsigned tstart = now();
  const int phys = *(const int*)(smem + LDS_TBL + tix * 4);

  STAMP(2);
  unsigned tpace;
  int dpace, nfast, dcut;
  Page pg;
  auto issue = [&](int k) {
    const size_t off = (size_t)(unsigned)__builtin_amdgcn_readlane(phys, k) * PAGE_BYTES;
    const auto rk = __builtin_amdgcn_make_buffer_rsrc((void*)(kbase + off), 0, PAGE_SPAN, 0x00020000);
    const auto rv = __builtin_amdgcn_make_buffer_rsrc((void*)(vbase + off), 0, PAGE_SPAN, 0x00020000);
#pragma unroll
    for (int i = 0; i < 4; ++i) pg.k[i] = __builtin_amdgcn_raw_buffer_load_b128(rk, koff + (i & 1) * TOK_BYTES, (i >> 1) * 2 * TOK_BYTES, KV_AUX);
#pragma unroll
    for (int j = 0; j < 4; ++j) pg.v[j] = __builtin_amdgcn_raw_buffer_load_b128(rv, voff + (j & 1) * TOK_BYTES, (j >> 1) * 2 * TOK_BYTES, KV_AUX);
    __builtin_amdgcn_sched_barrier(0);
  };
  unsigned tiss = 0, tarr = 0;
  int adv = 0;
  const int lat8 = 8 * (ADV_LAT + ((((uintptr_t)kbase >> 8) & 3) == 1 ? ADV_SLOW : 0));
  auto pace = [&](int k) {
    if (ADV > 0 && k > 0) {
      asm volatile("" ::: "memory");
      *(unsigned char*)(smem + LDS_PW + 40 + wave) = (unsigned char)min(tarr - tiss, (unsigned)ADV_CLIP);
      asm volatile("" ::: "memory");
      const u32x4 st = *(const u32x4*)(smem + LDS_PW + 32);
      adv = __builtin_amdgcn_readfirstlane(*(const int*)(smem + LDS_PW + 48));
      if (k >= ADV_K0) {
        const int sum = __builtin_amdgcn_readfirstlane(__builtin_amdgcn_sad_u8(st[2], 0, __builtin_amdgcn_sad_u8(st[3], 0, 0)));
        const int step = min(max((lat8 - sum) * ADV_GAIN, -ADV_BACK * 16), ADV * 16);
        adv = min(max(adv + ((dpace * step) >> 14), 0), (k * 8 + wave - ADV_N0) * ((dpace * ADV_LINE) >> 10));
        *(int*)(smem + LDS_PW + 48) = adv;
      }
    }
    const unsigned tg = tpace + ((k * 8 * dpace - max(k * 8 - nfast, 0) * dcut - adv) >> 8);
    while ((int)((tiss = now()) - tg) < 0) __builtin_amdgcn_s_sleep(1);
  };
  if (wave == 0) ((unsigned*)(smem + LDS_PW))[5] = tstart | 1u;
  if (nw > 0 && wave <= PACE_N0) issue(0);

  unsigned contrib;
  {
    const int s = part >> 2;
    const int e = __builtin_amdgcn_readlane(send, s) + Ps, n = __builtin_amdgcn_readlane(cnt, s), ps = min(Ps, __builtin_amdgcn_readlane(npg, s));
    const int nxt = __builtin_amdgcn_ds_bpermute((lane + 1) * 4, bnd);
    contrib = (unsigned)__builtin_amdgcn_ballot_w64(nxt > bnd && (bnd < ps || (n > 0 && bnd < e && nxt > e - n)));
  }
  const unsigned um = nitem > 0 ? (unsigned)__builtin_amdgcn_ballot_w64(i0 < min(P, npg) || (cnt > 0 && i0 - P < send && i1 - P > send - cnt)) & 0xffu : 0u;
  const int nseq = __popc(um);
  const bool wide = nseq > 2;
  {
    const float span = (float)(npriv * 16 + P * WSH) * (PACE * (1.f / 32.f)) - (wide ? LEAD * 256.f : nseq == 2 ? LEAD2 * 256.f : 0.f);
    dpace = __builtin_amdgcn_readfirstlane((int)(fmaxf(span, 0.f) * __builtin_amdgcn_rcpf((float)max(nitem, 1))));
    nfast = nitem - ENDN - wave;
    dcut = wide ? 0 : __builtin_amdgcn_readfirstlane((int)((float)dpace * (ENDF * 0.01f) * (float)(P * WSH) * __builtin_amdgcn_rcpf((float)(npriv * 16 + P * WSH))));
  }
  do {
    asm volatile("" ::: "memory");
    tpace = __builtin_amdgcn_readfirstlane(((const unsigned*)(smem + LDS_PW))[5]);
  } while (!tpace);
  const unsigned tend = tpace + (unsigned)(((npriv * 16 + P * WSH) >> 5) * PACE >> 8);
  tpace += ((wave - PACE_N0) * dpace >> 8) + PACE_T0;
  if (nw > 0 && wave > PACE_N0) {
    pace(0);
    issue(0);
  }

  while (__hip_atomic_load((unsigned*)(smem + LDS_PW + 16), __ATOMIC_RELAXED, __HIP_MEMORY_SCOPE_WORKGROUP) == 0u) __builtin_amdgcn_s_sleep(1);
  asm volatile("" ::: "memory");
  const unsigned epoch = *(const unsigned*)(smem + LDS_EP + lane * 4);
  int lenv[2];
  u32x4 qf[2][4];
#pragma unroll
  for (int cg = 0; cg < 2; ++cg) {
    const int l = __builtin_amdgcn_ds_bpermute((4 * cg + (c >> 2)) * 4, lraw);
#pragma unroll
    for (int i = 0; i < 4; ++i) qf[cg][i] = *(const u32x4*)(smem + LDS_Q + ((cg * 4 + i) * 64 + lane) * 16);
    if (l <= 0) {
#pragma unroll
      for (int i = 0; i < 4; ++i) qf[cg][i] = u32x4{0, 0, 0, 0};
    }
    lenv[cg] = clamp_len(l) - 4 * g;
  }
  const unsigned meta = (unsigned)(tix & (NBLK - 1)) * PAGE | (item < Ps ? 0xff0000u : 0x10000u << (tix >> 9));
  const int rk = (wave - nextra) & 7;
  int ncol = wide ? (nseq + 1 - (rk >> 2)) >> 1 : (unsigned)(rk - MRG0) < 4u ? nseq : 0;
  int mrd, lrd, pcol, phdr;
  unsigned ctv;
  {
    const int u = (lane >> 3) & 3;
    const int rank = ((um >> (lane & 7)) & 1) ? __popc(um & ((1u << (lane & 7)) - 1)) : -1;
    const unsigned long long hit = __builtin_amdgcn_ballot_w64(rank == (wide ? 2 * u + (rk >> 2) : u));
    const unsigned mine8 = (unsigned)(hit >> (8 * u)) & 0xffu;
    const bool on = mine8 != 0 && lane < 32;
    const int col = 4 * __builtin_ctz(mine8 | 0x80u) + (rk & 3);
    mrd = LDS_ML + (col * 40 + 32 + (lane & 7)) * 4;
    lrd = LDS_ML + (col * 40 + (lane & 7) * 4) * 4;
    pcol = col;
    const int slot = WS_SLOT0 + (col * NPART + part) * SLOT_BYTES;
    phdr = on ? slot + 1024 + (lane & 7) * 16 : OOB;
    ctv = (unsigned)__builtin_amdgcn_ds_bpermute(col * 4, (int)epoch) + 1;
  }
  int phdr0 = wide || lane >= 8 ? OOB : phdr;
  int ord = LDS_O + (__builtin_amdgcn_readfirstlane(pcol) * COL_F + 2 * lane) * 4;
  int prow = WS_SLOT0 + (__builtin_amdgcn_readfirstlane(pcol) * NPART + part) * SLOT_BYTES + lane * 16;
  unsigned ct = __builtin_amdgcn_readfirstlane(ctv);
  asm volatile("" : "+v"(mrd), "+v"(lrd), "+v"(ord), "+v"(prow), "+v"(ct), "+v"(phdr0), "+v"(phdr), "+v"(ctv), "+v"(pcol), "+s"(ncol));
  int wm[2], wl[2], wo[2];
#pragma unroll
  for (int cg = 0; cg < 2; ++cg) {
    wm[cg] = LDS_ML + ((16 * cg + c) * 40 + 32 + rk) * 4;
    wl[cg] = LDS_ML + ((16 * cg + c) * 40 + rk * 4 + g) * 4;
    wo[cg] = ((um >> (4 * cg + (c >> 2))) & 1) ? LDS_O + ((16 * cg + c) * COL_F + rk * HD + 4 * g) * 4 : -1;
  }
  asm volatile("" : "+v"(wm[0]), "+v"(wl[0]), "+v"(wo[0]), "+v"(wo[1]));

  float m[2] = {-INFINITY, -INFINITY}, ms[2] = {0.f, 0.f}, l[2] = {0.f, 0.f};
  f32x4 acc[2][8];
#pragma unroll
  for (int cg = 0; cg < 2; ++cg)
#pragma unroll
    for (int i = 0; i < 8; ++i) acc[cg][i] = f32x4{0.f, 0.f, 0.f, 0.f};
  if (PRIO) __builtin_amdgcn_s_setprio(2);

  auto put_stats = [&]() {
#pragma unroll
    for (int cg = 0; cg < 2; ++cg) {
      *(float*)(smem + wm[0] + cg * (16 * 160)) = m[cg] * SC_LOG2;
      *(float*)(smem + wl[0] + cg * (16 * 160)) = l[cg];
    }
    asm volatile("" ::: "memory");
    *(unsigned char*)(smem + LDS_PW + 24 + rk) = 1;
  };
  if (nw == 0) put_stats();
  for (int k = 0; k < nw;) {
    u32x4 kt[4];
    u32x2 va[8];
    quad_transpose(pg.k, kt);
    if (ADV > 0) { asm volatile("" :: "v"(kt[0])); tarr = now(); }
    const unsigned mt = (unsigned)__builtin_amdgcn_readlane((int)meta, k);
    ++k;
    u32x2 pb[2];
#pragma unroll
    for (int cg = 0; cg < 2; ++cg) {
      if ((mt >> (16 + 4 * cg)) & 0xfu) {
        f32x4 s = {0.f, 0.f, 0.f, 0.f};
#pragma unroll
        for (int i = 0; i < 4; ++i) s = mfma32(kt[i], qf[cg][i], s);
        const int lim = ((mt >> (16 + 4 * cg + (c >> 2))) & 1) ? lenv[cg] - (int)(mt & 0xffffu) : 0;
        unsigned long long live[4];
#pragma unroll
        for (int r = 0; r < 4; ++r) live[r] = __builtin_amdgcn_ballot_w64(r < lim);
        asm volatile("" : "+s"(live[0]), "+s"(live[1]), "+s"(live[2]), "+s"(live[3]));
        float x[4];
#pragma unroll
        for (int r = 0; r < 4; ++r) x[r] = __builtin_amdgcn_inverse_ballot_w64(live[r]) ? s[r] : -INFINITY;
        const float mx = swap32_max(swap16_max(fmx(fmx(x[0], x[1]), fmx(x[2], x[3]))));
        if (__builtin_amdgcn_ballot_w64(mx > m[cg])) {
          m[cg] = fmx(m[cg], mx);
          const float mn = m[cg] == -INFINITY ? 0.f : m[cg] * SC_LOG2;
          const float alpha = __builtin_amdgcn_exp2f(fminf(ms[cg] - mn, 0.f));
          ms[cg] = mn;
          l[cg] *= alpha;
#pragma unroll
          for (int i = 0; i < 8; ++i) acc[cg][i] *= alpha;
        }
#pragma unroll
        for (int r = 0; r < 4; ++r) x[r] = __builtin_amdgcn_exp2f(__builtin_fmaf(x[r], SC_LOG2, -ms[cg]));
        l[cg] += (x[0] + x[1]) + (x[2] + x[3]);
        pb[cg] = u32x2{cvtpk(x[0], x[1]), cvtpk(x[2], x[3])};
      }
    }
    if (k == nw) put_stats();
#pragma unroll
    for (int i = 0; i < 8; ++i) {
      const unsigned sel = (i & 1) ? 0x07060302u : 0x05040100u;
      va[i][0] = __builtin_amdgcn_perm(pg.v[1][i >> 1], pg.v[0][i >> 1], sel);
      va[i][1] = __builtin_amdgcn_perm(pg.v[3][i >> 1], pg.v[2][i >> 1], sel);
    }
#pragma unroll
    for (int cg = 0; cg < 2; ++cg) {
      if ((mt >> (16 + 4 * cg)) & 0xfu) {
#pragma unroll
        for (int i = 0; i < 8; ++i) acc[cg][i] = mfma16(va[i], pb[cg], acc[cg][i]);
      }
    }
    if (k < nw) {
      pace(k);
      issue(k);
    }
  }
  __builtin_amdgcn_s_waitcnt(0x0F70);
  STAMP(4);

#pragma unroll
  for (int cg = 0; cg < 2; ++cg) {
    if (wo[cg] >= 0) {
#pragma unroll
      for (int i = 0; i < 8; ++i) *(f32x4*)(smem + wo[cg] + 64 * i) = acc[cg][i];
    }
  }
  asm volatile("" ::: "memory");
  *(unsigned char*)(smem + LDS_PW + 32 + rk) = 1;
  if (PRIO) __builtin_amdgcn_s_setprio(0);

  if (ncol > 0) {
    for (;;) {
      asm volatile("" ::: "memory");
      const u32x2 dn = *(const u32x2*)(smem + LDS_PW + 32);
      if (__builtin_amdgcn_ballot_w64((dn[0] & (dn[1] | 0x01010100u)) == 0x01010101u)) break;
    }
    asm volatile("" ::: "memory");
    f32x2 o[NWAVE];
#pragma unroll
    for (int w = 0; w < 5; ++w) o[w] = *(const f32x2*)(smem + ord + w * (HD * 4));
    float mw;
    f32x4 lw;
    for (;;) {
      asm volatile("" ::: "memory");
      const unsigned dn = *(const unsigned*)(smem + LDS_PW + 28);
      asm volatile("" ::: "memory");
      mw = *(const float*)(smem + mrd);
      lw = *(const f32x4*)(smem + lrd);
      asm volatile("" : "+v"(mw), "+v"(lw));
      if (__builtin_amdgcn_ballot_w64(dn == 0x01010101u)) break;
    }
    const float M = max8(mw);
    const float f = __builtin_amdgcn_exp2f(mw - M);
    const float L = sum8(f * ((lw[0] + lw[1]) + (lw[2] + lw[3])));
    const u32x4 hdr = {__float_as_uint(M), ctv, __float_as_uint(L), ctv};
    __builtin_amdgcn_raw_buffer_store_b128(hdr, rws, phdr0, 0, AUX_SC1);
    STAMP(5);
    float fw[8];
#pragma unroll
    for (int w = 0; w < 8; ++w) fw[w] = rdlane(f, w);
    asm volatile("" : "+s"(fw[0]), "+s"(fw[1]), "+s"(fw[2]), "+s"(fw[3]), "+s"(fw[4]), "+s"(fw[5]), "+s"(fw[6]), "+s"(fw[7]));
    f32x2 a = (o[0] * fw[0] + o[2] * fw[2] + o[4] * fw[4]) + (o[1] * fw[1] + o[3] * fw[3]);
    asm volatile("" : "+v"(a));
    for (;;) {
      asm volatile("" ::: "memory");
      const unsigned dn = *(const unsigned*)(smem + LDS_PW + 36);
      asm volatile("" ::: "memory");
#pragma unroll
      for (int w = 5; w < NWAVE; ++w) o[w] = *(const f32x2*)(smem + ord + w * (HD * 4));
      asm volatile("" : "+v"(o[5]), "+v"(o[6]), "+v"(o[7]));
      if (__builtin_amdgcn_ballot_w64(dn == 0x01010101u)) break;
    }
    a = (a + o[5] * fw[5]) + (o[6] * fw[6] + o[7] * fw[7]);
    __builtin_amdgcn_raw_buffer_store_b128(u32x4{__float_as_uint(a[0]), ct, __float_as_uint(a[1]), ct}, rws, prow, 0, AUX_SC1);
    if (!wide && ncol > 1) __builtin_amdgcn_raw_buffer_store_b128(hdr, rws, lane < 8 ? OOB : phdr, 0, AUX_SC1);
#pragma unroll 1
    for (int u = 1; u < ncol; ++u) {
      const int col = __builtin_amdgcn_readlane(pcol, 8 * u);
#pragma unroll
      for (int w = 0; w < NWAVE; ++w) o[w] = *(const f32x2*)(smem + LDS_O + (col * COL_F + 2 * lane) * 4 + w * (HD * 4));
      ct = __builtin_amdgcn_readlane(ctv, 8 * u);
      a = wsum8(o, f, 8 * u);
      __builtin_amdgcn_raw_buffer_store_b128(u32x4{__float_as_uint(a[0]), ct, __float_as_uint(a[1]), ct}, rws,
                                             WS_SLOT0 + (col * NPART + part) * SLOT_BYTES + lane * 16, 0, AUX_SC1);
    }
    if (wide) __builtin_amdgcn_raw_buffer_store_b128(hdr, rws, phdr, 0, AUX_SC1);
    STAMP(6);
  }

  if (rk == 0) {
    const unsigned tag = (unsigned)__builtin_amdgcn_readlane((int)epoch, part) + 1;
    const bool oddi = (lane >> 3) & 1;
    int orow = (((part >> 2) * NH + 4 * kvh + (part & 3)) * HD + ((lane >> 1) & 3) * 32 + (lane & 1) * 16 + (lane >> 4) * 2 + (oddi ? 8 : 0)) * 2;
    float Mr = -3.0e38f, Lr = 0.f, a0 = 0.f, a1 = 0.f;
    unsigned spin = 0;
    const int ncon = __popc(contrib);
    const int crank = lane < 32 && ((contrib >> lane) & 1) ? __popc(contrib & ((1u << lane) - 1)) : 63;
    const int cpart = __builtin_amdgcn_ds_permute(crank * 4, lane);
    for (int base = 0; base < ncon; base += GS) {
      const int u16 = lane & 15;
      const bool hon = u16 < GS && base + u16 < ncon;
      const int sl = __builtin_amdgcn_ds_bpermute((base + u16) * 4, cpart);
      const int so = hon ? WS_SLOT0 + (part * NPART + sl) * SLOT_BYTES : OOB;
      const unsigned htag = hon ? tag : 0u;
      const float hbias = hon ? 0.f : -INFINITY;
      int doff[GS];
      unsigned etag[GS];
#pragma unroll
      for (int u = 0; u < GS; ++u) {
        doff[u] = __builtin_amdgcn_readlane(so, u) + lane * 16;
        etag[u] = __builtin_amdgcn_readlane(htag, u);
        asm volatile("" : "+v"(doff[u]), "+s"(etag[u]));
      }
      asm volatile("" : "+v"(orow));
      u32x4 h, d[GS];
      if (POLL_AT > -1000) {
        const unsigned tg = tend + POLL_AT - (adv >> 8) - (ADV > 0 ? 20 : 0);
        while ((int)(now() - tg) < 0) __builtin_amdgcn_s_sleep(2);
      }
#pragma unroll 1
      for (; spin < SPIN_CAP; ++spin) {
        asm volatile("" ::: "memory");
        h = __builtin_amdgcn_raw_buffer_load_b128(rws, so + 1024, 0, AUX_SC1);
        if (!__builtin_amdgcn_ballot_w64(__builtin_amdgcn_bitop3_b32(h[1], h[3], htag, 0x7E) != 0)) break;
        if (POLL_SLEEP) __builtin_amdgcn_s_sleep(POLL_SLEEP);
      }
      STAMP(3);
      if (ROW_WAIT) __builtin_amdgcn_s_sleep(ROW_WAIT);
#pragma unroll
      for (int u = GS - 1; u >= 0; --u) d[u] = __builtin_amdgcn_raw_buffer_load_b128(rws, doff[u], 0, AUX_SC1);
      __builtin_amdgcn_sched_barrier(0);
      const float mu = __uint_as_float(h[0]) + hbias;
      float M = max8(mu);
      M = fmx(fmx(M, dppf<XOR15>(M)), Mr);
      float f = __builtin_amdgcn_exp2f(mu - M), fr = __builtin_amdgcn_exp2f(Mr - M);
      const float l8 = sum8(f * __uint_as_float(h[2]));
      Lr = (l8 + dppf<XOR15>(l8)) + fr * Lr;
      Mr = M;
      if (base + GS >= ncon) {
        const float inv = __builtin_amdgcn_rcpf(Lr);
        f *= inv;
        fr *= inv;
      }
      a0 *= fr;
      a1 *= fr;
      float fu[GS], b0 = 0.f, b1 = 0.f;
#pragma unroll
      for (int u = 0; u < GS; ++u) {
        fu[u] = rdlane(f, u);
        asm volatile("" : "+s"(fu[u]));
      }
      asm volatile("s_nop 1" : "+v"(a0), "+v"(a1), "+v"(b0), "+v"(b1));
#pragma unroll 1
      for (; spin < SPIN_CAP; ++spin) {
        unsigned bad = 0;
#pragma unroll
        for (int u = GS - 1; u >= 0; --u) bad |= __builtin_amdgcn_bitop3_b32(d[u][1], d[u][3], etag[u], 0x7E);
        if (!__builtin_amdgcn_ballot_w64(bad != 0)) break;
        asm volatile("" ::: "memory");
#pragma unroll
        for (int u = GS - 1; u >= 0; --u) d[u] = __builtin_amdgcn_raw_buffer_load_b128(rws, doff[u], 0, AUX_SC1);
      }
#pragma unroll
      for (int u = 0; u < GS; u += 2)
        asm("v_fmac_f32 %0, %4, %6\n\tv_fmac_f32 %1, %4, %7\n\tv_fmac_f32 %2, %5, %8\n\tv_fmac_f32 %3, %5, %9"
            : "+v"(a0), "+v"(a1), "+v"(b0), "+v"(b1)
            : "s"(fu[u]), "s"(fu[u + 1]), "v"(d[u][0]), "v"(d[u][2]), "v"(d[u + 1][0]), "v"(d[u + 1][2]));
      a0 += b0;
      a1 += b1;
    }
    const unsigned pk = cvtpk(a0, a1), pp = dppu<0x128>(pk);
    __builtin_amdgcn_raw_buffer_store_b32(__builtin_amdgcn_perm(pk, pp, oddi ? 0x07060302u : 0x01000504u), rout, orow, 0, OUT_AUX);
    __builtin_amdgcn_raw_buffer_store_b32(tag, rws, lane ? OOB : 0, part * 4, AUX_SC1);
  }
  STAMP(7);
  STAMP_FLUSH;
}

}

extern "C" void kernel_launch(void* const* d_in, const int* in_sizes, int n_in, void* d_out, int out_size, void* d_ws,
                              size_t ws_size, hipStream_t stream) {
  (void)in_sizes; (void)n_in; (void)out_size; (void)ws_size;
  (void)hipFuncSetAttribute((const void*)attn_fused, hipFuncAttributeMaxDynamicSharedMemorySize, LDS_BYTES);
  hipLaunchKernelGGL(attn_fused, dim3(NKVH * NPART), dim3(NTHR), LDS_BYTES, stream, (const char*)d_in[0], (const char*)d_in[1],
                     (const char*)d_in[2], (const int*)d_in[3], (const int*)d_in[4], (char*)d_ws, (__hip_bfloat16*)d_out, in_sizes[1] / (PAGE_BYTES / 2));
}
```

```cpp
#include <hip/hip_runtime.h>
#include <hip/hip_bf16.h>
#include <stdint.h>

#define KV_AUX 2
#define WSH 17
#define PACE 500
#define PACE_N0 1
#define HINT_EARLY 1
#define POLL_SLEEP 0
#define SLIP_C 0
#define MAP 0
#define ROT 0
#define PF_N 2
#define PF_AUX 0
#define PACE_T0 0
#define MRG0 1
#define LEAD 170
#define LEAD2 25
#define OUT_TOUCH 0
#define OUT_AUX 16
#define ENDN 6
#define ENDF 100
#define POLL_AT -20
#define ROW_WAIT 0
#define GW 2
#define ADV 160
#define ADV_K0 1
#define ADV_LAT 135
#define ADV_SLOW 25
#define ADV_LINE 128
#define ADV_N0 6
#define ADV_GAIN 5
#define ADV_BACK 40
#define ADV_CLIP 200
#define CODE_PF 8
#define HDR_AHEAD 1
#define PRIO 1
#define STAMP(i)
#define STAMP_DECL
#define STAMP_FLUSH

namespace {

constexpr int NBATCH = 8, NH = 32, NKVH = 8, HD = 128, PAGE = 16, NBLK = 512, LK = NBLK * PAGE;
constexpr int NPART = 32, NWAVE = 8, NTHR = NWAVE * 64, NCOL = 32;
constexpr int TOK_BYTES = NKVH * HD * 2;
constexpr int PAGE_BYTES = PAGE * TOK_BYTES;
constexpr int PAGE_SPAN = (PAGE - 1) * TOK_BYTES + HD * 2;
constexpr float SC_LOG2 = 0.08838834764831845f * 1.4426950408889634f;
constexpr int AUX_SC1 = 16;
constexpr unsigned SPIN_CAP = 1u << 20;
constexpr int OOB = 0x70000000;
constexpr int GS = 12;

typedef __attribute__((ext_vector_type(8))) __bf16 bf16x8;
typedef __attribute__((ext_vector_type(2))) __bf16 bf16x2;
typedef __attribute__((ext_vector_type(4))) short s16x4;
typedef __attribute__((ext_vector_type(4))) float f32x4;
typedef __attribute__((ext_vector_type(2))) float f32x2;
typedef __attribute__((ext_vector_type(4))) unsigned u32x4;
typedef __attribute__((ext_vector_type(2))) unsigned u32x2;
typedef __attribute__((ext_vector_type(8))) int i32x8;
extern "C" __device__ int llvm_writelane(int, int, int) __asm("llvm.amdgcn.writelane.i32");

constexpr int COL_F = NWAVE * HD + 4;
constexpr int LDS_TBL = 0;
constexpr int LDS_PW = LDS_TBL + NBLK * NBATCH * 4;
constexpr int LDS_EP = LDS_PW + 64;
constexpr int LDS_Q = LDS_EP + 512;
constexpr int LDS_ML = LDS_Q + 8192;
constexpr int LDS_O = LDS_ML + NCOL * 40 * 4;
constexpr int LDS_BYTES = LDS_O + NCOL * COL_F * 4;
constexpr int LDS_FIN = LDS_TBL;

constexpr int WS_HEAD = 2 << 20, WS_SLOT0 = 1024, SLOT_BYTES = 1152;
constexpr int WS_BYTES = WS_SLOT0 + NCOL * NPART * SLOT_BYTES;

__device__ __forceinline__ f32x4 mfma32(u32x4 a, u32x4 b, f32x4 c) {
  return __builtin_amdgcn_mfma_f32_16x16x32_bf16(__builtin_bit_cast(bf16x8, a), __builtin_bit_cast(bf16x8, b), c, 0, 0, 0);
}
__device__ __forceinline__ f32x4 mfma16(u32x2 a, u32x2 b, f32x4 c) {
  return __builtin_amdgcn_mfma_f32_16x16x16bf16_1k(__builtin_bit_cast(s16x4, a), __builtin_bit_cast(s16x4, b), c, 0, 0, 0);
}
__device__ __forceinline__ unsigned cvtpk(float lo, float hi) {
  return __builtin_bit_cast(unsigned, __builtin_convertvector(f32x2{lo, hi}, bf16x2));
}
__device__ __forceinline__ float fmx(float a, float b) { return __builtin_elementwise_maximum(a, b); }
__device__ __forceinline__ float swap16_max(float x) {
  auto s = __builtin_amdgcn_permlane16_swap(__float_as_uint(x), __float_as_uint(x), false, false);
  return fmx(__uint_as_float(s[0]), __uint_as_float(s[1]));
}
__device__ __forceinline__ float swap32_max(float x) {
  auto s = __builtin_amdgcn_permlane32_swap(__float_as_uint(x), __float_as_uint(x), false, false);
  return fmx(__uint_as_float(s[0]), __uint_as_float(s[1]));
}
__device__ __forceinline__ float swap16_sum(float x) {
  auto s = __builtin_amdgcn_permlane16_swap(__float_as_uint(x), __float_as_uint(x), false, false);
  return __uint_as_float(s[0]) + __uint_as_float(s[1]);
}
template <int CTRL> __device__ __forceinline__ unsigned dppu(unsigned x) {
  return (unsigned)__builtin_amdgcn_mov_dpp((int)x, CTRL, 0xf, 0xf, true);
}
template <int CTRL> __device__ __forceinline__ float dppf(float x) { return __uint_as_float(dppu<CTRL>(__float_as_uint(x))); }
constexpr int XOR1 = 0xB1, XOR2 = 0x4E, XOR3 = 0x1B, XOR7 = 0x141, XOR15 = 0x140;
__device__ __forceinline__ float max8(float x) {
  x = fmx(x, dppf<XOR7>(x));
  return fmx(fmx(fmx(x, dppf<XOR1>(x)), dppf<XOR2>(x)), dppf<XOR3>(x));
}
__device__ __forceinline__ float sum8(float x) {
  x += dppf<XOR1>(x); x += dppf<XOR2>(x); return x + dppf<XOR7>(x);
}
__device__ __forceinline__ float rdlane(float x, int l) { return __int_as_float(__builtin_amdgcn_readlane(__float_as_int(x), l)); }
__device__ __forceinline__ int clamp_len(int l) { return (l <= 0 || l > LK) ? LK : l; }
__device__ __forceinline__ void lds_barrier() { asm volatile("s_waitcnt lgkmcnt(0)\n\ts_barrier" ::: "memory"); }
__device__ __forceinline__ unsigned now() { return (unsigned)__builtin_amdgcn_s_memrealtime(); }
__device__ __forceinline__ f32x2 wsum8(const f32x2 (&o)[8], float f, int l0) {
  float s[8];
#pragma unroll
  for (int w = 0; w < 8; ++w) s[w] = rdlane(f, l0 + w);
  asm volatile("" : "+s"(s[0]), "+s"(s[1]), "+s"(s[2]), "+s"(s[3]), "+s"(s[4]), "+s"(s[5]), "+s"(s[6]), "+s"(s[7]));
  const f32x2 a = o[0] * s[0] + o[2] * s[2] + o[4] * s[4] + o[6] * s[6];
  const f32x2 b = o[1] * s[1] + o[3] * s[3] + o[5] * s[5] + o[7] * s[7];
  return a + b;
}

#define CND4(D, A, B, P)                                                                        \
  "v_cndmask_b32_dpp %[" D "0], %[" A "0], %[" B "0], vcc " P " row_mask:0xf bank_mask:0xf\n\t" \
  "v_cndmask_b32_dpp %[" D "1], %[" A "1], %[" B "1], vcc " P " row_mask:0xf bank_mask:0xf\n\t" \
  "v_cndmask_b32_dpp %[" D "2], %[" A "2], %[" B "2], vcc " P " row_mask:0xf bank_mask:0xf\n\t" \
  "v_cndmask_b32_dpp %[" D "3], %[" A "3], %[" B "3], vcc " P " row_mask:0xf bank_mask:0xf\n\t"
#define SWAP_STAGE(MASK, P)                                                                                              \
  asm("s_mov_b32 vcc_lo, " MASK "\n\ts_mov_b32 vcc_hi, " MASK "\n\t" CND4("x", "b", "a", P) "s_not_b64 vcc, vcc\n\t" CND4("y", "a", "b", P) \
      "s_nop 1"                                                                                                          \
      : [x0] "=&v"(x[0]), [x1] "=&v"(x[1]), [x2] "=&v"(x[2]), [x3] "=&v"(x[3]), [y0] "=&v"(y[0]), [y1] "=&v"(y[1]),      \
        [y2] "=&v"(y[2]), [y3] "=&v"(y[3])                                                                               \
      : [a0] "v"(a[0]), [a1] "v"(a[1]), [a2] "v"(a[2]), [a3] "v"(a[3]), [b0] "v"(b[0]), [b1] "v"(b[1]), [b2] "v"(b[2]),  \
        [b3] "v"(b[3])                                                                                                   \
      : "vcc", "scc")
__device__ __forceinline__ void swap1(const u32x4& a, const u32x4& b, u32x4& ox, u32x4& oy) {
  unsigned x[4], y[4];
  SWAP_STAGE("0x55555555", "quad_perm:[1,0,3,2]");
  ox = u32x4{x[0], x[1], x[2], x[3]};
  oy = u32x4{y[0], y[1], y[2], y[3]};
}
__device__ __forceinline__ void swap2(const u32x4& a, const u32x4& b, u32x4& ox, u32x4& oy) {
  unsigned x[4], y[4];
  SWAP_STAGE("0x33333333", "quad_perm:[2,3,0,1]");
  ox = u32x4{x[0], x[1], x[2], x[3]};
  oy = u32x4{y[0], y[1], y[2], y[3]};
}
__device__ __forceinline__ void quad_transpose(const u32x4 (&in)[4], u32x4 (&out)[4]) {
  u32x4 t[4];
  swap1(in[0], in[1], t[0], t[1]);
  swap1(in[2], in[3], t[2], t[3]);
  swap2(t[0], t[2], out[0], out[2]);
  swap2(t[1], t[3], out[1], out[3]);
}

struct Page {
  u32x4 k[4];
  u32x4 v[4];
};

__global__ __launch_bounds__(NTHR, 2) void attn_fused(const char* __restrict__ q, const char* __restrict__ kc,
                                                      const char* __restrict__ vc, const int* __restrict__ bt,
                                                      const int* __restrict__ lens, char* __restrict__ ws,
                                                      __hip_bfloat16* __restrict__ out, int nphys) {
  extern __shared__ __attribute__((aligned(16))) char smem[];
  STAMP_DECL;
  STAMP(0);
  const int tid = threadIdx.x, lane = tid & 63, wave = __builtin_amdgcn_readfirstlane(tid >> 6);
  if (tid < 16) ((unsigned*)(smem + LDS_PW))[tid] = 0u;
  unsigned warm0 = 0;
  if (CODE_PF && wave == 0) {
    unsigned long long pc;
    unsigned len;
    asm volatile("s_getpc_b64 %0\n.Lcode_begin:\n\ts_mov_b32 %1, .Lcode_end-.Lcode_begin" : "=s"(pc), "=s"(len));
    const auto rc = __builtin_amdgcn_make_buffer_rsrc((void*)pc, 0, len, 0x00020000);
    warm0 = __builtin_amdgcn_raw_buffer_load_b32(rc, (lane * CODE_PF + ((blockIdx.x >> 3) & (CODE_PF - 1))) * 128, 0, 0);
  }
  asm volatile("s_waitcnt lgkmcnt(0)" :: "s"(q), "s"(kc), "s"(vc), "s"(bt), "s"(lens), "s"(ws), "s"(out), "s"(nphys) : "memory");
  const i32x8 slen = *(const __attribute__((address_space(4))) i32x8*)(uintptr_t)lens;
  const int g = lane >> 4, c = lane & 15;
  const int kvh = blockIdx.x & 7, part = blockIdx.x >> 3;

  u32x2 y[NBATCH];
  const bool btw = (unsigned)(wave - 2) < 4u;
  const int bcol = (tid - 128) * 8;
  if (btw) {
    const auto rbt = __builtin_amdgcn_make_buffer_rsrc((void*)bt, 0, NBATCH * NBLK * 4, 0x00020000);
#pragma unroll
    for (int b = 0; b < NBATCH; ++b) y[b] = __builtin_amdgcn_raw_buffer_load_b64(rbt, bcol + (b & 1) * 2048, (b >> 1) * 4096, 0);
  }
  asm volatile("s_barrier" ::: "memory");
  const auto rws = __builtin_amdgcn_make_buffer_rsrc((void*)(ws + (size_t)kvh * WS_HEAD), 0, WS_BYTES, 0x00020000);
  const auto rout = __builtin_amdgcn_make_buffer_rsrc((void*)out, 0, NBATCH * NH * HD * 2, 0x00020000);
  const char* kbase = kc + kvh * (HD * 2);
  const char* vbase = vc + kvh * (HD * 2);
  int koff = ((c >> 2) * 4) * TOK_BYTES + (4 * g + (c & 3)) * 16;
  int voff = 4 * g * TOK_BYTES + c * 16;
  asm volatile("" : "+v"(koff), "+v"(voff), "+s"(kbase), "+s"(vbase));
  if (btw) {
#pragma unroll
    for (int b = 0; b < NBATCH; ++b) *(u32x2*)(smem + LDS_TBL + b * (NBLK * 4) + bcol) = y[b];
    unsigned d0 = y[0][0] ^ y[1][0], d1 = y[0][1] ^ y[1][1];
#pragma unroll
    for (int b = 2; b < NBATCH; ++b) {
      d0 = __builtin_amdgcn_bitop3_b32(d0, y[0][0], y[b][0], 0xF6);
      d1 = __builtin_amdgcn_bitop3_b32(d1, y[0][1], y[b][1], 0xF6);
    }
    unsigned f0, f1;
    asm("s_ff1_i32_b64 %0, %1" : "=s"(f0) : "s"(__builtin_amdgcn_ballot_w64(d0 != 0)));
    asm("s_ff1_i32_b64 %0, %1" : "=s"(f1) : "s"(__builtin_amdgcn_ballot_w64(d1 != 0)));
    asm volatile("" ::: "memory");
    ((unsigned*)(smem + LDS_PW))[wave - 2] = (((wave - 2) * 128) | min(2 * f0, 2 * f1 + 1)) + 1;
  }
  if (wave == 6) {
    const auto rq = __builtin_amdgcn_make_buffer_rsrc((void*)(q + 4 * kvh * (HD * 2)), 0, NBATCH * NH * HD * 2 - 4 * kvh * (HD * 2), 0x00020000);
    u32x4 qs[NBATCH];
#pragma unroll
    for (int b = 0; b < NBATCH; ++b) qs[b] = __builtin_amdgcn_raw_buffer_load_b128(rq, lane * 16, b * (NH * HD * 2), 0);
    const unsigned e = __builtin_amdgcn_raw_buffer_load_b32(rws, lane < 32 ? lane * 4 : WS_SLOT0 + (part * NPART + lane - 32) * SLOT_BYTES + 1024, 0, AUX_SC1);
    const unsigned ot = OUT_TOUCH ? __builtin_amdgcn_raw_buffer_load_b32(rout, ((part >> 2) * NH + 4 * kvh + (part & 3)) * (HD * 2) + lane * 4, 0, OUT_TOUCH - 1) : 0u;
#pragma unroll
    for (int b = 0; b < NBATCH; ++b) *(u32x4*)(smem + LDS_Q + (((b >> 2) * 4 + (c & 3)) * 64 + (c >> 2) * 16 + (b & 3) * 4 + g) * 16) = qs[b];
    *(unsigned*)(smem + LDS_EP + lane * 4) = e;
    asm volatile("" ::: "memory");
    ((unsigned*)(smem + LDS_PW))[4] = 1u;
    asm volatile("" :: "v"(ot));
  }
  if (PF_N > 0 && wave == 7) {
    const auto rk = __builtin_amdgcn_make_buffer_rsrc((void*)kc, 0, (unsigned)nphys * PAGE_BYTES, 0x00020000);
    const auto rv = __builtin_amdgcn_make_buffer_rsrc((void*)vc, 0, (unsigned)nphys * PAGE_BYTES, 0x00020000);
    unsigned wk[(PF_N + 1) / 2], wv[(PF_N + 1) / 2];
#pragma unroll
    for (int u = 0; u < (PF_N + 1) / 2; ++u) {
      const int j = 2 * u + (lane >> 5);
      const unsigned page = (unsigned)(((part * PF_N + j) * 2 + 1) * nphys) / (2 * NPART * PF_N);
      const unsigned off = j < PF_N ? page * PAGE_BYTES + ((lane >> 1) & 15) * TOK_BYTES + kvh * (HD * 2) + (lane & 1) * 128 : 0xfffffff0u;
      wk[u] = __builtin_amdgcn_raw_buffer_load_b32(rk, off, 0, PF_AUX);
      wv[u] = __builtin_amdgcn_raw_buffer_load_b32(rv, off, 0, PF_AUX);
    }
#pragma unroll
    for (int u = 0; u < (PF_N + 1) / 2; ++u) asm volatile("" :: "v"(wk[u]), "v"(wv[u]));
  }
  asm volatile("" :: "v"(warm0));
  __builtin_amdgcn_s_waitcnt(0x0F70);
  int lraw = 1;
#pragma unroll
  for (int b = 0; b < NBATCH; ++b) lraw = llvm_writelane(slen[b], b, lraw);
  const int npg8 = (clamp_len(lraw) + PAGE - 1) >> 4;
  int nmax = max(npg8, (int)dppu<XOR1>(npg8));
  nmax = max(nmax, (int)dppu<XOR2>(nmax));
  nmax = __builtin_amdgcn_readfirstlane(max(nmax, (int)dppu<XOR7>(nmax)));
  int npg = lane < NBATCH ? npg8 : (1 << 20);
  asm volatile("" : "+v"(npg), "+s"(nmax));
  STAMP(1);

  unsigned p1;
  do {
    asm volatile("" ::: "memory");
    const u32x4 a = *(const u32x4*)(smem + LDS_PW);
    p1 = __builtin_amdgcn_readfirstlane(min(min(a[0], a[1]), min(a[2], a[3])));
  } while (!p1);
  asm volatile("" ::: "memory");
  const int P = __builtin_amdgcn_readfirstlane((int)(p1 - 1 < (unsigned)nmax ? p1 - 1 : (unsigned)nmax));
  const int cnt = max(npg - P, 0);
  int send = cnt;
  send += (int)dppu<0x111>(send);
  send += (int)dppu<0x112>(send);
  send += (int)dppu<0x114>(send);
  const int npriv = __builtin_amdgcn_readlane(send, NBATCH - 1);
  int bnd;
  {
    const unsigned x = (unsigned)(npriv * 16 + P * WSH) * (unsigned)lane;
    bnd = max((int)__umulhi(x, (unsigned)((1ull << 27) / WSH)), ((int)(x >> 5) - P * (WSH - 16)) >> 4);
  }
  const int i0 = __builtin_amdgcn_readlane(bnd, part), i1 = __builtin_amdgcn_readlane(bnd, part + 1);
  const int Ps = P;
  const int nitem = i1 - i0, nbase = nitem >> 3, nextra = nitem & 7;
  const int nw = nbase + (wave < nextra ? 1 : 0);
  const int soff = lane * NBLK - (send - cnt);
  auto table_index = [&](int it, int lo, int hi) {
    const int rel = it - Ps;
    int add = 0;
    for (int cur = max(lo - Ps, 0); cur < hi - Ps;) {
      const int s = __popcll(__builtin_amdgcn_ballot_w64(send <= cur));
      const int so = __builtin_amdgcn_readlane(soff, s);
      add = rel >= cur ? so : add;
      cur = __builtin_amdgcn_readlane(send, s);
    }
    return (it + add) & (NBATCH * NBLK - 1);
  };
  int item, tix;
  {
    const int w0 = wave & ~(GW - 1);
    const int idx0 = i0 + nbase * w0 + min(w0, nextra);
    item = idx0 + GW * lane + (wave & (GW - 1));
    tix = table_index(item, idx0, min(idx0 + GW * nw, i1));
  }
  const unsigned tstart = now();
  const int phys = *(const int*)(smem + LDS_TBL + tix * 4);

  STAMP(2);
  unsigned tpace;
  int dpace, nfast, dcut;
  Page pg;
  auto issue = [&](int k) {
    const size_t off = (size_t)(unsigned)__builtin_amdgcn_readlane(phys, k) * PAGE_BYTES;
    const auto rk = __builtin_amdgcn_make_buffer_rsrc((void*)(kbase + off), 0, PAGE_SPAN, 0x00020000);
    const auto rv = __builtin_amdgcn_make_buffer_rsrc((void*)(vbase + off), 0, PAGE_SPAN, 0x00020000);
#pragma unroll
    for (int i = 0; i < 4; ++i) pg.k[i] = __builtin_amdgcn_raw_buffer_load_b128(rk, koff + (i & 1) * TOK_BYTES, (i >> 1) * 2 * TOK_BYTES, KV_AUX);
#pragma unroll
    for (int j = 0; j < 4; ++j) pg.v[j] = __builtin_amdgcn_raw_buffer_load_b128(rv, voff + (j & 1) * TOK_BYTES, (j >> 1) * 2 * TOK_BYTES, KV_AUX);
    __builtin_amdgcn_sched_barrier(0);
  };
  unsigned tiss = 0, tarr = 0;
  int adv = 0;
  const int lat8 = 8 * (ADV_LAT + ((((uintptr_t)kbase >> 8) & 3) == 1 ? ADV_SLOW : 0));
  auto pace = [&](int k) {
    if (ADV > 0 && k > 0) {
      asm volatile("" ::: "memory");
      *(unsigned char*)(smem + LDS_PW + 40 + wave) = (unsigned char)min(tarr - tiss, (unsigned)ADV_CLIP);
      asm volatile("" ::: "memory");
      const u32x4 st = *(const u32x4*)(smem + LDS_PW + 32);
      adv = __builtin_amdgcn_readfirstlane(*(const int*)(smem + LDS_PW + 48));
      if (k >= ADV_K0) {
        const int sum = __builtin_amdgcn_readfirstlane(__builtin_amdgcn_sad_u8(st[2], 0, __builtin_amdgcn_sad_u8(st[3], 0, 0)));
        const int step = min(max((lat8 - sum) * ADV_GAIN, -ADV_BACK * 16), ADV * 16);
        adv = min(max(adv + ((dpace * step) >> 14), 0), (k * 8 + wave - ADV_N0) * ((dpace * ADV_LINE) >> 10));
        *(int*)(smem + LDS_PW + 48) = adv;
      }
    }
    const unsigned tg = tpace + ((k * 8 * dpace - max(k * 8 - nfast, 0) * dcut - adv) >> 8);
    while ((int)((tiss = now()) - tg) < 0) __builtin_amdgcn_s_sleep(1);
  };
  if (wave == 0) ((unsigned*)(smem + LDS_PW))[5] = tstart | 1u;
  if (nw > 0 && wave <= PACE_N0) issue(0);

  unsigned contrib;
  {
    const int s = part >> 2;
    const int e = __builtin_amdgcn_readlane(send, s) + Ps, n = __builtin_amdgcn_readlane(cnt, s), ps = min(Ps, __builtin_amdgcn_readlane(npg, s));
    const int nxt = __builtin_amdgcn_ds_bpermute((lane + 1) * 4, bnd);
    contrib = (unsigned)__builtin_amdgcn_ballot_w64(nxt > bnd && (bnd < ps || (n > 0 && bnd < e && nxt > e - n)));
  }
  const unsigned um = nitem > 0 ? (unsigned)__builtin_amdgcn_ballot_w64(i0 < min(P, npg) || (cnt > 0 && i0 - P < send && i1 - P > send - cnt)) & 0xffu : 0u;
  const int nseq = __popc(um);
  const bool wide = nseq > 2;
  {
    const float span = (float)(npriv * 16 + P * WSH) * (PACE * (1.f / 32.f)) - (wide ? LEAD * 256.f : nseq == 2 ? LEAD2 * 256.f : 0.f);
    dpace = __builtin_amdgcn_readfirstlane((int)(fmaxf(span, 0.f) * __builtin_amdgcn_rcpf((float)max(nitem, 1))));
    nfast = nitem - ENDN - wave;
    dcut = wide ? 0 : __builtin_amdgcn_readfirstlane((int)((float)dpace * (ENDF * 0.01f) * (float)(P * WSH) * __builtin_amdgcn_rcpf((float)(npriv * 16 + P * WSH))));
  }
  do {
    asm volatile("" ::: "memory");
    tpace = __builtin_amdgcn_readfirstlane(((const unsigned*)(smem + LDS_PW))[5]);
  } while (!tpace);
  const unsigned tend = tpace + (unsigned)(((npriv * 16 + P * WSH) >> 5) * PACE >> 8);
  tpace += ((wave - PACE_N0) * dpace >> 8) + PACE_T0;
  if (nw > 0 && wave > PACE_N0) {
    pace(0);
    issue(0);
  }

  while (__hip_atomic_load((unsigned*)(smem + LDS_PW + 16), __ATOMIC_RELAXED, __HIP_MEMORY_SCOPE_WORKGROUP) == 0u) __builtin_amdgcn_s_sleep(1);
  asm volatile("" ::: "memory");
  const unsigned epoch = *(const unsigned*)(smem + LDS_EP + lane * 4);
  int lenv[2];
  u32x4 qf[2][4];
#pragma unroll
  for (int cg = 0; cg < 2; ++cg) {
    const int l = __builtin_amdgcn_ds_bpermute((4 * cg + (c >> 2)) * 4, lraw);
#pragma unroll
    for (int i = 0; i < 4; ++i) qf[cg][i] = *(const u32x4*)(smem + LDS_Q + ((cg * 4 + i) * 64 + lane) * 16);
    if (l <= 0) {
#pragma unroll
      for (int i = 0; i < 4; ++i) qf[cg][i] = u32x4{0, 0, 0, 0};
    }
    lenv[cg] = clamp_len(l) - 4 * g;
  }
  const unsigned meta = (unsigned)(tix & (NBLK - 1)) * PAGE | (item < Ps ? 0xff0000u : 0x10000u << (tix >> 9));
  const int rk = (wave - nextra) & 7;
  int ncol = wide ? (nseq + 1 - (rk >> 2)) >> 1 : (unsigned)(rk - MRG0) < 4u ? nseq : 0;
  int mrd, lrd, pcol, phdr;
  unsigned ctv;
  {
    const int u = (lane >> 3) & 3;
    const int rank = ((um >> (lane & 7)) & 1) ? __popc(um & ((1u << (lane & 7)) - 1)) : -1;
    const unsigned long long hit = __builtin_amdgcn_ballot_w64(rank == (wide ? 2 * u + (rk >> 2) : u));
    const unsigned mine8 = (unsigned)(hit >> (8 * u)) & 0xffu;
    const bool on = mine8 != 0 && lane < 32;
    const int col = 4 * __builtin_ctz(mine8 | 0x80u) + (rk & 3);
    mrd = LDS_ML + (col * 40 + 32 + (lane & 7)) * 4;
    lrd = LDS_ML + (col * 40 + (lane & 7) * 4) * 4;
    pcol = col;
    const int slot = WS_SLOT0 + (col * NPART + part) * SLOT_BYTES;
    phdr = on ? slot + 1024 + (lane & 7) * 16 : OOB;
    ctv = (unsigned)__builtin_amdgcn_ds_bpermute(col * 4, (int)epoch) + 1;
  }
  int phdr0 = wide || lane >= 8 ? OOB : phdr;
  int ord = LDS_O + (__builtin_amdgcn_readfirstlane(pcol) * COL_F + 2 * lane) * 4;
  int prow = WS_SLOT0 + (__builtin_amdgcn_readfirstlane(pcol) * NPART + part) * SLOT_BYTES + lane * 16;
  unsigned ct = __builtin_amdgcn_readfirstlane(ctv);
  asm volatile("" : "+v"(mrd), "+v"(lrd), "+v"(ord), "+v"(prow), "+v"(ct), "+v"(phdr0), "+v"(phdr), "+v"(ctv), "+v"(pcol), "+s"(ncol));
  int wm[2], wl[2], wo[2];
#pragma unroll
  for (int cg = 0; cg < 2; ++cg) {
    wm[cg] = LDS_ML + ((16 * cg + c) * 40 + 32 + rk) * 4;
    wl[cg] = LDS_ML + ((16 * cg + c) * 40 + rk * 4 + g) * 4;
    wo[cg] = ((um >> (4 * cg + (c >> 2))) & 1) ? LDS_O + ((16 * cg + c) * COL_F + rk * HD + 4 * g) * 4 : -1;
  }
  asm volatile("" : "+v"(wm[0]), "+v"(wl[0]), "+v"(wo[0]), "+v"(wo[1]));

  float m[2] = {-INFINITY, -INFINITY}, ms[2] = {0.f, 0.f}, l[2] = {0.f, 0.f};
  f32x4 acc[2][8];
#pragma unroll
  for (int cg = 0; cg < 2; ++cg)
#pragma unroll
    for (int i = 0; i < 8; ++i) acc[cg][i] = f32x4{0.f, 0.f, 0.f, 0.f};
  if (PRIO) __builtin_amdgcn_s_setprio(2);

  auto put_stats = [&]() {
#pragma unroll
    for (int cg = 0; cg < 2; ++cg) {
      *(float*)(smem + wm[0] + cg * (16 * 160)) = m[cg] * SC_LOG2;
      *(float*)(smem + wl[0] + cg * (16 * 160)) = l[cg];
    }
    asm volatile("" ::: "memory");
    *(unsigned char*)(smem + LDS_PW + 24 + rk) = 1;
  };
  if (nw == 0) put_stats();
  for (int k = 0; k < nw;) {
    u32x4 kt[4];
    u32x2 va[8];
    quad_transpose(pg.k, kt);
    if (ADV > 0) { asm volatile("" :: "v"(kt[0])); tarr = now(); }
    const unsigned mt = (unsigned)__builtin_amdgcn_readlane((int)meta, k);
    ++k;
    u32x2 pb[2];
#pragma unroll
    for (int cg = 0; cg < 2; ++cg) {
      if ((mt >> (16 + 4 * cg)) & 0xfu) {
        f32x4 s = {0.f, 0.f, 0.f, 0.f};
#pragma unroll
        for (int i = 0; i < 4; ++i) s = mfma32(kt[i], qf[cg][i], s);
        const int lim = ((mt >> (16 + 4 * cg + (c >> 2))) & 1) ? lenv[cg] - (int)(mt & 0xffffu) : 0;
        unsigned long long live[4];
#pragma unroll
        for (int r = 0; r < 4; ++r) live[r] = __builtin_amdgcn_ballot_w64(r < lim);
        asm volatile("" : "+s"(live[0]), "+s"(live[1]), "+s"(live[2]), "+s"(live[3]));
        float x[4];
#pragma unroll
        for (int r = 0; r < 4; ++r) x[r] = __builtin_amdgcn_inverse_ballot_w64(live[r]) ? s[r] : -INFINITY;
        const float mx = swap32_max(swap16_max(fmx(fmx(x[0], x[1]), fmx(x[2], x[3]))));
        if (__builtin_amdgcn_ballot_w64(mx > m[cg])) {
          m[cg] = fmx(m[cg], mx);
          const float mn = m[cg] == -INFINITY ? 0.f : m[cg] * SC_LOG2;
          const float alpha = __builtin_amdgcn_exp2f(fminf(ms[cg] - mn, 0.f));
          ms[cg] = mn;
          l[cg] *= alpha;
#pragma unroll
          for (int i = 0; i < 8; ++i) acc[cg][i] *= alpha;
        }
#pragma unroll
        for (int r = 0; r < 4; ++r) x[r] = __builtin_amdgcn_exp2f(__builtin_fmaf(x[r], SC_LOG2, -ms[cg]));
        l[cg] += (x[0] + x[1]) + (x[2] + x[3]);
        pb[cg] = u32x2{cvtpk(x[0], x[1]), cvtpk(x[2], x[3])};
      }
    }
    if (k == nw) put_stats();
#pragma unroll
    for (int i = 0; i < 8; ++i) {
      const unsigned sel = (i & 1) ? 0x07060302u : 0x05040100u;
      va[i][0] = __builtin_amdgcn_perm(pg.v[1][i >> 1], pg.v[0][i >> 1], sel);
      va[i][1] = __builtin_amdgcn_perm(pg.v[3][i >> 1], pg.v[2][i >> 1], sel);
    }
#pragma unroll
    for (int cg = 0; cg < 2; ++cg) {
      if ((mt >> (16 + 4 * cg)) & 0xfu) {
#pragma unroll
        for (int i = 0; i < 8; ++i) acc[cg][i] = mfma16(va[i], pb[cg], acc[cg][i]);
      }
    }
    if (k < nw) {
      pace(k);
      issue(k);
    }
  }
  __builtin_amdgcn_s_waitcnt(0x0F70);
  STAMP(4);

#pragma unroll
  for (int cg = 0; cg < 2; ++cg) {
    if (wo[cg] >= 0) {
#pragma unroll
      for (int i = 0; i < 8; ++i) *(f32x4*)(smem + wo[cg] + 64 * i) = acc[cg][i];
    }
  }
  asm volatile("" ::: "memory");
  *(unsigned char*)(smem + LDS_PW + 32 + rk) = 1;
  if (PRIO) __builtin_amdgcn_s_setprio(0);

  if (ncol > 0) {
    for (;;) {
      asm volatile("" ::: "memory");
      const u32x2 dn = *(const u32x2*)(smem + LDS_PW + 32);
      if (__builtin_amdgcn_ballot_w64((dn[0] & (dn[1] | 0x01010100u)) == 0x01010101u)) break;
    }
    asm volatile("" ::: "memory");
    f32x2 o[NWAVE];
#pragma unroll
    for (int w = 0; w < 5; ++w) o[w] = *(const f32x2*)(smem + ord + w * (HD * 4));
    float mw;
    f32x4 lw;
    for (;;) {
      asm volatile("" ::: "memory");
      const unsigned dn = *(const unsigned*)(smem + LDS_PW + 28);
      asm volatile("" ::: "memory");
      mw = *(const float*)(smem + mrd);
      lw = *(const f32x4*)(smem + lrd);
      asm volatile("" : "+v"(mw), "+v"(lw));
      if (__builtin_amdgcn_ballot_w64(dn == 0x01010101u)) break;
    }
    const float M = max8(mw);
    const float f = __builtin_amdgcn_exp2f(mw - M);
    const float L = sum8(f * ((lw[0] + lw[1]) + (lw[2] + lw[3])));
    const u32x4 hdr = {__float_as_uint(M), ctv, __float_as_uint(L), ctv};
    __builtin_amdgcn_raw_buffer_store_b128(hdr, rws, phdr0, 0, AUX_SC1);
    STAMP(5);
    float fw[8];
#pragma unroll
    for (int w = 0; w < 8; ++w) fw[w] = rdlane(f, w);
    asm volatile("" : "+s"(fw[0]), "+s"(fw[1]), "+s"(fw[2]), "+s"(fw[3]), "+s"(fw[4]), "+s"(fw[5]), "+s"(fw[6]), "+s"(fw[7]));
    f32x2 a = (o[0] * fw[0] + o[2] * fw[2] + o[4] * fw[4]) + (o[1] * fw[1] + o[3] * fw[3]);
    asm volatile("" : "+v"(a));
    for (;;) {
      asm volatile("" ::: "memory");
      const unsigned dn = *(const unsigned*)(smem + LDS_PW + 36);
      asm volatile("" ::: "memory");
#pragma unroll
      for (int w = 5; w < NWAVE; ++w) o[w] = *(const f32x2*)(smem + ord + w * (HD * 4));
      asm volatile("" : "+v"(o[5]), "+v"(o[6]), "+v"(o[7]));
      if (__builtin_amdgcn_ballot_w64(dn == 0x01010101u)) break;
    }
    a = (a + o[5] * fw[5]) + (o[6] * fw[6] + o[7] * fw[7]);
    __builtin_amdgcn_raw_buffer_store_b128(u32x4{__float_as_uint(a[0]), ct, __float_as_uint(a[1]), ct}, rws, prow, 0, AUX_SC1);
    const int phdr1 = !wide && lane < 8 ? OOB : phdr;
#pragma unroll 1
    for (int u = 1; u < ncol; ++u) {
      const int col = __builtin_amdgcn_readlane(pcol, 8 * u);
#pragma unroll
      for (int w = 0; w < NWAVE; ++w) o[w] = *(const f32x2*)(smem + LDS_O + (col * COL_F + 2 * lane) * 4 + w * (HD * 4));
      if (u == max(ncol - HDR_AHEAD, 1)) __builtin_amdgcn_raw_buffer_store_b128(hdr, rws, phdr1, 0, AUX_SC1);
      ct = __builtin_amdgcn_readlane(ctv, 8 * u);
      a = wsum8(o, f, 8 * u);
      __builtin_amdgcn_raw_buffer_store_b128(u32x4{__float_as_uint(a[0]), ct, __float_as_uint(a[1]), ct}, rws,
                                             WS_SLOT0 + (col * NPART + part) * SLOT_BYTES + lane * 16, 0, AUX_SC1);
    }
    if (wide && ncol == 1) __builtin_amdgcn_raw_buffer_store_b128(hdr, rws, phdr1, 0, AUX_SC1);
    STAMP(6);
  }

  if (rk == 0) {
    const unsigned tag = (unsigned)__builtin_amdgcn_readlane((int)epoch, part) + 1;
    const bool oddi = (lane >> 3) & 1;
    int orow = (((part >> 2) * NH + 4 * kvh + (part & 3)) * HD + ((lane >> 1) & 3) * 32 + (lane & 1) * 16 + (lane >> 4) * 2 + (oddi ? 8 : 0)) * 2;
    float Mr = -3.0e38f, Lr = 0.f, a0 = 0.f, a1 = 0.f;
    unsigned spin = 0;
    const int ncon = __popc(contrib);
    const int crank = lane < 32 && ((contrib >> lane) & 1) ? __popc(contrib & ((1u << lane) - 1)) : 63;
    const int cpart = __builtin_amdgcn_ds_permute(crank * 4, lane);
    for (int base = 0; base < ncon; base += GS) {
      const int u16 = lane & 15;
      const bool hon = u16 < GS && base + u16 < ncon;
      const int sl = __builtin_amdgcn_ds_bpermute((base + u16) * 4, cpart);
      const int so = hon ? WS_SLOT0 + (part * NPART + sl) * SLOT_BYTES : OOB;
      const unsigned htag = hon ? tag : 0u;
      const float hbias = hon ? 0.f : -INFINITY;
      int doff[GS];
      unsigned etag[GS];
#pragma unroll
      for (int u = 0; u < GS; ++u) {
        doff[u] = __builtin_amdgcn_readlane(so, u) + lane * 16;
        etag[u] = __builtin_amdgcn_readlane(htag, u);
        asm volatile("" : "+v"(doff[u]), "+s"(etag[u]));
      }
      asm volatile("" : "+v"(orow));
      u32x4 h, d[GS];
      for (;;) {
        asm volatile("" ::: "memory");
        const int av = __builtin_amdgcn_readfirstlane(*(const int*)(smem + LDS_PW + 48));
        if (wide || (int)(now() - (tend + POLL_AT - (av >> 8))) >= 0) break;
        __builtin_amdgcn_s_sleep(2);
      }
#pragma unroll 1
      for (; spin < SPIN_CAP; ++spin) {
        asm volatile("" ::: "memory");
        h = __builtin_amdgcn_raw_buffer_load_b128(rws, so + 1024, 0, AUX_SC1);
        if (!__builtin_amdgcn_ballot_w64(__builtin_amdgcn_bitop3_b32(h[1], h[3], htag, 0x7E) != 0)) break;
        if (POLL_SLEEP) __builtin_amdgcn_s_sleep(POLL_SLEEP);
      }
      STAMP(3);
      if (ROW_WAIT) __builtin_amdgcn_s_sleep(ROW_WAIT);
#pragma unroll
      for (int u = GS - 1; u >= 0; --u) d[u] = __builtin_amdgcn_raw_buffer_load_b128(rws, doff[u], 0, AUX_SC1);
      __builtin_amdgcn_sched_barrier(0);
      const float mu = __uint_as_float(h[0]) + hbias;
      float M = max8(mu);
      M = fmx(fmx(M, dppf<XOR15>(M)), Mr);
      float f = __builtin_amdgcn_exp2f(mu - M), fr = __builtin_amdgcn_exp2f(Mr - M);
      const float l8 = sum8(f * __uint_as_float(h[2]));
      Lr = (l8 + dppf<XOR15>(l8)) + fr * Lr;
      Mr = M;
      if (base + GS >= ncon) {
        const float inv = __builtin_amdgcn_rcpf(Lr);
        f *= inv;
        fr *= inv;
      }
      a0 *= fr;
      a1 *= fr;
      float fu[GS], b0 = 0.f, b1 = 0.f;
#pragma unroll
      for (int u = 0; u < GS; ++u) {
        fu[u] = rdlane(f, u);
        asm volatile("" : "+s"(fu[u]));
      }
      asm volatile("s_nop 1" : "+v"(a0), "+v"(a1), "+v"(b0), "+v"(b1));
#pragma unroll 1
      for (; spin < SPIN_CAP; ++spin) {
        unsigned bad = 0;
#pragma unroll
        for (int u = GS - 1; u >= 0; --u) bad |= __builtin_amdgcn_bitop3_b32(d[u][1], d[u][3], etag[u], 0x7E);
        if (!__builtin_amdgcn_ballot_w64(bad != 0)) break;
        asm volatile("" ::: "memory");
#pragma unroll
        for (int u = GS - 1; u >= 0; --u) d[u] = __builtin_amdgcn_raw_buffer_load_b128(rws, doff[u], 0, AUX_SC1);
      }
#pragma unroll
      for (int u = 0; u < GS; u += 2)
        asm("v_fmac_f32 %0, %4, %6\n\tv_fmac_f32 %1, %4, %7\n\tv_fmac_f32 %2, %5, %8\n\tv_fmac_f32 %3, %5, %9"
            : "+v"(a0), "+v"(a1), "+v"(b0), "+v"(b1)
            : "s"(fu[u]), "s"(fu[u + 1]), "v"(d[u][0]), "v"(d[u][2]), "v"(d[u + 1][0]), "v"(d[u + 1][2]));
      a0 += b0;
      a1 += b1;
    }
    const unsigned pk = cvtpk(a0, a1), pp = dppu<0x128>(pk);
    __builtin_amdgcn_raw_buffer_store_b32(__builtin_amdgcn_perm(pk, pp, oddi ? 0x07060302u : 0x01000504u), rout, orow, 0, OUT_AUX);
    __builtin_amdgcn_raw_buffer_store_b32(tag, rws, lane ? OOB : 0, part * 4, AUX_SC1);
    if (CODE_PF) asm volatile(".Lcode_end:");
  }
  STAMP(7);
  STAMP_FLUSH;
}

}

extern "C" void kernel_launch(void* const* d_in, const int* in_sizes, int n_in, void* d_out, int out_size, void* d_ws,
                              size_t ws_size, hipStream_t stream) {
  (void)in_sizes; (void)n_in; (void)out_size; (void)ws_size;
  (void)hipFuncSetAttribute((const void*)attn_fused, hipFuncAttributeMaxDynamicSharedMemorySize, LDS_BYTES);
  hipLaunchKernelGGL(attn_fused, dim3(NKVH * NPART), dim3(NTHR), LDS_BYTES, stream, (const char*)d_in[0], (const char*)d_in[1],
                     (const char*)d_in[2], (const int*)d_in[3], (const int*)d_in[4], (char*)d_ws, (__hip_bfloat16*)d_out, in_sizes[1] / (PAGE_BYTES / 2));
}
```

```cpp
#define PROBE 1
#define PWAVE 0
#define PMED 1
#include <hip/hip_runtime.h>
#include <hip/hip_bf16.h>
#include <stdint.h>

#define KV_AUX 2
#define WSH 17
#define PACE 500
#define PACE_N0 1
#define HINT_EARLY 1
#define POLL_SLEEP 0
#define SLIP_C 0
#define MAP 0
#define ROT 0
#define PF_N 2
#define PF_AUX 0
#define PACE_T0 0
#define MRG0 1
#define LEAD 170
#define LEAD2 25
#define OUT_TOUCH 0
#define OUT_AUX 16
#define ENDN 6
#define ENDF 100
#define POLL_AT -20
#define ROW_WAIT 0
#define GW 2
#define ADV 160
#define ADV_K0 1
#define ADV_LAT 135
#define ADV_SLOW 25
#define ADV_LINE 128
#define ADV_N0 6
#define ADV_GAIN 5
#define ADV_BACK 40
#define ADV_CLIP 200
#define CODE_PF 8
#define HDR_AHEAD 1
#define PRIO 1
#define STAMP_DECL unsigned long long st_[8] = {0, 0, 0, 0, 0, 0, 0, 0}
#define STAMP(i) do { if ((i) == 0 || (i) == PROBE) asm volatile("s_memrealtime %0\n\ts_waitcnt lgkmcnt(0)" : "=s"(st_[i]) :: "memory"); } while (0)
#define STAMP_FLUSH do { if (wave == PWAVE && (PMED == 0 || (blockIdx.x >> 3) == 13)) { const unsigned long long tg_ = st_[0] + 10000ull + 20ull * (st_[PROBE] - st_[0]); for (int it_ = 0; it_ < 4000000; ++it_) if (__builtin_amdgcn_s_memrealtime() >= tg_) break; } } while (0)

namespace {

constexpr int NBATCH = 8, NH = 32, NKVH = 8, HD = 128, PAGE = 16, NBLK = 512, LK = NBLK * PAGE;
constexpr int NPART = 32, NWAVE = 8, NTHR = NWAVE * 64, NCOL = 32;
constexpr int TOK_BYTES = NKVH * HD * 2;
constexpr int PAGE_BYTES = PAGE * TOK_BYTES;
constexpr int PAGE_SPAN = (PAGE - 1) * TOK_BYTES + HD * 2;
constexpr float SC_LOG2 = 0.08838834764831845f * 1.4426950408889634f;
constexpr int AUX_SC1 = 16;
constexpr unsigned SPIN_CAP = 1u << 20;
constexpr int OOB = 0x70000000;
constexpr int GS = 12;

typedef __attribute__((ext_vector_type(8))) __bf16 bf16x8;
typedef __attribute__((ext_vector_type(2))) __bf16 bf16x2;
typedef __attribute__((ext_vector_type(4))) short s16x4;
typedef __attribute__((ext_vector_type(4))) float f32x4;
typedef __attribute__((ext_vector_type(2))) float f32x2;
typedef __attribute__((ext_vector_type(4))) unsigned u32x4;
typedef __attribute__((ext_vector_type(2))) unsigned u32x2;
typedef __attribute__((ext_vector_type(8))) int i32x8;
extern "C" __device__ int llvm_writelane(int, int, int) __asm("llvm.amdgcn.writelane.i32");

constexpr int COL_F = NWAVE * HD + 4;
constexpr int LDS_TBL = 0;
constexpr int LDS_PW = LDS_TBL + NBLK * NBATCH * 4;
constexpr int LDS_EP = LDS_PW + 64;
constexpr int LDS_Q = LDS_EP + 512;
constexpr int LDS_ML = LDS_Q + 8192;
constexpr int LDS_O = LDS_ML + NCOL * 40 * 4;
constexpr int LDS_BYTES = LDS_O + NCOL * COL_F * 4;
constexpr int LDS_FIN = LDS_TBL;

constexpr int WS_HEAD = 2 << 20, WS_SLOT0 = 1024, SLOT_BYTES = 1152;
constexpr int WS_BYTES = WS_SLOT0 + NCOL * NPART * SLOT_BYTES;

__device__ __forceinline__ f32x4 mfma32(u32x4 a, u32x4 b, f32x4 c) {
  return __builtin_amdgcn_mfma_f32_16x16x32_bf16(__builtin_bit_cast(bf16x8, a), __builtin_bit_cast(bf16x8, b), c, 0, 0, 0);
}
__device__ __forceinline__ f32x4 mfma16(u32x2 a, u32x2 b, f32x4 c) {
  return __builtin_amdgcn_mfma_f32_16x16x16bf16_1k(__builtin_bit_cast(s16x4, a), __builtin_bit_cast(s16x4, b), c, 0, 0, 0);
}
__device__ __forceinline__ unsigned cvtpk(float lo, float hi) {
  return __builtin_bit_cast(unsigned, __builtin_convertvector(f32x2{lo, hi}, bf16x2));
}
__device__ __forceinline__ float fmx(float a, float b) { return __builtin_elementwise_maximum(a, b); }
__device__ __forceinline__ float swap16_max(float x) {
  auto s = __builtin_amdgcn_permlane16_swap(__float_as_uint(x), __float_as_uint(x), false, false);
  return fmx(__uint_as_float(s[0]), __uint_as_float(s[1]));
}
__device__ __forceinline__ float swap32_max(float x) {
  auto s = __builtin_amdgcn_permlane32_swap(__float_as_uint(x), __float_as_uint(x), false, false);
  return fmx(__uint_as_float(s[0]), __uint_as_float(s[1]));
}
__device__ __forceinline__ float swap16_sum(float x) {
  auto s = __builtin_amdgcn_permlane16_swap(__float_as_uint(x), __float_as_uint(x), false, false);
  return __uint_as_float(s[0]) + __uint_as_float(s[1]);
}
template <int CTRL> __device__ __forceinline__ unsigned dppu(unsigned x) {
  return (unsigned)__builtin_amdgcn_mov_dpp((int)x, CTRL, 0xf, 0xf, true);
}
template <int CTRL> __device__ __forceinline__ float dppf(float x) { return __uint_as_float(dppu<CTRL>(__float_as_uint(x))); }
constexpr int XOR1 = 0xB1, XOR2 = 0x4E, XOR3 = 0x1B, XOR7 = 0x141, XOR15 = 0x140;
__device__ __forceinline__ float max8(float x) {
  x = fmx(x, dppf<XOR7>(x));
  return fmx(fmx(fmx(x, dppf<XOR1>(x)), dppf<XOR2>(x)), dppf<XOR3>(x));
}
__device__ __forceinline__ float sum8(float x) {
  x += dppf<XOR1>(x); x += dppf<XOR2>(x); return x + dppf<XOR7>(x);
}
__device__ __forceinline__ float rdlane(float x, int l) { return __int_as_float(__builtin_amdgcn_readlane(__float_as_int(x), l)); }
__device__ __forceinline__ int clamp_len(int l) { return (l <= 0 || l > LK) ? LK : l; }
__device__ __forceinline__ void lds_barrier() { asm volatile("s_waitcnt lgkmcnt(0)\n\ts_barrier" ::: "memory"); }
__device__ __forceinline__ unsigned now() { return (unsigned)__builtin_amdgcn_s_memrealtime(); }
__device__ __forceinline__ f32x2 wsum8(const f32x2 (&o)[8], float f, int l0) {
  float s[8];
#pragma unroll
  for (int w = 0; w < 8; ++w) s[w] = rdlane(f, l0 + w);
  asm volatile("" : "+s"(s[0]), "+s"(s[1]), "+s"(s[2]), "+s"(s[3]), "+s"(s[4]), "+s"(s[5]), "+s"(s[6]), "+s"(s[7]));
  const f32x2 a = o[0] * s[0] + o[2] * s[2] + o[4] * s[4] + o[6] * s[6];
  const f32x2 b = o[1] * s[1] + o[3] * s[3] + o[5] * s[5] + o[7] * s[7];
  return a + b;
}

#define CND4(D, A, B, P)                                                                        \
  "v_cndmask_b32_dpp %[" D "0], %[" A "0], %[" B "0], vcc " P " row_mask:0xf bank_mask:0xf\n\t" \
  "v_cndmask_b32_dpp %[" D "1], %[" A "1], %[" B "1], vcc " P " row_mask:0xf bank_mask:0xf\n\t" \
  "v_cndmask_b32_dpp %[" D "2], %[" A "2], %[" B "2], vcc " P " row_mask:0xf bank_mask:0xf\n\t" \
  "v_cndmask_b32_dpp %[" D "3], %[" A "3], %[" B "3], vcc " P " row_mask:0xf bank_mask:0xf\n\t"
#define SWAP_STAGE(MASK, P)                                                                                              \
  asm("s_mov_b32 vcc_lo, " MASK "\n\ts_mov_b32 vcc_hi, " MASK "\n\t" CND4("x", "b", "a", P) "s_not_b64 vcc, vcc\n\t" CND4("y", "a", "b", P) \
      "s_nop 1"                                                                                                          \
      : [x0] "=&v"(x[0]), [x1] "=&v"(x[1]), [x2] "=&v"(x[2]), [x3] "=&v"(x[3]), [y0] "=&v"(y[0]), [y1] "=&v"(y[1]),      \
        [y2] "=&v"(y[2]), [y3] "=&v"(y[3])                                                                               \
      : [a0] "v"(a[0]), [a1] "v"(a[1]), [a2] "v"(a[2]), [a3] "v"(a[3]), [b0] "v"(b[0]), [b1] "v"(b[1]), [b2] "v"(b[2]),  \
        [b3] "v"(b[3])                                                                                                   \
      : "vcc", "scc")
__device__ __forceinline__ void swap1(const u32x4& a, const u32x4& b, u32x4& ox, u32x4& oy) {
  unsigned x[4], y[4];
  SWAP_STAGE("0x55555555", "quad_perm:[1,0,3,2]");
  ox = u32x4{x[0], x[1], x[2], x[3]};
  oy = u32x4{y[0], y[1], y[2], y[3]};
}
__device__ __forceinline__ void swap2(const u32x4& a, const u32x4& b, u32x4& ox, u32x4& oy) {
  unsigned x[4], y[4];
  SWAP_STAGE("0x33333333", "quad_perm:[2,3,0,1]");
  ox = u32x4{x[0], x[1], x[2], x[3]};
  oy = u32x4{y[0], y[1], y[2], y[3]};
}
__device__ __forceinline__ void quad_transpose(const u32x4 (&in)[4], u32x4 (&out)[4]) {
  u32x4 t[4];
  swap1(in[0], in[1], t[0], t[1]);
  swap1(in[2], in[3], t[2], t[3]);
  swap2(t[0], t[2], out[0], out[2]);
  swap2(t[1], t[3], out[1], out[3]);
}

struct Page {
  u32x4 k[4];
  u32x4 v[4];
};

__global__ __launch_bounds__(NTHR, 2) void attn_fused(const char* __restrict__ q, const char* __restrict__ kc,
                                                      const char* __restrict__ vc, const int* __restrict__ bt,
                                                      const int* __restrict__ lens, char* __restrict__ ws,
                                                      __hip_bfloat16* __restrict__ out, int nphys) {
  extern __shared__ __attribute__((aligned(16))) char smem[];
  STAMP_DECL;
  st_[0] = __builtin_amdgcn_s_memrealtime();
  __builtin_amdgcn_sched_barrier(0);
  const int tid = threadIdx.x, lane = tid & 63, wave = __builtin_amdgcn_readfirstlane(tid >> 6);
  if (tid < 16) ((unsigned*)(smem + LDS_PW))[tid] = 0u;
  unsigned warm0 = 0;
  if (CODE_PF && wave == 0) {
    unsigned long long pc;
    unsigned len;
    asm volatile("s_getpc_b64 %0\n.Lcode_begin:\n\ts_mov_b32 %1, .Lcode_end-.Lcode_begin" : "=s"(pc), "=s"(len));
    const auto rc = __builtin_amdgcn_make_buffer_rsrc((void*)pc, 0, len, 0x00020000);
    warm0 = __builtin_amdgcn_raw_buffer_load_b32(rc, (lane * CODE_PF + ((blockIdx.x >> 3) & (CODE_PF - 1))) * 128, 0, 0);
  }
  asm volatile("s_waitcnt lgkmcnt(0)" :: "s"(q), "s"(kc), "s"(vc), "s"(bt), "s"(lens), "s"(ws), "s"(out), "s"(nphys) : "memory");
  STAMP(1);
  const i32x8 slen = *(const __attribute__((address_space(4))) i32x8*)(uintptr_t)lens;
  const int g = lane >> 4, c = lane & 15;
  const int kvh = blockIdx.x & 7, part = blockIdx.x >> 3;

  u32x2 y[NBATCH];
  const bool btw = (unsigned)(wave - 2) < 4u;
  const int bcol = (tid - 128) * 8;
  if (btw) {
    const auto rbt = __builtin_amdgcn_make_buffer_rsrc((void*)bt, 0, NBATCH * NBLK * 4, 0x00020000);
#pragma unroll
    for (int b = 0; b < NBATCH; ++b) y[b] = __builtin_amdgcn_raw_buffer_load_b64(rbt, bcol + (b & 1) * 2048, (b >> 1) * 4096, 0);
  }
  asm volatile("s_barrier" ::: "memory");
  if (PROBE == 3) st_[3] = __builtin_amdgcn_s_memrealtime();
  const auto rws = __builtin_amdgcn_make_buffer_rsrc((void*)(ws + (size_t)kvh * WS_HEAD), 0, WS_BYTES, 0x00020000);
  const auto rout = __builtin_amdgcn_make_buffer_rsrc((void*)out, 0, NBATCH * NH * HD * 2, 0x00020000);
  const char* kbase = kc + kvh * (HD * 2);
  const char* vbase = vc + kvh * (HD * 2);
  int koff = ((c >> 2) * 4) * TOK_BYTES + (4 * g + (c & 3)) * 16;
  int voff = 4 * g * TOK_BYTES + c * 16;
  asm volatile("" : "+v"(koff), "+v"(voff), "+s"(kbase), "+s"(vbase));
  if (btw) {
    if (PROBE == 4) { asm volatile("" :: "v"(y[0]), "v"(y[1]), "v"(y[2]), "v"(y[3]), "v"(y[4]), "v"(y[5]), "v"(y[6]), "v"(y[7])); st_[4] = __builtin_amdgcn_s_memrealtime(); }
#pragma unroll
    for (int b = 0; b < NBATCH; ++b) *(u32x2*)(smem + LDS_TBL + b * (NBLK * 4) + bcol) = y[b];
    unsigned d0 = y[0][0] ^ y[1][0], d1 = y[0][1] ^ y[1][1];
#pragma unroll
    for (int b = 2; b < NBATCH; ++b) {
      d0 = __builtin_amdgcn_bitop3_b32(d0, y[0][0], y[b][0], 0xF6);
      d1 = __builtin_amdgcn_bitop3_b32(d1, y[0][1], y[b][1], 0xF6);
    }
    unsigned f0, f1;
    asm("s_ff1_i32_b64 %0, %1" : "=s"(f0) : "s"(__builtin_amdgcn_ballot_w64(d0 != 0)));
    asm("s_ff1_i32_b64 %0, %1" : "=s"(f1) : "s"(__builtin_amdgcn_ballot_w64(d1 != 0)));
    asm volatile("" ::: "memory");
    ((unsigned*)(smem + LDS_PW))[wave - 2] = (((wave - 2) * 128) | min(2 * f0, 2 * f1 + 1)) + 1;
  }
  if (wave == 6) {
    const auto rq = __builtin_amdgcn_make_buffer_rsrc((void*)(q + 4 * kvh * (HD * 2)), 0, NBATCH * NH * HD * 2 - 4 * kvh * (HD * 2), 0x00020000);
    u32x4 qs[NBATCH];
#pragma unroll
    for (int b = 0; b < NBATCH; ++b) qs[b] = __builtin_amdgcn_raw_buffer_load_b128(rq, lane * 16, b * (NH * HD * 2), 0);
    const unsigned e = __builtin_amdgcn_raw_buffer_load_b32(rws, lane < 32 ? lane * 4 : WS_SLOT0 + (part * NPART + lane - 32) * SLOT_BYTES + 1024, 0, AUX_SC1);
    const unsigned ot = OUT_TOUCH ? __builtin_amdgcn_raw_buffer_load_b32(rout, ((part >> 2) * NH + 4 * kvh + (part & 3)) * (HD * 2) + lane * 4, 0, OUT_TOUCH - 1) : 0u;
#pragma unroll
    for (int b = 0; b < NBATCH; ++b) *(u32x4*)(smem + LDS_Q + (((b >> 2) * 4 + (c & 3)) * 64 + (c >> 2) * 16 + (b & 3) * 4 + g) * 16) = qs[b];
    *(unsigned*)(smem + LDS_EP + lane * 4) = e;
    asm volatile("" ::: "memory");
    ((unsigned*)(smem + LDS_PW))[4] = 1u;
    asm volatile("" :: "v"(ot));
  }
  if (PF_N > 0 && wave == 7) {
    const auto rk = __builtin_amdgcn_make_buffer_rsrc((void*)kc, 0, (unsigned)nphys * PAGE_BYTES, 0x00020000);
    const auto rv = __builtin_amdgcn_make_buffer_rsrc((void*)vc, 0, (unsigned)nphys * PAGE_BYTES, 0x00020000);
    unsigned wk[(PF_N + 1) / 2], wv[(PF_N + 1) / 2];
#pragma unroll
    for (int u = 0; u < (PF_N + 1) / 2; ++u) {
      const int j = 2 * u + (lane >> 5);
      const unsigned page = (unsigned)(((part * PF_N + j) * 2 + 1) * nphys) / (2 * NPART * PF_N);
      const unsigned off = j < PF_N ? page * PAGE_BYTES + ((lane >> 1) & 15) * TOK_BYTES + kvh * (HD * 2) + (lane & 1) * 128 : 0xfffffff0u;
      wk[u] = __builtin_amdgcn_raw_buffer_load_b32(rk, off, 0, PF_AUX);
      wv[u] = __builtin_amdgcn_raw_buffer_load_b32(rv, off, 0, PF_AUX);
    }
#pragma unroll
    for (int u = 0; u < (PF_N + 1) / 2; ++u) asm volatile("" :: "v"(wk[u]), "v"(wv[u]));
  }
  asm volatile("" :: "v"(warm0));
  __builtin_amdgcn_s_waitcnt(0x0F70);
  int lraw = 1;
#pragma unroll
  for (int b = 0; b < NBATCH; ++b) lraw = llvm_writelane(slen[b], b, lraw);
  const int npg8 = (clamp_len(lraw) + PAGE - 1) >> 4;
  int nmax = max(npg8, (int)dppu<XOR1>(npg8));
  nmax = max(nmax, (int)dppu<XOR2>(nmax));
  nmax = __builtin_amdgcn_readfirstlane(max(nmax, (int)dppu<XOR7>(nmax)));
  int npg = lane < NBATCH ? npg8 : (1 << 20);
  asm volatile("" : "+v"(npg), "+s"(nmax));

  unsigned p1;
  do {
    asm volatile("" ::: "memory");
    const u32x4 a = *(const u32x4*)(smem + LDS_PW);
    p1 = __builtin_amdgcn_readfirstlane(min(min(a[0], a[1]), min(a[2], a[3])));
  } while (!p1);
  asm volatile("" ::: "memory");
  const int P = __builtin_amdgcn_readfirstlane((int)(p1 - 1 < (unsigned)nmax ? p1 - 1 : (unsigned)nmax));
  STAMP(5);
  const int cnt = max(npg - P, 0);
  int send = cnt;
  send += (int)dppu<0x111>(send);
  send += (int)dppu<0x112>(send);
  send += (int)dppu<0x114>(send);
  const int npriv = __builtin_amdgcn_readlane(send, NBATCH - 1);
  int bnd;
  {
    const unsigned x = (unsigned)(npriv * 16 + P * WSH) * (unsigned)lane;
    bnd = max((int)__umulhi(x, (unsigned)((1ull << 27) / WSH)), ((int)(x >> 5) - P * (WSH - 16)) >> 4);
  }
  const int i0 = __builtin_amdgcn_readlane(bnd, part), i1 = __builtin_amdgcn_readlane(bnd, part + 1);
  const int Ps = P;
  const int nitem = i1 - i0, nbase = nitem >> 3, nextra = nitem & 7;
  const int nw = nbase + (wave < nextra ? 1 : 0);
  const int soff = lane * NBLK - (send - cnt);
  auto table_index = [&](int it, int lo, int hi) {
    const int rel = it - Ps;
    int add = 0;
    for (int cur = max(lo - Ps, 0); cur < hi - Ps;) {
      const int s = __popcll(__builtin_amdgcn_ballot_w64(send <= cur));
      const int so = __builtin_amdgcn_readlane(soff, s);
      add = rel >= cur ? so : add;
      cur = __builtin_amdgcn_readlane(send, s);
    }
    return (it + add) & (NBATCH * NBLK - 1);
  };
  int item, tix;
  {
    const int w0 = wave & ~(GW - 1);
    const int idx0 = i0 + nbase * w0 + min(w0, nextra);
    item = idx0 + GW * lane + (wave & (GW - 1));
    tix = table_index(item, idx0, min(idx0 + GW * nw, i1));
  }
  const unsigned tstart = now();
  const int phys = *(const int*)(smem + LDS_TBL + tix * 4);

  STAMP(2);
  unsigned tpace;
  int dpace, nfast, dcut;
  Page pg;
  auto issue = [&](int k) {
    const size_t off = (size_t)(unsigned)__builtin_amdgcn_readlane(phys, k) * PAGE_BYTES;
    const auto rk = __builtin_amdgcn_make_buffer_rsrc((void*)(kbase + off), 0, PAGE_SPAN, 0x00020000);
    const auto rv = __builtin_amdgcn_make_buffer_rsrc((void*)(vbase + off), 0, PAGE_SPAN, 0x00020000);
#pragma unroll
    for (int i = 0; i < 4; ++i) pg.k[i] = __builtin_amdgcn_raw_buffer_load_b128(rk, koff + (i & 1) * TOK_BYTES, (i >> 1) * 2 * TOK_BYTES, KV_AUX);
#pragma unroll
    for (int j = 0; j < 4; ++j) pg.v[j] = __builtin_amdgcn_raw_buffer_load_b128(rv, voff + (j & 1) * TOK_BYTES, (j >> 1) * 2 * TOK_BYTES, KV_AUX);
    __builtin_amdgcn_sched_barrier(0);
  };
  unsigned tiss = 0, tarr = 0;
  int adv = 0;
  const int lat8 = 8 * (ADV_LAT + ((((uintptr_t)kbase >> 8) & 3) == 1 ? ADV_SLOW : 0));
  auto pace = [&](int k) {
    if (ADV > 0 && k > 0) {
      asm volatile("" ::: "memory");
      *(unsigned char*)(smem + LDS_PW + 40 + wave) = (unsigned char)min(tarr - tiss, (unsigned)ADV_CLIP);
      asm volatile("" ::: "memory");
      const u32x4 st = *(const u32x4*)(smem + LDS_PW + 32);
      adv = __builtin_amdgcn_readfirstlane(*(const int*)(smem + LDS_PW + 48));
      if (k >= ADV_K0) {
        const int sum = __builtin_amdgcn_readfirstlane(__builtin_amdgcn_sad_u8(st[2], 0, __builtin_amdgcn_sad_u8(st[3], 0, 0)));
        const int step = min(max((lat8 - sum) * ADV_GAIN, -ADV_BACK * 16), ADV * 16);
        adv = min(max(adv + ((dpace * step) >> 14), 0), (k * 8 + wave - ADV_N0) * ((dpace * ADV_LINE) >> 10));
        *(int*)(smem + LDS_PW + 48) = adv;
      }
    }
    const unsigned tg = tpace + ((k * 8 * dpace - max(k * 8 - nfast, 0) * dcut - adv) >> 8);
    while ((int)((tiss = now()) - tg) < 0) __builtin_amdgcn_s_sleep(1);
  };
  if (wave == 0) ((unsigned*)(smem + LDS_PW))[5] = tstart | 1u;
  if (nw > 0 && wave <= PACE_N0) issue(0);

  unsigned contrib;
  {
    const int s = part >> 2;
    const int e = __builtin_amdgcn_readlane(send, s) + Ps, n = __builtin_amdgcn_readlane(cnt, s), ps = min(Ps, __builtin_amdgcn_readlane(npg, s));
    const int nxt = __builtin_amdgcn_ds_bpermute((lane + 1) * 4, bnd);
    contrib = (unsigned)__builtin_amdgcn_ballot_w64(nxt > bnd && (bnd < ps || (n > 0 && bnd < e && nxt > e - n)));
  }
  const unsigned um = nitem > 0 ? (unsigned)__builtin_amdgcn_ballot_w64(i0 < min(P, npg) || (cnt > 0 && i0 - P < send && i1 - P > send - cnt)) & 0xffu : 0u;
  const int nseq = __popc(um);
  const bool wide = nseq > 2;
  {
    const float span = (float)(npriv * 16 + P * WSH) * (PACE * (1.f / 32.f)) - (wide ? LEAD * 256.f : nseq == 2 ? LEAD2 * 256.f : 0.f);
    dpace = __builtin_amdgcn_readfirstlane((int)(fmaxf(span, 0.f) * __builtin_amdgcn_rcpf((float)max(nitem, 1))));
    nfast = nitem - ENDN - wave;
    dcut = wide ? 0 : __builtin_amdgcn_readfirstlane((int)((float)dpace * (ENDF * 0.01f) * (float)(P * WSH) * __builtin_amdgcn_rcpf((float)(npriv * 16 + P * WSH))));
  }
  do {
    asm volatile("" ::: "memory");
    tpace = __builtin_amdgcn_readfirstlane(((const unsigned*)(smem + LDS_PW))[5]);
  } while (!tpace);
  const unsigned tend = tpace + (unsigned)(((npriv * 16 + P * WSH) >> 5) * PACE >> 8);
  tpace += ((wave - PACE_N0) * dpace >> 8) + PACE_T0;
  if (nw > 0 && wave > PACE_N0) {
    pace(0);
    issue(0);
  }

  while (__hip_atomic_load((unsigned*)(smem + LDS_PW + 16), __ATOMIC_RELAXED, __HIP_MEMORY_SCOPE_WORKGROUP) == 0u) __builtin_amdgcn_s_sleep(1);
  asm volatile("" ::: "memory");
  const unsigned epoch = *(const unsigned*)(smem + LDS_EP + lane * 4);
  int lenv[2];
  u32x4 qf[2][4];
#pragma unroll
  for (int cg = 0; cg < 2; ++cg) {
    const int l = __builtin_amdgcn_ds_bpermute((4 * cg + (c >> 2)) * 4, lraw);
#pragma unroll
    for (int i = 0; i < 4; ++i) qf[cg][i] = *(const u32x4*)(smem + LDS_Q + ((cg * 4 + i) * 64 + lane) * 16);
    if (l <= 0) {
#pragma unroll
      for (int i = 0; i < 4; ++i) qf[cg][i] = u32x4{0, 0, 0, 0};
    }
    lenv[cg] = clamp_len(l) - 4 * g;
  }
  const unsigned meta = (unsigned)(tix & (NBLK - 1)) * PAGE | (item < Ps ? 0xff0000u : 0x10000u << (tix >> 9));
  const int rk = (wave - nextra) & 7;
  int ncol = wide ? (nseq + 1 - (rk >> 2)) >> 1 : (unsigned)(rk - MRG0) < 4u ? nseq : 0;
  int mrd, lrd, pcol, phdr;
  unsigned ctv;
  {
    const int u = (lane >> 3) & 3;
    const int rank = ((um >> (lane & 7)) & 1) ? __popc(um & ((1u << (lane & 7)) - 1)) : -1;
    const unsigned long long hit = __builtin_amdgcn_ballot_w64(rank == (wide ? 2 * u + (rk >> 2) : u));
    const unsigned mine8 = (unsigned)(hit >> (8 * u)) & 0xffu;
    const bool on = mine8 != 0 && lane < 32;
    const int col = 4 * __builtin_ctz(mine8 | 0x80u) + (rk & 3);
    mrd = LDS_ML + (col * 40 + 32 + (lane & 7)) * 4;
    lrd = LDS_ML + (col * 40 + (lane & 7) * 4) * 4;
    pcol = col;
    const int slot = WS_SLOT0 + (col * NPART + part) * SLOT_BYTES;
    phdr = on ? slot + 1024 + (lane & 7) * 16 : OOB;
    ctv = (unsigned)__builtin_amdgcn_ds_bpermute(col * 4, (int)epoch) + 1;
  }
  int phdr0 = wide || lane >= 8 ? OOB : phdr;
  int ord = LDS_O + (__builtin_amdgcn_readfirstlane(pcol) * COL_F + 2 * lane) * 4;
  int prow = WS_SLOT0 + (__builtin_amdgcn_readfirstlane(pcol) * NPART + part) * SLOT_BYTES + lane * 16;
  unsigned ct = __builtin_amdgcn_readfirstlane(ctv);
  asm volatile("" : "+v"(mrd), "+v"(lrd), "+v"(ord), "+v"(prow), "+v"(ct), "+v"(phdr0), "+v"(phdr), "+v"(ctv), "+v"(pcol), "+s"(ncol));
  int wm[2], wl[2], wo[2];
#pragma unroll
  for (int cg = 0; cg < 2; ++cg) {
    wm[cg] = LDS_ML + ((16 * cg + c) * 40 + 32 + rk) * 4;
    wl[cg] = LDS_ML + ((16 * cg + c) * 40 + rk * 4 + g) * 4;
    wo[cg] = ((um >> (4 * cg + (c >> 2))) & 1) ? LDS_O + ((16 * cg + c) * COL_F + rk * HD + 4 * g) * 4 : -1;
  }
  asm volatile("" : "+v"(wm[0]), "+v"(wl[0]), "+v"(wo[0]), "+v"(wo[1]));

  float m[2] = {-INFINITY, -INFINITY}, ms[2] = {0.f, 0.f}, l[2] = {0.f, 0.f};
  f32x4 acc[2][8];
#pragma unroll
  for (int cg = 0; cg < 2; ++cg)
#pragma unroll
    for (int i = 0; i < 8; ++i) acc[cg][i] = f32x4{0.f, 0.f, 0.f, 0.f};
  if (PRIO) __builtin_amdgcn_s_setprio(2);

  auto put_stats = [&]() {
#pragma unroll
    for (int cg = 0; cg < 2; ++cg) {
      *(float*)(smem + wm[0] + cg * (16 * 160)) = m[cg] * SC_LOG2;
      *(float*)(smem + wl[0] + cg * (16 * 160)) = l[cg];
    }
    asm volatile("" ::: "memory");
    *(unsigned char*)(smem + LDS_PW + 24 + rk) = 1;
  };
  if (nw == 0) put_stats();
  for (int k = 0; k < nw;) {
    u32x4 kt[4];
    u32x2 va[8];
    quad_transpose(pg.k, kt);
    if (k == 0) { asm volatile("" :: "v"(kt[0])); STAMP(6); }
    if (ADV > 0) { asm volatile("" :: "v"(kt[0])); tarr = now(); }
    const unsigned mt = (unsigned)__builtin_amdgcn_readlane((int)meta, k);
    ++k;
    u32x2 pb[2];
#pragma unroll
    for (int cg = 0; cg < 2; ++cg) {
      if ((mt >> (16 + 4 * cg)) & 0xfu) {
        f32x4 s = {0.f, 0.f, 0.f, 0.f};
#pragma unroll
        for (int i = 0; i < 4; ++i) s = mfma32(kt[i], qf[cg][i], s);
        const int lim = ((mt >> (16 + 4 * cg + (c >> 2))) & 1) ? lenv[cg] - (int)(mt & 0xffffu) : 0;
        unsigned long long live[4];
#pragma unroll
        for (int r = 0; r < 4; ++r) live[r] = __builtin_amdgcn_ballot_w64(r < lim);
        asm volatile("" : "+s"(live[0]), "+s"(live[1]), "+s"(live[2]), "+s"(live[3]));
        float x[4];
#pragma unroll
        for (int r = 0; r < 4; ++r) x[r] = __builtin_amdgcn_inverse_ballot_w64(live[r]) ? s[r] : -INFINITY;
        const float mx = swap32_max(swap16_max(fmx(fmx(x[0], x[1]), fmx(x[2], x[3]))));
        if (__builtin_amdgcn_ballot_w64(mx > m[cg])) {
          m[cg] = fmx(m[cg], mx);
          const float mn = m[cg] == -INFINITY ? 0.f : m[cg] * SC_LOG2;
          const float alpha = __builtin_amdgcn_exp2f(fminf(ms[cg] - mn, 0.f));
          ms[cg] = mn;
          l[cg] *= alpha;
#pragma unroll
          for (int i = 0; i < 8; ++i) acc[cg][i] *= alpha;
        }
#pragma unroll
        for (int r = 0; r < 4; ++r) x[r] = __builtin_amdgcn_exp2f(__builtin_fmaf(x[r], SC_LOG2, -ms[cg]));
        l[cg] += (x[0] + x[1]) + (x[2] + x[3]);
        pb[cg] = u32x2{cvtpk(x[0], x[1]), cvtpk(x[2], x[3])};
      }
    }
    if (k == nw) put_stats();
#pragma unroll
    for (int i = 0; i < 8; ++i) {
      const unsigned sel = (i & 1) ? 0x07060302u : 0x05040100u;
      va[i][0] = __builtin_amdgcn_perm(pg.v[1][i >> 1], pg.v[0][i >> 1], sel);
      va[i][1] = __builtin_amdgcn_perm(pg.v[3][i >> 1], pg.v[2][i >> 1], sel);
    }
#pragma unroll
    for (int cg = 0; cg < 2; ++cg) {
      if ((mt >> (16 + 4 * cg)) & 0xfu) {
#pragma unroll
        for (int i = 0; i < 8; ++i) acc[cg][i] = mfma16(va[i], pb[cg], acc[cg][i]);
      }
    }
    if (k < nw) {
      pace(k);
      issue(k);
    }
  }
  __builtin_amdgcn_s_waitcnt(0x0F70);

#pragma unroll
  for (int cg = 0; cg < 2; ++cg) {
    if (wo[cg] >= 0) {
#pragma unroll
      for (int i = 0; i < 8; ++i) *(f32x4*)(smem + wo[cg] + 64 * i) = acc[cg][i];
    }
  }
  asm volatile("" ::: "memory");
  *(unsigned char*)(smem + LDS_PW + 32 + rk) = 1;
  if (PRIO) __builtin_amdgcn_s_setprio(0);

  if (ncol > 0) {
    for (;;) {
      asm volatile("" ::: "memory");
      const u32x2 dn = *(const u32x2*)(smem + LDS_PW + 32);
      if (__builtin_amdgcn_ballot_w64((dn[0] & (dn[1] | 0x01010100u)) == 0x01010101u)) break;
    }
    asm volatile("" ::: "memory");
    f32x2 o[NWAVE];
#pragma unroll
    for (int w = 0; w < 5; ++w) o[w] = *(const f32x2*)(smem + ord + w * (HD * 4));
    float mw;
    f32x4 lw;
    for (;;) {
      asm volatile("" ::: "memory");
      const unsigned dn = *(const unsigned*)(smem + LDS_PW + 28);
      asm volatile("" ::: "memory");
      mw = *(const float*)(smem + mrd);
      lw = *(const f32x4*)(smem + lrd);
      asm volatile("" : "+v"(mw), "+v"(lw));
      if (__builtin_amdgcn_ballot_w64(dn == 0x01010101u)) break;
    }
    const float M = max8(mw);
    const float f = __builtin_amdgcn_exp2f(mw - M);
    const float L = sum8(f * ((lw[0] + lw[1]) + (lw[2] + lw[3])));
    const u32x4 hdr = {__float_as_uint(M), ctv, __float_as_uint(L), ctv};
    __builtin_amdgcn_raw_buffer_store_b128(hdr, rws, phdr0, 0, AUX_SC1);

    float fw[8];
#pragma unroll
    for (int w = 0; w < 8; ++w) fw[w] = rdlane(f, w);
    asm volatile("" : "+s"(fw[0]), "+s"(fw[1]), "+s"(fw[2]), "+s"(fw[3]), "+s"(fw[4]), "+s"(fw[5]), "+s"(fw[6]), "+s"(fw[7]));
    f32x2 a = (o[0] * fw[0] + o[2] * fw[2] + o[4] * fw[4]) + (o[1] * fw[1] + o[3] * fw[3]);
    asm volatile("" : "+v"(a));
    for (;;) {
      asm volatile("" ::: "memory");
      const unsigned dn = *(const unsigned*)(smem + LDS_PW + 36);
      asm volatile("" ::: "memory");
#pragma unroll
      for (int w = 5; w < NWAVE; ++w) o[w] = *(const f32x2*)(smem + ord + w * (HD * 4));
      asm volatile("" : "+v"(o[5]), "+v"(o[6]), "+v"(o[7]));
      if (__builtin_amdgcn_ballot_w64(dn == 0x01010101u)) break;
    }
    a = (a + o[5] * fw[5]) + (o[6] * fw[6] + o[7] * fw[7]);
    __builtin_amdgcn_raw_buffer_store_b128(u32x4{__float_as_uint(a[0]), ct, __float_as_uint(a[1]), ct}, rws, prow, 0, AUX_SC1);
    const int phdr1 = !wide && lane < 8 ? OOB : phdr;
#pragma unroll 1
    for (int u = 1; u < ncol; ++u) {
      const int col = __builtin_amdgcn_readlane(pcol, 8 * u);
#pragma unroll
      for (int w = 0; w < NWAVE; ++w) o[w] = *(const f32x2*)(smem + LDS_O + (col * COL_F + 2 * lane) * 4 + w * (HD * 4));
      if (u == max(ncol - HDR_AHEAD, 1)) __builtin_amdgcn_raw_buffer_store_b128(hdr, rws, phdr1, 0, AUX_SC1);
      ct = __builtin_amdgcn_readlane(ctv, 8 * u);
      a = wsum8(o, f, 8 * u);
      __builtin_amdgcn_raw_buffer_store_b128(u32x4{__float_as_uint(a[0]), ct, __float_as_uint(a[1]), ct}, rws,
                                             WS_SLOT0 + (col * NPART + part) * SLOT_BYTES + lane * 16, 0, AUX_SC1);
    }
    if (wide && ncol == 1) __builtin_amdgcn_raw_buffer_store_b128(hdr, rws, phdr1, 0, AUX_SC1);

  }

  if (rk == 0) {
    const unsigned tag = (unsigned)__builtin_amdgcn_readlane((int)epoch, part) + 1;
    const bool oddi = (lane >> 3) & 1;
    int orow = (((part >> 2) * NH + 4 * kvh + (part & 3)) * HD + ((lane >> 1) & 3) * 32 + (lane & 1) * 16 + (lane >> 4) * 2 + (oddi ? 8 : 0)) * 2;
    float Mr = -3.0e38f, Lr = 0.f, a0 = 0.f, a1 = 0.f;
    unsigned spin = 0;
    const int ncon = __popc(contrib);
    const int crank = lane < 32 && ((contrib >> lane) & 1) ? __popc(contrib & ((1u << lane) - 1)) : 63;
    const int cpart = __builtin_amdgcn_ds_permute(crank * 4, lane);
    for (int base = 0; base < ncon; base += GS) {
      const int u16 = lane & 15;
      const bool hon = u16 < GS && base + u16 < ncon;
      const int sl = __builtin_amdgcn_ds_bpermute((base + u16) * 4, cpart);
      const int so = hon ? WS_SLOT0 + (part * NPART + sl) * SLOT_BYTES : OOB;
      const unsigned htag = hon ? tag : 0u;
      const float hbias = hon ? 0.f : -INFINITY;
      int doff[GS];
      unsigned etag[GS];
#pragma unroll
      for (int u = 0; u < GS; ++u) {
        doff[u] = __builtin_amdgcn_readlane(so, u) + lane * 16;
        etag[u] = __builtin_amdgcn_readlane(htag, u);
        asm volatile("" : "+v"(doff[u]), "+s"(etag[u]));
      }
      asm volatile("" : "+v"(orow));
      u32x4 h, d[GS];
      for (;;) {
        asm volatile("" ::: "memory");
        const int av = __builtin_amdgcn_readfirstlane(*(const int*)(smem + LDS_PW + 48));
        if (wide || (int)(now() - (tend + POLL_AT - (av >> 8))) >= 0) break;
        __builtin_amdgcn_s_sleep(2);
      }
#pragma unroll 1
      for (; spin < SPIN_CAP; ++spin) {
        asm volatile("" ::: "memory");
        h = __builtin_amdgcn_raw_buffer_load_b128(rws, so + 1024, 0, AUX_SC1);
        if (!__builtin_amdgcn_ballot_w64(__builtin_amdgcn_bitop3_b32(h[1], h[3], htag, 0x7E) != 0)) break;
        if (POLL_SLEEP) __builtin_amdgcn_s_sleep(POLL_SLEEP);
      }

      if (ROW_WAIT) __builtin_amdgcn_s_sleep(ROW_WAIT);
#pragma unroll
      for (int u = GS - 1; u >= 0; --u) d[u] = __builtin_amdgcn_raw_buffer_load_b128(rws, doff[u], 0, AUX_SC1);
      __builtin_amdgcn_sched_barrier(0);
      const float mu = __uint_as_float(h[0]) + hbias;
      float M = max8(mu);
      M = fmx(fmx(M, dppf<XOR15>(M)), Mr);
      float f = __builtin_amdgcn_exp2f(mu - M), fr = __builtin_amdgcn_exp2f(Mr - M);
      const float l8 = sum8(f * __uint_as_float(h[2]));
      Lr = (l8 + dppf<XOR15>(l8)) + fr * Lr;
      Mr = M;
      if (base + GS >= ncon) {
        const float inv = __builtin_amdgcn_rcpf(Lr);
        f *= inv;
        fr *= inv;
      }
      a0 *= fr;
      a1 *= fr;
      float fu[GS], b0 = 0.f, b1 = 0.f;
#pragma unroll
      for (int u = 0; u < GS; ++u) {
        fu[u] = rdlane(f, u);
        asm volatile("" : "+s"(fu[u]));
      }
      asm volatile("s_nop 1" : "+v"(a0), "+v"(a1), "+v"(b0), "+v"(b1));
#pragma unroll 1
      for (; spin < SPIN_CAP; ++spin) {
        unsigned bad = 0;
#pragma unroll
        for (int u = GS - 1; u >= 0; --u) bad |= __builtin_amdgcn_bitop3_b32(d[u][1], d[u][3], etag[u], 0x7E);
        if (!__builtin_amdgcn_ballot_w64(bad != 0)) break;
        asm volatile("" ::: "memory");
#pragma unroll
        for (int u = GS - 1; u >= 0; --u) d[u] = __builtin_amdgcn_raw_buffer_load_b128(rws, doff[u], 0, AUX_SC1);
      }
#pragma unroll
      for (int u = 0; u < GS; u += 2)
        asm("v_fmac_f32 %0, %4, %6\n\tv_fmac_f32 %1, %4, %7\n\tv_fmac_f32 %2, %5, %8\n\tv_fmac_f32 %3, %5, %9"
            : "+v"(a0), "+v"(a1), "+v"(b0), "+v"(b1)
            : "s"(fu[u]), "s"(fu[u + 1]), "v"(d[u][0]), "v"(d[u][2]), "v"(d[u + 1][0]), "v"(d[u + 1][2]));
      a0 += b0;
      a1 += b1;
    }
    const unsigned pk = cvtpk(a0, a1), pp = dppu<0x128>(pk);
    __builtin_amdgcn_raw_buffer_store_b32(__builtin_amdgcn_perm(pk, pp, oddi ? 0x07060302u : 0x01000504u), rout, orow, 0, OUT_AUX);
    __builtin_amdgcn_raw_buffer_store_b32(tag, rws, lane ? OOB : 0, part * 4, AUX_SC1);
    if (CODE_PF) asm volatile(".Lcode_end:");
  }

  STAMP_FLUSH;
}

}

extern "C" void kernel_launch(void* const* d_in, const int* in_sizes, int n_in, void* d_out, int out_size, void* d_ws,
                              size_t ws_size, hipStream_t stream) {
  (void)in_sizes; (void)n_in; (void)out_size; (void)ws_size;
  (void)hipFuncSetAttribute((const void*)attn_fused, hipFuncAttributeMaxDynamicSharedMemorySize, LDS_BYTES);
  hipLaunchKernelGGL(attn_fused, dim3(NKVH * NPART), dim3(NTHR), LDS_BYTES, stream, (const char*)d_in[0], (const char*)d_in[1],
                     (const char*)d_in[2], (const int*)d_in[3], (const int*)d_in[4], (char*)d_ws, (__hip_bfloat16*)d_out, in_sizes[1] / (PAGE_BYTES / 2));
}
```

```cpp
#include <hip/hip_runtime.h>
#include <hip/hip_bf16.h>
#include <stdint.h>

#define KV_AUX 2
#define WSH 17
#define PACE 500
#define PACE_N0 1
#define HINT_EARLY 1
#define POLL_SLEEP 0
#define SLIP_C 0
#define MAP 0
#define ROT 0
#define PF_N 2
#define PF_AUX 0
#define PACE_T0 0
#define MRG0 1
#define LEAD 170
#define LEAD2 25
#define OUT_TOUCH 0
#define OUT_AUX 16
#define ENDN 6
#define ENDF 100
#define POLL_AT -20
#define ROW_WAIT 0
#define GW 2
#define ADV 160
#define ADV_K0 1
#define ADV_LAT 135
#define ADV_SLOW 25
#define ADV_LINE 128
#define ADV_N0 6
#define ADV_GAIN 5
#define ADV_BACK 40
#define ADV_CLIP 200
#define CODE_PF 8
#define HDR_AHEAD 1
#define CODE_WAVE 7
#define PRIO 1
#define STAMP(i)
#define STAMP_DECL
#define STAMP_FLUSH

namespace {

constexpr int NBATCH = 8, NH = 32, NKVH = 8, HD = 128, PAGE = 16, NBLK = 512, LK = NBLK * PAGE;
constexpr int NPART = 32, NWAVE = 8, NTHR = NWAVE * 64, NCOL = 32;
constexpr int TOK_BYTES = NKVH * HD * 2;
constexpr int PAGE_BYTES = PAGE * TOK_BYTES;
constexpr int PAGE_SPAN = (PAGE - 1) * TOK_BYTES + HD * 2;
constexpr float SC_LOG2 = 0.08838834764831845f * 1.4426950408889634f;
constexpr int AUX_SC1 = 16;
constexpr unsigned SPIN_CAP = 1u << 20;
constexpr int OOB = 0x70000000;
constexpr int GS = 12;

typedef __attribute__((ext_vector_type(8))) __bf16 bf16x8;
typedef __attribute__((ext_vector_type(2))) __bf16 bf16x2;
typedef __attribute__((ext_vector_type(4))) short s16x4;
typedef __attribute__((ext_vector_type(4))) float f32x4;
typedef __attribute__((ext_vector_type(2))) float f32x2;
typedef __attribute__((ext_vector_type(4))) unsigned u32x4;
typedef __attribute__((ext_vector_type(2))) unsigned u32x2;
typedef __attribute__((ext_vector_type(8))) int i32x8;
extern "C" __device__ int llvm_writelane(int, int, int) __asm("llvm.amdgcn.writelane.i32");

constexpr int COL_F = NWAVE * HD + 4;
constexpr int LDS_TBL = 0;
constexpr int LDS_PW = LDS_TBL + NBLK * NBATCH * 4;
constexpr int LDS_EP = LDS_PW + 64;
constexpr int LDS_Q = LDS_EP + 512;
constexpr int LDS_ML = LDS_Q + 8192;
constexpr int LDS_O = LDS_ML + NCOL * 40 * 4;
constexpr int LDS_BYTES = LDS_O + NCOL * COL_F * 4;
constexpr int LDS_FIN = LDS_TBL;

constexpr int WS_HEAD = 2 << 20, WS_SLOT0 = 1024, SLOT_BYTES = 1152;
constexpr int WS_BYTES = WS_SLOT0 + NCOL * NPART * SLOT_BYTES;

__device__ __forceinline__ f32x4 mfma32(u32x4 a, u32x4 b, f32x4 c) {
  return __builtin_amdgcn_mfma_f32_16x16x32_bf16(__builtin_bit_cast(bf16x8, a), __builtin_bit_cast(bf16x8, b), c, 0, 0, 0);
}
__device__ __forceinline__ f32x4 mfma16(u32x2 a, u32x2 b, f32x4 c) {
  return __builtin_amdgcn_mfma_f32_16x16x16bf16_1k(__builtin_bit_cast(s16x4, a), __builtin_bit_cast(s16x4, b), c, 0, 0, 0);
}
__device__ __forceinline__ unsigned cvtpk(float lo, float hi) {
  return __builtin_bit_cast(unsigned, __builtin_convertvector(f32x2{lo, hi}, bf16x2));
}
__device__ __forceinline__ float fmx(float a, float b) { return __builtin_elementwise_maximum(a, b); }
__device__ __forceinline__ float swap16_max(float x) {
  auto s = __builtin_amdgcn_permlane16_swap(__float_as_uint(x), __float_as_uint(x), false, false);
  return fmx(__uint_as_float(s[0]), __uint_as_float(s[1]));
}
__device__ __forceinline__ float swap32_max(float x) {
  auto s = __builtin_amdgcn_permlane32_swap(__float_as_uint(x), __float_as_uint(x), false, false);
  return fmx(__uint_as_float(s[0]), __uint_as_float(s[1]));
}
__device__ __forceinline__ float swap16_sum(float x) {
  auto s = __builtin_amdgcn_permlane16_swap(__float_as_uint(x), __float_as_uint(x), false, false);
  return __uint_as_float(s[0]) + __uint_as_float(s[1]);
}
template <int CTRL> __device__ __forceinline__ unsigned dppu(unsigned x) {
  return (unsigned)__builtin_amdgcn_mov_dpp((int)x, CTRL, 0xf, 0xf, true);
}
template <int CTRL> __device__ __forceinline__ float dppf(float x) { return __uint_as_float(dppu<CTRL>(__float_as_uint(x))); }
constexpr int XOR1 = 0xB1, XOR2 = 0x4E, XOR3 = 0x1B, XOR7 = 0x141, XOR15 = 0x140;
__device__ __forceinline__ float max8(float x) {
  x = fmx(x, dppf<XOR7>(x));
  return fmx(fmx(fmx(x, dppf<XOR1>(x)), dppf<XOR2>(x)), dppf<XOR3>(x));
}
__device__ __forceinline__ float sum8(float x) {
  x += dppf<XOR1>(x); x += dppf<XOR2>(x); return x + dppf<XOR7>(x);
}
__device__ __forceinline__ float rdlane(float x, int l) { return __int_as_float(__builtin_amdgcn_readlane(__float_as_int(x), l)); }
__device__ __forceinline__ int clamp_len(int l) { return (l <= 0 || l > LK) ? LK : l; }
__device__ __forceinline__ void lds_barrier() { asm volatile("s_waitcnt lgkmcnt(0)\n\ts_barrier" ::: "memory"); }
__device__ __forceinline__ unsigned now() { return (unsigned)__builtin_amdgcn_s_memrealtime(); }
__device__ __forceinline__ f32x2 wsum8(const f32x2 (&o)[8], float f, int l0) {
  float s[8];
#pragma unroll
  for (int w = 0; w < 8; ++w) s[w] = rdlane(f, l0 + w);
  asm volatile("" : "+s"(s[0]), "+s"(s[1]), "+s"(s[2]), "+s"(s[3]), "+s"(s[4]), "+s"(s[5]), "+s"(s[6]), "+s"(s[7]));
  const f32x2 a = o[0] * s[0] + o[2] * s[2] + o[4] * s[4] + o[6] * s[6];
  const f32x2 b = o[1] * s[1] + o[3] * s[3] + o[5] * s[5] + o[7] * s[7];
  return a + b;
}

#define CND4(D, A, B, P)                                                                        \
  "v_cndmask_b32_dpp %[" D "0], %[" A "0], %[" B "0], vcc " P " row_mask:0xf bank_mask:0xf\n\t" \
  "v_cndmask_b32_dpp %[" D "1], %[" A "1], %[" B "1], vcc " P " row_mask:0xf bank_mask:0xf\n\t" \
  "v_cndmask_b32_dpp %[" D "2], %[" A "2], %[" B "2], vcc " P " row_mask:0xf bank_mask:0xf\n\t" \
  "v_cndmask_b32_dpp %[" D "3], %[" A "3], %[" B "3], vcc " P " row_mask:0xf bank_mask:0xf\n\t"
#define SWAP_STAGE(MASK, P)                                                                                              \
  asm("s_mov_b32 vcc_lo, " MASK "\n\ts_mov_b32 vcc_hi, " MASK "\n\t" CND4("x", "b", "a", P) "s_not_b64 vcc, vcc\n\t" CND4("y", "a", "b", P) \
      "s_nop 1"                                                                                                          \
      : [x0] "=&v"(x[0]), [x1] "=&v"(x[1]), [x2] "=&v"(x[2]), [x3] "=&v"(x[3]), [y0] "=&v"(y[0]), [y1] "=&v"(y[1]),      \
        [y2] "=&v"(y[2]), [y3] "=&v"(y[3])                                                                               \
      : [a0] "v"(a[0]), [a1] "v"(a[1]), [a2] "v"(a[2]), [a3] "v"(a[3]), [b0] "v"(b[0]), [b1] "v"(b[1]), [b2] "v"(b[2]),  \
        [b3] "v"(b[3])                                                                                                   \
      : "vcc", "scc")
__device__ __forceinline__ void swap1(const u32x4& a, const u32x4& b, u32x4& ox, u32x4& oy) {
  unsigned x[4], y[4];
  SWAP_STAGE("0x55555555", "quad_perm:[1,0,3,2]");
  ox = u32x4{x[0], x[1], x[2], x[3]};
  oy = u32x4{y[0], y[1], y[2], y[3]};
}
__device__ __forceinline__ void swap2(const u32x4& a, const u32x4& b, u32x4& ox, u32x4& oy) {
  unsigned x[4], y[4];
  SWAP_STAGE("0x33333333", "quad_perm:[2,3,0,1]");
  ox = u32x4{x[0], x[1], x[2], x[3]};
  oy = u32x4{y[0], y[1], y[2], y[3]};
}
__device__ __forceinline__ void quad_transpose(const u32x4 (&in)[4], u32x4 (&out)[4]) {
  u32x4 t[4];
  swap1(in[0], in[1], t[0], t[1]);
  swap1(in[2], in[3], t[2], t[3]);
  swap2(t[0], t[2], out[0], out[2]);
  swap2(t[1], t[3], out[1], out[3]);
}

struct Page {
  u32x4 k[4];
  u32x4 v[4];
};

__global__ __launch_bounds__(NTHR, 2) void attn_fused(const char* __restrict__ q, const char* __restrict__ kc,
                                                      const char* __restrict__ vc, const int* __restrict__ bt,
                                                      const int* __restrict__ lens, char* __restrict__ ws,
                                                      __hip_bfloat16* __restrict__ out, int nphys) {
  extern __shared__ __attribute__((aligned(16))) char smem[];
  STAMP_DECL;
  STAMP(0);
  const int tid = threadIdx.x, lane = tid & 63, wave = __builtin_amdgcn_readfirstlane(tid >> 6);
  if (tid < 16) ((unsigned*)(smem + LDS_PW))[tid] = 0u;
  unsigned warm0 = 0;
  if (CODE_PF && wave == CODE_WAVE) {
    unsigned long long pc;
    unsigned len;
    asm volatile("s_getpc_b64 %0\n.Lcode_begin:\n\ts_mov_b32 %1, .Lcode_end-.Lcode_begin" : "=s"(pc), "=s"(len));
    const auto rc = __builtin_amdgcn_make_buffer_rsrc((void*)pc, 0, max((int)len, 0), 0x00020000);
    warm0 = __builtin_amdgcn_raw_buffer_load_b32(rc, (lane * CODE_PF + ((blockIdx.x >> 3) & (CODE_PF - 1))) * 128, 0, 0);
  }
  asm volatile("s_waitcnt lgkmcnt(0)" :: "s"(q), "s"(kc), "s"(vc), "s"(bt), "s"(lens), "s"(ws), "s"(out), "s"(nphys) : "memory");
  const i32x8 slen = *(const __attribute__((address_space(4))) i32x8*)(uintptr_t)lens;
  const int g = lane >> 4, c = lane & 15;
  const int kvh = blockIdx.x & 7, part = blockIdx.x >> 3;

  u32x2 y[NBATCH];
  const bool btw = (unsigned)(wave - 2) < 4u;
  const int bcol = (tid - 128) * 8;
  if (btw) {
    const auto rbt = __builtin_amdgcn_make_buffer_rsrc((void*)bt, 0, NBATCH * NBLK * 4, 0x00020000);
#pragma unroll
    for (int b = 0; b < NBATCH; ++b) y[b] = __builtin_amdgcn_raw_buffer_load_b64(rbt, bcol + (b & 1) * 2048, (b >> 1) * 4096, 0);
  }
  asm volatile("s_barrier" ::: "memory");
  const auto rws = __builtin_amdgcn_make_buffer_rsrc((void*)(ws + (size_t)kvh * WS_HEAD), 0, WS_BYTES, 0x00020000);
  const auto rout = __builtin_amdgcn_make_buffer_rsrc((void*)out, 0, NBATCH * NH * HD * 2, 0x00020000);
  const char* kbase = kc + kvh * (HD * 2);
  const char* vbase = vc + kvh * (HD * 2);
  int koff = ((c >> 2) * 4) * TOK_BYTES + (4 * g + (c & 3)) * 16;
  int voff = 4 * g * TOK_BYTES + c * 16;
  asm volatile("" : "+v"(koff), "+v"(voff), "+s"(kbase), "+s"(vbase));
  if (btw) {
#pragma unroll
    for (int b = 0; b < NBATCH; ++b) *(u32x2*)(smem + LDS_TBL + b * (NBLK * 4) + bcol) = y[b];
    unsigned d0 = y[0][0] ^ y[1][0], d1 = y[0][1] ^ y[1][1];
#pragma unroll
    for (int b = 2; b < NBATCH; ++b) {
      d0 = __builtin_amdgcn_bitop3_b32(d0, y[0][0], y[b][0], 0xF6);
      d1 = __builtin_amdgcn_bitop3_b32(d1, y[0][1], y[b][1], 0xF6);
    }
    unsigned f0, f1;
    asm("s_ff1_i32_b64 %0, %1" : "=s"(f0) : "s"(__builtin_amdgcn_ballot_w64(d0 != 0)));
    asm("s_ff1_i32_b64 %0, %1" : "=s"(f1) : "s"(__builtin_amdgcn_ballot_w64(d1 != 0)));
    asm volatile("" ::: "memory");
    ((unsigned*)(smem + LDS_PW))[wave - 2] = (((wave - 2) * 128) | min(2 * f0, 2 * f1 + 1)) + 1;
  }
  if (wave == 6) {
    const auto rq = __builtin_amdgcn_make_buffer_rsrc((void*)(q + 4 * kvh * (HD * 2)), 0, NBATCH * NH * HD * 2 - 4 * kvh * (HD * 2), 0x00020000);
    u32x4 qs[NBATCH];
#pragma unroll
    for (int b = 0; b < NBATCH; ++b) qs[b] = __builtin_amdgcn_raw_buffer_load_b128(rq, lane * 16, b * (NH * HD * 2), 0);
    const unsigned e = __builtin_amdgcn_raw_buffer_load_b32(rws, lane < 32 ? lane * 4 : WS_SLOT0 + (part * NPART + lane - 32) * SLOT_BYTES + 1024, 0, AUX_SC1);
    const unsigned ot = OUT_TOUCH ? __builtin_amdgcn_raw_buffer_load_b32(rout, ((part >> 2) * NH + 4 * kvh + (part & 3)) * (HD * 2) + lane * 4, 0, OUT_TOUCH - 1) : 0u;
#pragma unroll
    for (int b = 0; b < NBATCH; ++b) *(u32x4*)(smem + LDS_Q + (((b >> 2) * 4 + (c & 3)) * 64 + (c >> 2) * 16 + (b & 3) * 4 + g) * 16) = qs[b];
    *(unsigned*)(smem + LDS_EP + lane * 4) = e;
    asm volatile("" ::: "memory");
    ((unsigned*)(smem + LDS_PW))[4] = 1u;
    asm volatile("" :: "v"(ot));
  }
  if (PF_N > 0 && wave == 7) {
    const auto rk = __builtin_amdgcn_make_buffer_rsrc((void*)kc, 0, (unsigned)nphys * PAGE_BYTES, 0x00020000);
    const auto rv = __builtin_amdgcn_make_buffer_rsrc((void*)vc, 0, (unsigned)nphys * PAGE_BYTES, 0x00020000);
    unsigned wk[(PF_N + 1) / 2], wv[(PF_N + 1) / 2];
#pragma unroll
    for (int u = 0; u < (PF_N + 1) / 2; ++u) {
      const int j = 2 * u + (lane >> 5);
      const unsigned page = (unsigned)(((part * PF_N + j) * 2 + 1) * nphys) / (2 * NPART * PF_N);
      const unsigned off = j < PF_N ? page * PAGE_BYTES + ((lane >> 1) & 15) * TOK_BYTES + kvh * (HD * 2) + (lane & 1) * 128 : 0xfffffff0u;
      wk[u] = __builtin_amdgcn_raw_buffer_load_b32(rk, off, 0, PF_AUX);
      wv[u] = __builtin_amdgcn_raw_buffer_load_b32(rv, off, 0, PF_AUX);
    }
#pragma unroll
    for (int u = 0; u < (PF_N + 1) / 2; ++u) asm volatile("" :: "v"(wk[u]), "v"(wv[u]));
  }
  asm volatile("" :: "v"(warm0));
  __builtin_amdgcn_s_waitcnt(0x0F70);
  int lraw = 1;
#pragma unroll
  for (int b = 0; b < NBATCH; ++b) lraw = llvm_writelane(slen[b], b, lraw);
  const int npg8 = (clamp_len(lraw) + PAGE - 1) >> 4;
  int nmax = max(npg8, (int)dppu<XOR1>(npg8));
  nmax = max(nmax, (int)dppu<XOR2>(nmax));
  nmax = __builtin_amdgcn_readfirstlane(max(nmax, (int)dppu<XOR7>(nmax)));
  int npg = lane < NBATCH ? npg8 : (1 << 20);
  asm volatile("" : "+v"(npg), "+s"(nmax));
  STAMP(1);

  unsigned p1;
  do {
    asm volatile("" ::: "memory");
    const u32x4 a = *(const u32x4*)(smem + LDS_PW);
    p1 = __builtin_amdgcn_readfirstlane(min(min(a[0], a[1]), min(a[2], a[3])));
  } while (!p1);
  asm volatile("" ::: "memory");
  const int P = __builtin_amdgcn_readfirstlane((int)(p1 - 1 < (unsigned)nmax ? p1 - 1 : (unsigned)nmax));
  const int cnt = max(npg - P, 0);
  int send = cnt;
  send += (int)dppu<0x111>(send);
  send += (int)dppu<0x112>(send);
  send += (int)dppu<0x114>(send);
  const int npriv = __builtin_amdgcn_readlane(send, NBATCH - 1);
  int bnd;
  {
    const unsigned x = (unsigned)(npriv * 16 + P * WSH) * (unsigned)lane;
    bnd = max((int)__umulhi(x, (unsigned)((1ull << 27) / WSH)), ((int)(x >> 5) - P * (WSH - 16)) >> 4);
  }
  const int i0 = __builtin_amdgcn_readlane(bnd, part), i1 = __builtin_amdgcn_readlane(bnd, part + 1);
  const int Ps = P;
  const int nitem = i1 - i0, nbase = nitem >> 3, nextra = nitem & 7;
  const int nw = nbase + (wave < nextra ? 1 : 0);
  const int soff = lane * NBLK - (send - cnt);
  auto table_index = [&](int it, int lo, int hi) {
    const int rel = it - Ps;
    int add = 0;
    for (int cur = max(lo - Ps, 0); cur < hi - Ps;) {
      const int s = __popcll(__builtin_amdgcn_ballot_w64(send <= cur));
      const int so = __builtin_amdgcn_readlane(soff, s);
      add = rel >= cur ? so : add;
      cur = __builtin_amdgcn_readlane(send, s);
    }
    return (it + add) & (NBATCH * NBLK - 1);
  };
  int item, tix;
  {
    const int w0 = wave & ~(GW - 1);
    const int idx0 = i0 + nbase * w0 + min(w0, nextra);
    item = idx0 + GW * lane + (wave & (GW - 1));
    tix = table_index(item, idx0, min(idx0 + GW * nw, i1));
  }
  const unsigned tstart = now();
  const int phys = *(const int*)(smem + LDS_TBL + tix * 4);

  STAMP(2);
  unsigned tpace;
  int dpace, nfast, dcut;
  Page pg;
  auto issue = [&](int k) {
    const size_t off = (size_t)(unsigned)__builtin_amdgcn_readlane(phys, k) * PAGE_BYTES;
    const auto rk = __builtin_amdgcn_make_buffer_rsrc((void*)(kbase + off), 0, PAGE_SPAN, 0x00020000);
    const auto rv = __builtin_amdgcn_make_buffer_rsrc((void*)(vbase + off), 0, PAGE_SPAN, 0x00020000);
#pragma unroll
    for (int i = 0; i < 4; ++i) pg.k[i] = __builtin_amdgcn_raw_buffer_load_b128(rk, koff + (i & 1) * TOK_BYTES, (i >> 1) * 2 * TOK_BYTES, KV_AUX);
#pragma unroll
    for (int j = 0; j < 4; ++j) pg.v[j] = __builtin_amdgcn_raw_buffer_load_b128(rv, voff + (j & 1) * TOK_BYTES, (j >> 1) * 2 * TOK_BYTES, KV_AUX);
    __builtin_amdgcn_sched_barrier(0);
  };
  unsigned tiss = 0, tarr = 0;
  int adv = 0;
  const int lat8 = 8 * (ADV_LAT + ((((uintptr_t)kbase >> 8) & 3) == 1 ? ADV_SLOW : 0));
  auto pace = [&](int k) {
    if (ADV > 0 && k > 0) {
      asm volatile("" ::: "memory");
      *(unsigned char*)(smem + LDS_PW + 40 + wave) = (unsigned char)min(tarr - tiss, (unsigned)ADV_CLIP);
      asm volatile("" ::: "memory");
      const u32x4 st = *(const u32x4*)(smem + LDS_PW + 32);
      adv = __builtin_amdgcn_readfirstlane(*(const int*)(smem + LDS_PW + 48));
      if (k >= ADV_K0) {
        const int sum = __builtin_amdgcn_readfirstlane(__builtin_amdgcn_sad_u8(st[2], 0, __builtin_amdgcn_sad_u8(st[3], 0, 0)));
        const int step = min(max((lat8 - sum) * ADV_GAIN, -ADV_BACK * 16), ADV * 16);
        adv = min(max(adv + ((dpace * step) >> 14), 0), (k * 8 + wave - ADV_N0) * ((dpace * ADV_LINE) >> 10));
        *(int*)(smem + LDS_PW + 48) = adv;
      }
    }
    const unsigned tg = tpace + ((k * 8 * dpace - max(k * 8 - nfast, 0) * dcut - adv) >> 8);
    while ((int)((tiss = now()) - tg) < 0) __builtin_amdgcn_s_sleep(1);
  };
  if (wave == 0) ((unsigned*)(smem + LDS_PW))[5] = tstart | 1u;
  if (nw > 0 && wave <= PACE_N0) issue(0);

  unsigned contrib;
  {
    const int s = part >> 2;
    const int e = __builtin_amdgcn_readlane(send, s) + Ps, n = __builtin_amdgcn_readlane(cnt, s), ps = min(Ps, __builtin_amdgcn_readlane(npg, s));
    const int nxt = __builtin_amdgcn_ds_bpermute((lane + 1) * 4, bnd);
    contrib = (unsigned)__builtin_amdgcn_ballot_w64(nxt > bnd && (bnd < ps || (n > 0 && bnd < e && nxt > e - n)));
  }
  const unsigned um = nitem > 0 ? (unsigned)__builtin_amdgcn_ballot_w64(i0 < min(P, npg) || (cnt > 0 && i0 - P < send && i1 - P > send - cnt)) & 0xffu : 0u;
  const int nseq = __popc(um);
  const bool wide = nseq > 2;
  {
    const float span = (float)(npriv * 16 + P * WSH) * (PACE * (1.f / 32.f)) - (wide ? LEAD * 256.f : nseq == 2 ? LEAD2 * 256.f : 0.f);
    dpace = __builtin_amdgcn_readfirstlane((int)(fmaxf(span, 0.f) * __builtin_amdgcn_rcpf((float)max(nitem, 1))));
    nfast = nitem - ENDN - wave;
    dcut = wide ? 0 : __builtin_amdgcn_readfirstlane((int)((float)dpace * (ENDF * 0.01f) * (float)(P * WSH) * __builtin_amdgcn_rcpf((float)(npriv * 16 + P * WSH))));
  }
  do {
    asm volatile("" ::: "memory");
    tpace = __builtin_amdgcn_readfirstlane(((const unsigned*)(smem + LDS_PW))[5]);
  } while (!tpace);
  const unsigned tend = tpace + (unsigned)(((npriv * 16 + P * WSH) >> 5) * PACE >> 8);
  tpace += ((wave - PACE_N0) * dpace >> 8) + PACE_T0;
  if (nw > 0 && wave > PACE_N0) {
    pace(0);
    issue(0);
  }

  while (__hip_atomic_load((unsigned*)(smem + LDS_PW + 16), __ATOMIC_RELAXED, __HIP_MEMORY_SCOPE_WORKGROUP) == 0u) __builtin_amdgcn_s_sleep(1);
  asm volatile("" ::: "memory");
  const unsigned epoch = *(const unsigned*)(smem + LDS_EP + lane * 4);
  int lenv[2];
  u32x4 qf[2][4];
#pragma unroll
  for (int cg = 0; cg < 2; ++cg) {
    const int l = __builtin_amdgcn_ds_bpermute((4 * cg + (c >> 2)) * 4, lraw);
#pragma unroll
    for (int i = 0; i < 4; ++i) qf[cg][i] = *(const u32x4*)(smem + LDS_Q + ((cg * 4 + i) * 64 + lane) * 16);
    if (l <= 0) {
#pragma unroll
      for (int i = 0; i < 4; ++i) qf[cg][i] = u32x4{0, 0, 0, 0};
    }
    lenv[cg] = clamp_len(l) - 4 * g;
  }
  const unsigned meta = (unsigned)(tix & (NBLK - 1)) * PAGE | (item < Ps ? 0xff0000u : 0x10000u << (tix >> 9));
  const int rk = (wave - nextra) & 7;
  int ncol = wide ? (nseq + 1 - (rk >> 2)) >> 1 : (unsigned)(rk - MRG0) < 4u ? nseq : 0;
  int mrd, lrd, pcol, phdr;
  unsigned ctv;
  {
    const int u = (lane >> 3) & 3;
    const int rank = ((um >> (lane & 7)) & 1) ? __popc(um & ((1u << (lane & 7)) - 1)) : -1;
    const unsigned long long hit = __builtin_amdgcn_ballot_w64(rank == (wide ? 2 * u + (rk >> 2) : u));
    const unsigned mine8 = (unsigned)(hit >> (8 * u)) & 0xffu;
    const bool on = mine8 != 0 && lane < 32;
    const int col = 4 * __builtin_ctz(mine8 | 0x80u) + (rk & 3);
    mrd = LDS_ML + (col * 40 + 32 + (lane & 7)) * 4;
    lrd = LDS_ML + (col * 40 + (lane & 7) * 4) * 4;
    pcol = col;
    const int slot = WS_SLOT0 + (col * NPART + part) * SLOT_BYTES;
    phdr = on ? slot + 1024 + (lane & 7) * 16 : OOB;
    ctv = (unsigned)__builtin_amdgcn_ds_bpermute(col * 4, (int)epoch) + 1;
  }
  int phdr0 = wide || lane >= 8 ? OOB : phdr;
  int ord = LDS_O + (__builtin_amdgcn_readfirstlane(pcol) * COL_F + 2 * lane) * 4;
  int prow = WS_SLOT0 + (__builtin_amdgcn_readfirstlane(pcol) * NPART + part) * SLOT_BYTES + lane * 16;
  unsigned ct = __builtin_amdgcn_readfirstlane(ctv);
  asm volatile("" : "+v"(mrd), "+v"(lrd), "+v"(ord), "+v"(prow), "+v"(ct), "+v"(phdr0), "+v"(phdr), "+v"(ctv), "+v"(pcol), "+s"(ncol));
  int wm[2], wl[2], wo[2];
#pragma unroll
  for (int cg = 0; cg < 2; ++cg) {
    wm[cg] = LDS_ML + ((16 * cg + c) * 40 + 32 + rk) * 4;
    wl[cg] = LDS_ML + ((16 * cg + c) * 40 + rk * 4 + g) * 4;
    wo[cg] = ((um >> (4 * cg + (c >> 2))) & 1) ? LDS_O + ((16 * cg + c) * COL_F + rk * HD + 4 * g) * 4 : -1;
  }
  asm volatile("" : "+v"(wm[0]), "+v"(wl[0]), "+v"(wo[0]), "+v"(wo[1]));

  float m[2] = {-INFINITY, -INFINITY}, ms[2] = {0.f, 0.f}, l[2] = {0.f, 0.f};
  f32x4 acc[2][8];
#pragma unroll
  for (int cg = 0; cg < 2; ++cg)
#pragma unroll
    for (int i = 0; i < 8; ++i) acc[cg][i] = f32x4{0.f, 0.f, 0.f, 0.f};
  if (PRIO) __builtin_amdgcn_s_setprio(2);

  auto put_stats = [&]() {
#pragma unroll
    for (int cg = 0; cg < 2; ++cg) {
      *(float*)(smem + wm[0] + cg * (16 * 160)) = m[cg] * SC_LOG2;
      *(float*)(smem + wl[0] + cg * (16 * 160)) = l[cg];
    }
    asm volatile("" ::: "memory");
    *(unsigned char*)(smem + LDS_PW + 24 + rk) = 1;
  };
  if (nw == 0) put_stats();
  for (int k = 0; k < nw;) {
    u32x4 kt[4];
    u32x2 va[8];
    quad_transpose(pg.k, kt);
    if (ADV > 0) { asm volatile("" :: "v"(kt[0])); tarr = now(); }
    const unsigned mt = (unsigned)__builtin_amdgcn_readlane((int)meta, k);
    ++k;
    u32x2 pb[2];
#pragma unroll
    for (int cg = 0; cg < 2; ++cg) {
      if ((mt >> (16 + 4 * cg)) & 0xfu) {
        f32x4 s = {0.f, 0.f, 0.f, 0.f};
#pragma unroll
        for (int i = 0; i < 4; ++i) s = mfma32(kt[i], qf[cg][i], s);
        const int lim = ((mt >> (16 + 4 * cg + (c >> 2))) & 1) ? lenv[cg] - (int)(mt & 0xffffu) : 0;
        unsigned long long live[4];
#pragma unroll
        for (int r = 0; r < 4; ++r) live[r] = __builtin_amdgcn_ballot_w64(r < lim);
        asm volatile("" : "+s"(live[0]), "+s"(live[1]), "+s"(live[2]), "+s"(live[3]));
        float x[4];
#pragma unroll
        for (int r = 0; r < 4; ++r) x[r] = __builtin_amdgcn_inverse_ballot_w64(live[r]) ? s[r] : -INFINITY;
        const float mx = swap32_max(swap16_max(fmx(fmx(x[0], x[1]), fmx(x[2], x[3]))));
        if (__builtin_amdgcn_ballot_w64(mx > m[cg])) {
          m[cg] = fmx(m[cg], mx);
          const float mn = m[cg] == -INFINITY ? 0.f : m[cg] * SC_LOG2;
          const float alpha = __builtin_amdgcn_exp2f(fminf(ms[cg] - mn, 0.f));
          ms[cg] = mn;
          l[cg] *= alpha;
#pragma unroll
          for (int i = 0; i < 8; ++i) acc[cg][i] *= alpha;
        }
#pragma unroll
        for (int r = 0; r < 4; ++r) x[r] = __builtin_amdgcn_exp2f(__builtin_fmaf(x[r], SC_LOG2, -ms[cg]));
        l[cg] += (x[0] + x[1]) + (x[2] + x[3]);
        pb[cg] = u32x2{cvtpk(x[0], x[1]), cvtpk(x[2], x[3])};
      }
    }
    if (k == nw) put_stats();
#pragma unroll
    for (int i = 0; i < 8; ++i) {
      const unsigned sel = (i & 1) ? 0x07060302u : 0x05040100u;
      va[i][0] = __builtin_amdgcn_perm(pg.v[1][i >> 1], pg.v[0][i >> 1], sel);
      va[i][1] = __builtin_amdgcn_perm(pg.v[3][i >> 1], pg.v[2][i >> 1], sel);
    }
#pragma unroll
    for (int cg = 0; cg < 2; ++cg) {
      if ((mt >> (16 + 4 * cg)) & 0xfu) {
#pragma unroll
        for (int i = 0; i < 8; ++i) acc[cg][i] = mfma16(va[i], pb[cg], acc[cg][i]);
      }
    }
    if (k < nw) {
      pace(k);
      issue(k);
    }
  }
  __builtin_amdgcn_s_waitcnt(0x0F70);
  STAMP(4);

#pragma unroll
  for (int cg = 0; cg < 2; ++cg) {
    if (wo[cg] >= 0) {
#pragma unroll
      for (int i = 0; i < 8; ++i) *(f32x4*)(smem + wo[cg] + 64 * i) = acc[cg][i];
    }
  }
  asm volatile("" ::: "memory");
  *(unsigned char*)(smem + LDS_PW + 32 + rk) = 1;
  if (PRIO) __builtin_amdgcn_s_setprio(0);

  if (ncol > 0) {
    for (;;) {
      asm volatile("" ::: "memory");
      const u32x2 dn = *(const u32x2*)(smem + LDS_PW + 32);
      if (__builtin_amdgcn_ballot_w64((dn[0] & (dn[1] | 0x01010100u)) == 0x01010101u)) break;
    }
    asm volatile("" ::: "memory");
    f32x2 o[NWAVE];
#pragma unroll
    for (int w = 0; w < 5; ++w) o[w] = *(const f32x2*)(smem + ord + w * (HD * 4));
    float mw;
    f32x4 lw;
    for (;;) {
      asm volatile("" ::: "memory");
      const unsigned dn = *(const unsigned*)(smem + LDS_PW + 28);
      asm volatile("" ::: "memory");
      mw = *(const float*)(smem + mrd);
      lw = *(const f32x4*)(smem + lrd);
      asm volatile("" : "+v"(mw), "+v"(lw));
      if (__builtin_amdgcn_ballot_w64(dn == 0x01010101u)) break;
    }
    const float M = max8(mw);
    const float f = __builtin_amdgcn_exp2f(mw - M);
    const float L = sum8(f * ((lw[0] + lw[1]) + (lw[2] + lw[3])));
    const u32x4 hdr = {__float_as_uint(M), ctv, __float_as_uint(L), ctv};
    __builtin_amdgcn_raw_buffer_store_b128(hdr, rws, phdr0, 0, AUX_SC1);
    STAMP(5);
    float fw[8];
#pragma unroll
    for (int w = 0; w < 8; ++w) fw[w] = rdlane(f, w);
    asm volatile("" : "+s"(fw[0]), "+s"(fw[1]), "+s"(fw[2]), "+s"(fw[3]), "+s"(fw[4]), "+s"(fw[5]), "+s"(fw[6]), "+s"(fw[7]));
    f32x2 a = (o[0] * fw[0] + o[2] * fw[2] + o[4] * fw[4]) + (o[1] * fw[1] + o[3] * fw[3]);
    asm volatile("" : "+v"(a));
    for (;;) {
      asm volatile("" ::: "memory");
      const unsigned dn = *(const unsigned*)(smem + LDS_PW + 36);
      asm volatile("" ::: "memory");
#pragma unroll
      for (int w = 5; w < NWAVE; ++w) o[w] = *(const f32x2*)(smem + ord + w * (HD * 4));
      asm volatile("" : "+v"(o[5]), "+v"(o[6]), "+v"(o[7]));
      if (__builtin_amdgcn_ballot_w64(dn == 0x01010101u)) break;
    }
    a = (a + o[5] * fw[5]) + (o[6] * fw[6] + o[7] * fw[7]);
    __builtin_amdgcn_raw_buffer_store_b128(u32x4{__float_as_uint(a[0]), ct, __float_as_uint(a[1]), ct}, rws, prow, 0, AUX_SC1);
    const int phdr1 = !wide && lane < 8 ? OOB : phdr;
#pragma unroll 1
    for (int u = 1; u < ncol; ++u) {
      const int col = __builtin_amdgcn_readlane(pcol, 8 * u);
#pragma unroll
      for (int w = 0; w < NWAVE; ++w) o[w] = *(const f32x2*)(smem + LDS_O + (col * COL_F + 2 * lane) * 4 + w * (HD * 4));
      if (u == max(ncol - HDR_AHEAD, 1)) __builtin_amdgcn_raw_buffer_store_b128(hdr, rws, phdr1, 0, AUX_SC1);
      ct = __builtin_amdgcn_readlane(ctv, 8 * u);
      a = wsum8(o, f, 8 * u);
      __builtin_amdgcn_raw_buffer_store_b128(u32x4{__float_as_uint(a[0]), ct, __float_as_uint(a[1]), ct}, rws,
                                             WS_SLOT0 + (col * NPART + part) * SLOT_BYTES + lane * 16, 0, AUX_SC1);
    }
    if (wide && ncol == 1) __builtin_amdgcn_raw_buffer_store_b128(hdr, rws, phdr1, 0, AUX_SC1);
    STAMP(6);
  }

  if (rk == 0) {
    const unsigned tag = (unsigned)__builtin_amdgcn_readlane((int)epoch, part) + 1;
    const bool oddi = (lane >> 3) & 1;
    int orow = (((part >> 2) * NH + 4 * kvh + (part & 3)) * HD + ((lane >> 1) & 3) * 32 + (lane & 1) * 16 + (lane >> 4) * 2 + (oddi ? 8 : 0)) * 2;
    float Mr = -3.0e38f, Lr = 0.f, a0 = 0.f, a1 = 0.f;
    unsigned spin = 0;
    const int ncon = __popc(contrib);
    const int crank = lane < 32 && ((contrib >> lane) & 1) ? __popc(contrib & ((1u << lane) - 1)) : 63;
    const int cpart = __builtin_amdgcn_ds_permute(crank * 4, lane);
    for (int base = 0; base < ncon; base += GS) {
      const int u16 = lane & 15;
      const bool hon = u16 < GS && base + u16 < ncon;
      const int sl = __builtin_amdgcn_ds_bpermute((base + u16) * 4, cpart);
      const int so = hon ? WS_SLOT0 + (part * NPART + sl) * SLOT_BYTES : OOB;
      const unsigned htag = hon ? tag : 0u;
      const float hbias = hon ? 0.f : -INFINITY;
      int doff[GS];
      unsigned etag[GS];
#pragma unroll
      for (int u = 0; u < GS; ++u) {
        doff[u] = __builtin_amdgcn_readlane(so, u) + lane * 16;
        etag[u] = __builtin_amdgcn_readlane(htag, u);
        asm volatile("" : "+v"(doff[u]), "+s"(etag[u]));
      }
      asm volatile("" : "+v"(orow));
      u32x4 h, d[GS];
      for (;;) {
        asm volatile("" ::: "memory");
        const int av = __builtin_amdgcn_readfirstlane(*(const int*)(smem + LDS_PW + 48));
        if (wide || (int)(now() - (tend + POLL_AT - (av >> 8))) >= 0) break;
        __builtin_amdgcn_s_sleep(2);
      }
#pragma unroll 1
      for (; spin < SPIN_CAP; ++spin) {
        asm volatile("" ::: "memory");
        h = __builtin_amdgcn_raw_buffer_load_b128(rws, so + 1024, 0, AUX_SC1);
        if (!__builtin_amdgcn_ballot_w64(__builtin_amdgcn_bitop3_b32(h[1], h[3], htag, 0x7E) != 0)) break;
        if (POLL_SLEEP) __builtin_amdgcn_s_sleep(POLL_SLEEP);
      }
      STAMP(3);
      if (ROW_WAIT) __builtin_amdgcn_s_sleep(ROW_WAIT);
#pragma unroll
      for (int u = GS - 1; u >= 0; --u) d[u] = __builtin_amdgcn_raw_buffer_load_b128(rws, doff[u], 0, AUX_SC1);
      __builtin_amdgcn_sched_barrier(0);
      const float mu = __uint_as_float(h[0]) + hbias;
      float M = max8(mu);
      M = fmx(fmx(M, dppf<XOR15>(M)), Mr);
      float f = __builtin_amdgcn_exp2f(mu - M), fr = __builtin_amdgcn_exp2f(Mr - M);
      const float l8 = sum8(f * __uint_as_float(h[2]));
      Lr = (l8 + dppf<XOR15>(l8)) + fr * Lr;
      Mr = M;
      if (base + GS >= ncon) {
        const float inv = __builtin_amdgcn_rcpf(Lr);
        f *= inv;
        fr *= inv;
      }
      a0 *= fr;
      a1 *= fr;
      float fu[GS], b0 = 0.f, b1 = 0.f;
#pragma unroll
      for (int u = 0; u < GS; ++u) {
        fu[u] = rdlane(f, u);
        asm volatile("" : "+s"(fu[u]));
      }
      asm volatile("s_nop 1" : "+v"(a0), "+v"(a1), "+v"(b0), "+v"(b1));
#pragma unroll 1
      for (; spin < SPIN_CAP; ++spin) {
        unsigned bad = 0;
#pragma unroll
        for (int u = GS - 1; u >= 0; --u) bad |= __builtin_amdgcn_bitop3_b32(d[u][1], d[u][3], etag[u], 0x7E);
        if (!__builtin_amdgcn_ballot_w64(bad != 0)) break;
        asm volatile("" ::: "memory");
#pragma unroll
        for (int u = GS - 1; u >= 0; --u) d[u] = __builtin_amdgcn_raw_buffer_load_b128(rws, doff[u], 0, AUX_SC1);
      }
#pragma unroll
      for (int u = 0; u < GS; u += 2)
        asm("v_fmac_f32 %0, %4, %6\n\tv_fmac_f32 %1, %4, %7\n\tv_fmac_f32 %2, %5, %8\n\tv_fmac_f32 %3, %5, %9"
            : "+v"(a0), "+v"(a1), "+v"(b0), "+v"(b1)
            : "s"(fu[u]), "s"(fu[u + 1]), "v"(d[u][0]), "v"(d[u][2]), "v"(d[u + 1][0]), "v"(d[u + 1][2]));
      a0 += b0;
      a1 += b1;
    }
    const unsigned pk = cvtpk(a0, a1), pp = dppu<0x128>(pk);
    __builtin_amdgcn_raw_buffer_store_b32(__builtin_amdgcn_perm(pk, pp, oddi ? 0x07060302u : 0x01000504u), rout, orow, 0, OUT_AUX);
    __builtin_amdgcn_raw_buffer_store_b32(tag, rws, lane ? OOB : 0, part * 4, AUX_SC1);
    if (CODE_PF) asm volatile(".Lcode_end:");
  }
  STAMP(7);
  STAMP_FLUSH;
}

}

extern "C" void kernel_launch(void* const* d_in, const int* in_sizes, int n_in, void* d_out, int out_size, void* d_ws,
                              size_t ws_size, hipStream_t stream) {
  (void)in_sizes; (void)n_in; (void)out_size; (void)ws_size;
  (void)hipFuncSetAttribute((const void*)attn_fused, hipFuncAttributeMaxDynamicSharedMemorySize, LDS_BYTES);
  hipLaunchKernelGGL(attn_fused, dim3(NKVH * NPART), dim3(NTHR), LDS_BYTES, stream, (const char*)d_in[0], (const char*)d_in[1],
                     (const char*)d_in[2], (const int*)d_in[3], (const int*)d_in[4], (char*)d_ws, (__hip_bfloat16*)d_out, in_sizes[1] / (PAGE_BYTES / 2));
}
```

```cpp
#include <hip/hip_runtime.h>
#include <hip/hip_bf16.h>
#include <stdint.h>

namespace {

constexpr int NBATCH = 8, NH = 32, NKVH = 8, HD = 128, PAGE = 16, NBLK = 512, LK = NBLK * PAGE;
constexpr int NPART = 32, NWAVE = 8, NTHR = NWAVE * 64, NCOL = 32;
constexpr int PACE = 500;
constexpr int WSH = 17;
constexpr int LEAD = 170;
constexpr int LEAD2 = 25;
constexpr int ENDN = 6;
constexpr int ADV_LAT = 117;
constexpr int ADV_SLOW = 25;
constexpr int ADV_GAIN = 9;
constexpr int ADV_UP = 160, ADV_DOWN = 40, ADV_LINE = 128;
constexpr int POLL_AT = -20;
constexpr int PF_N = 2;
constexpr int TOK_BYTES = NKVH * HD * 2;
constexpr int PAGE_BYTES = PAGE * TOK_BYTES;
constexpr int PAGE_SPAN = (PAGE - 1) * TOK_BYTES + HD * 2;
constexpr float SC_LOG2 = 0.08838834764831845f * 1.4426950408889634f;
constexpr int AUX_NT = 2, AUX_SC1 = 16;
constexpr unsigned SPIN_CAP = 1u << 20;
constexpr int OOB = 0x70000000;
constexpr int GS = 12;

typedef __attribute__((ext_vector_type(8))) __bf16 bf16x8;
typedef __attribute__((ext_vector_type(2))) __bf16 bf16x2;
typedef __attribute__((ext_vector_type(4))) short s16x4;
typedef __attribute__((ext_vector_type(4))) float f32x4;
typedef __attribute__((ext_vector_type(2))) float f32x2;
typedef __attribute__((ext_vector_type(4))) unsigned u32x4;
typedef __attribute__((ext_vector_type(2))) unsigned u32x2;
typedef __attribute__((ext_vector_type(8))) int i32x8;
extern "C" __device__ int llvm_writelane(int, int, int) __asm("llvm.amdgcn.writelane.i32");

constexpr int COL_F = NWAVE * HD + 4;
constexpr int LDS_TBL = 0;
constexpr int LDS_PW = LDS_TBL + NBLK * NBATCH * 4;
constexpr int CW_PREFIX = LDS_PW;
constexpr int CW_Q = LDS_PW + 16;
constexpr int CW_ORIGIN = LDS_PW + 20;
constexpr int CW_STATS = LDS_PW + 24;
constexpr int CW_ROWS = LDS_PW + 32;
constexpr int CW_LAT = LDS_PW + 40;
constexpr int CW_ADV = LDS_PW + 48;
constexpr int LDS_EP = LDS_PW + 64;
constexpr int LDS_Q = LDS_EP + 256;
constexpr int LDS_ML = LDS_Q + 8192;
constexpr int LDS_O = LDS_ML + NCOL * 40 * 4;
constexpr int LDS_BYTES = LDS_O + NCOL * COL_F * 4;

constexpr int WS_HEAD = 2 << 20, WS_SLOT0 = 1024, SLOT_BYTES = 1152;
constexpr int WS_BYTES = WS_SLOT0 + NCOL * NPART * SLOT_BYTES;

__device__ __forceinline__ f32x4 mfma32(u32x4 a, u32x4 b, f32x4 c) {
  return __builtin_amdgcn_mfma_f32_16x16x32_bf16(__builtin_bit_cast(bf16x8, a), __builtin_bit_cast(bf16x8, b), c, 0, 0, 0);
}
__device__ __forceinline__ f32x4 mfma16(u32x2 a, u32x2 b, f32x4 c) {
  return __builtin_amdgcn_mfma_f32_16x16x16bf16_1k(__builtin_bit_cast(s16x4, a), __builtin_bit_cast(s16x4, b), c, 0, 0, 0);
}
__device__ __forceinline__ unsigned cvtpk(float lo, float hi) {
  return __builtin_bit_cast(unsigned, __builtin_convertvector(f32x2{lo, hi}, bf16x2));
}
__device__ __forceinline__ float fmx(float a, float b) { return __builtin_elementwise_maximum(a, b); }
__device__ __forceinline__ float swap16_max(float x) {
  auto s = __builtin_amdgcn_permlane16_swap(__float_as_uint(x), __float_as_uint(x), false, false);
  return fmx(__uint_as_float(s[0]), __uint_as_float(s[1]));
}
__device__ __forceinline__ float swap32_max(float x) {
  auto s = __builtin_amdgcn_permlane32_swap(__float_as_uint(x), __float_as_uint(x), false, false);
  return fmx(__uint_as_float(s[0]), __uint_as_float(s[1]));
}
template <int CTRL> __device__ __forceinline__ unsigned dppu(unsigned x) {
  return (unsigned)__builtin_amdgcn_mov_dpp((int)x, CTRL, 0xf, 0xf, true);
}
template <int CTRL> __device__ __forceinline__ float dppf(float x) { return __uint_as_float(dppu<CTRL>(__float_as_uint(x))); }
constexpr int XOR1 = 0xB1, XOR2 = 0x4E, XOR3 = 0x1B, XOR7 = 0x141, XOR15 = 0x140;
__device__ __forceinline__ float max8(float x) {
  x = fmx(x, dppf<XOR7>(x));
  return fmx(fmx(fmx(x, dppf<XOR1>(x)), dppf<XOR2>(x)), dppf<XOR3>(x));
}
__device__ __forceinline__ float sum8(float x) {
  x += dppf<XOR1>(x); x += dppf<XOR2>(x); return x + dppf<XOR7>(x);
}
__device__ __forceinline__ float rdlane(float x, int l) { return __int_as_float(__builtin_amdgcn_readlane(__float_as_int(x), l)); }
__device__ __forceinline__ int clamp_len(int l) { return (l <= 0 || l > LK) ? LK : l; }
__device__ __forceinline__ unsigned now() { return (unsigned)__builtin_amdgcn_s_memrealtime(); }
__device__ __forceinline__ f32x2 wsum8(const f32x2 (&o)[8], float f, int l0) {
  float s[8];
#pragma unroll
  for (int w = 0; w < 8; ++w) s[w] = rdlane(f, l0 + w);
  asm volatile("" : "+s"(s[0]), "+s"(s[1]), "+s"(s[2]), "+s"(s[3]), "+s"(s[4]), "+s"(s[5]), "+s"(s[6]), "+s"(s[7]));
  const f32x2 a = o[0] * s[0] + o[2] * s[2] + o[4] * s[4] + o[6] * s[6];
  const f32x2 b = o[1] * s[1] + o[3] * s[3] + o[5] * s[5] + o[7] * s[7];
  return a + b;
}

#define CND4(D, A, B, P)                                                                        \
  "v_cndmask_b32_dpp %[" D "0], %[" A "0], %[" B "0], vcc " P " row_mask:0xf bank_mask:0xf\n\t" \
  "v_cndmask_b32_dpp %[" D "1], %[" A "1], %[" B "1], vcc " P " row_mask:0xf bank_mask:0xf\n\t" \
  "v_cndmask_b32_dpp %[" D "2], %[" A "2], %[" B "2], vcc " P " row_mask:0xf bank_mask:0xf\n\t" \
  "v_cndmask_b32_dpp %[" D "3], %[" A "3], %[" B "3], vcc " P " row_mask:0xf bank_mask:0xf\n\t"
#define SWAP_STAGE(MASK, P)                                                                                              \
  asm("s_mov_b32 vcc_lo, " MASK "\n\ts_mov_b32 vcc_hi, " MASK "\n\t" CND4("x", "b", "a", P) "s_not_b64 vcc, vcc\n\t" CND4("y", "a", "b", P) \
      "s_nop 1"                                                                                                          \
      : [x0] "=&v"(x[0]), [x1] "=&v"(x[1]), [x2] "=&v"(x[2]), [x3] "=&v"(x[3]), [y0] "=&v"(y[0]), [y1] "=&v"(y[1]),      \
        [y2] "=&v"(y[2]), [y3] "=&v"(y[3])                                                                               \
      : [a0] "v"(a[0]), [a1] "v"(a[1]), [a2] "v"(a[2]), [a3] "v"(a[3]), [b0] "v"(b[0]), [b1] "v"(b[1]), [b2] "v"(b[2]),  \
        [b3] "v"(b[3])                                                                                                   \
      : "vcc", "scc")
__device__ __forceinline__ void swap1(const u32x4& a, const u32x4& b, u32x4& ox, u32x4& oy) {
  unsigned x[4], y[4];
  SWAP_STAGE("0x55555555", "quad_perm:[1,0,3,2]");
  ox = u32x4{x[0], x[1], x[2], x[3]};
  oy = u32x4{y[0], y[1], y[2], y[3]};
}
__device__ __forceinline__ void swap2(const u32x4& a, const u32x4& b, u32x4& ox, u32x4& oy) {
  unsigned x[4], y[4];
  SWAP_STAGE("0x33333333", "quad_perm:[2,3,0,1]");
  ox = u32x4{x[0], x[1], x[2], x[3]};
  oy = u32x4{y[0], y[1], y[2], y[3]};
}
__device__ __forceinline__ void quad_transpose(const u32x4 (&in)[4], u32x4 (&out)[4]) {
  u32x4 t[4];
  swap1(in[0], in[1], t[0], t[1]);
  swap1(in[2], in[3], t[2], t[3]);
  swap2(t[0], t[2], out[0], out[2]);
  swap2(t[1], t[3], out[1], out[3]);
}

struct Page {
  u32x4 k[4];
  u32x4 v[4];
};

__global__ __launch_bounds__(NTHR, 2) void attn_fused(const char* __restrict__ q, const char* __restrict__ kc,
                                                      const char* __restrict__ vc, const int* __restrict__ bt,
                                                      const int* __restrict__ lens, char* __restrict__ ws,
                                                      __hip_bfloat16* __restrict__ out, int nphys) {
  extern __shared__ __attribute__((aligned(16))) char smem[];
  const int tid = threadIdx.x, lane = tid & 63, wave = __builtin_amdgcn_readfirstlane(tid >> 6);
  if (tid < 16) ((unsigned*)(smem + LDS_PW))[tid] = 0u;
  unsigned warm0 = 0;
  if (wave == 0) {
    unsigned long long pc;
    unsigned len;
    asm volatile("s_getpc_b64 %0\n.Lcode_begin:\n\ts_mov_b32 %1, .Lcode_end-.Lcode_begin" : "=s"(pc), "=s"(len));
    const auto rc = __builtin_amdgcn_make_buffer_rsrc((void*)pc, 0, max((int)len, 0), 0x00020000);
    warm0 = __builtin_amdgcn_raw_buffer_load_b32(rc, (lane * 8 + ((blockIdx.x >> 3) & 7)) * 128, 0, 0);
  }
  asm volatile("s_waitcnt lgkmcnt(0)" :: "s"(q), "s"(kc), "s"(vc), "s"(bt), "s"(lens), "s"(ws), "s"(out), "s"(nphys) : "memory");
  const i32x8 slen = *(const __attribute__((address_space(4))) i32x8*)(uintptr_t)lens;
  const int g = lane >> 4, c = lane & 15;
  const int kvh = blockIdx.x & 7, part = blockIdx.x >> 3;

  u32x2 y[NBATCH];
  const bool btw = (unsigned)(wave - 2) < 4u;
  const int bcol = (tid - 128) * 8;
  if (btw) {
    const auto rbt = __builtin_amdgcn_make_buffer_rsrc((void*)bt, 0, NBATCH * NBLK * 4, 0x00020000);
#pragma unroll
    for (int b = 0; b < NBATCH; ++b) y[b] = __builtin_amdgcn_raw_buffer_load_b64(rbt, bcol + (b & 1) * 2048, (b >> 1) * 4096, 0);
  }
  asm volatile("s_barrier" ::: "memory");
  const auto rws = __builtin_amdgcn_make_buffer_rsrc((void*)(ws + (size_t)kvh * WS_HEAD), 0, WS_BYTES, 0x00020000);
  const auto rout = __builtin_amdgcn_make_buffer_rsrc((void*)out, 0, NBATCH * NH * HD * 2, 0x00020000);
  const char* kbase = kc + kvh * (HD * 2);
  const char* vbase = vc + kvh * (HD * 2);
  int koff = ((c >> 2) * 4) * TOK_BYTES + (4 * g + (c & 3)) * 16;
  int voff = 4 * g * TOK_BYTES + c * 16;
  asm volatile("" : "+v"(koff), "+v"(voff), "+s"(kbase), "+s"(vbase));
  if (btw) {
#pragma unroll
    for (int b = 0; b < NBATCH; ++b) *(u32x2*)(smem + LDS_TBL + b * (NBLK * 4) + bcol) = y[b];
    unsigned d0 = y[0][0] ^ y[1][0], d1 = y[0][1] ^ y[1][1];
#pragma unroll
    for (int b = 2; b < NBATCH; ++b) {
      d0 = __builtin_amdgcn_bitop3_b32(d0, y[0][0], y[b][0], 0xF6);
      d1 = __builtin_amdgcn_bitop3_b32(d1, y[0][1], y[b][1], 0xF6);
    }
    unsigned f0, f1;
    asm("s_ff1_i32_b64 %0, %1" : "=s"(f0) : "s"(__builtin_amdgcn_ballot_w64(d0 != 0)));
    asm("s_ff1_i32_b64 %0, %1" : "=s"(f1) : "s"(__builtin_amdgcn_ballot_w64(d1 != 0)));
    asm volatile("" ::: "memory");
    ((unsigned*)(smem + CW_PREFIX))[wave - 2] = (((wave - 2) * 128) | min(2 * f0, 2 * f1 + 1)) + 1;
  }
  if (wave == 6) {
    const auto rq = __builtin_amdgcn_make_buffer_rsrc((void*)(q + 4 * kvh * (HD * 2)), 0, NBATCH * NH * HD * 2 - 4 * kvh * (HD * 2), 0x00020000);
    u32x4 qs[NBATCH];
#pragma unroll
    for (int b = 0; b < NBATCH; ++b) qs[b] = __builtin_amdgcn_raw_buffer_load_b128(rq, lane * 16, b * (NH * HD * 2), 0);
    const unsigned e = __builtin_amdgcn_raw_buffer_load_b32(rws, lane < 32 ? lane * 4 : WS_SLOT0 + (part * NPART + lane - 32) * SLOT_BYTES + 1024, 0, AUX_SC1);
#pragma unroll
    for (int b = 0; b < NBATCH; ++b) *(u32x4*)(smem + LDS_Q + (((b >> 2) * 4 + (c & 3)) * 64 + (c >> 2) * 16 + (b & 3) * 4 + g) * 16) = qs[b];
    *(unsigned*)(smem + LDS_EP + lane * 4) = e;
    asm volatile("" ::: "memory");
    *(unsigned*)(smem + CW_Q) = 1u;
  }
  if (wave == 7) {
    static_assert(PF_N == 2, "32 lanes per page");
    const auto rk = __builtin_amdgcn_make_buffer_rsrc((void*)kc, 0, (unsigned)nphys * PAGE_BYTES, 0x00020000);
    const auto rv = __builtin_amdgcn_make_buffer_rsrc((void*)vc, 0, (unsigned)nphys * PAGE_BYTES, 0x00020000);
    const unsigned page = (unsigned)(((part * PF_N + (lane >> 5)) * 2 + 1) * nphys) / (2 * NPART * PF_N);
    const unsigned off = page * PAGE_BYTES + ((lane >> 1) & 15) * TOK_BYTES + kvh * (HD * 2) + (lane & 1) * 128;
    const unsigned wk = __builtin_amdgcn_raw_buffer_load_b32(rk, off, 0, 0), wv = __builtin_amdgcn_raw_buffer_load_b32(rv, off, 0, 0);
    asm volatile("" :: "v"(wk), "v"(wv));
  }
  asm volatile("" :: "v"(warm0));
  __builtin_amdgcn_s_waitcnt(0x0F70);
  int lraw = 1;
#pragma unroll
  for (int b = 0; b < NBATCH; ++b) lraw = llvm_writelane(slen[b], b, lraw);
  const int npg8 = (clamp_len(lraw) + PAGE - 1) >> 4;
  int nmax = max(npg8, (int)dppu<XOR1>(npg8));
  nmax = max(nmax, (int)dppu<XOR2>(nmax));
  nmax = __builtin_amdgcn_readfirstlane(max(nmax, (int)dppu<XOR7>(nmax)));
  int npg = lane < NBATCH ? npg8 : (1 << 20);
  asm volatile("" : "+v"(npg), "+s"(nmax));

  unsigned p1;
  do {
    asm volatile("" ::: "memory");
    const u32x4 a = *(const u32x4*)(smem + CW_PREFIX);
    p1 = __builtin_amdgcn_readfirstlane(min(min(a[0], a[1]), min(a[2], a[3])));
  } while (!p1);
  asm volatile("" ::: "memory");
  const int P = __builtin_amdgcn_readfirstlane((int)(p1 - 1 < (unsigned)nmax ? p1 - 1 : (unsigned)nmax));
  const int cnt = max(npg - P, 0);
  int send = cnt;
  send += (int)dppu<0x111>(send);
  send += (int)dppu<0x112>(send);
  send += (int)dppu<0x114>(send);
  const int npriv = __builtin_amdgcn_readlane(send, NBATCH - 1);
  int bnd;
  {
    const unsigned x = (unsigned)(npriv * 16 + P * WSH) * (unsigned)lane;
    bnd = max((int)__umulhi(x, (unsigned)((1ull << 27) / WSH)), ((int)(x >> 5) - P * (WSH - 16)) >> 4);
  }
  const int i0 = __builtin_amdgcn_readlane(bnd, part), i1 = __builtin_amdgcn_readlane(bnd, part + 1);
  const int nitem = i1 - i0, nbase = nitem >> 3, nextra = nitem & 7;
  const int nw = nbase + (wave < nextra ? 1 : 0);
  const int soff = lane * NBLK - (send - cnt);
  const int idx0 = i0 + nbase * (wave & ~1) + min(wave & ~1, nextra);
  const int item = idx0 + 2 * lane + (wave & 1);
  int tix = 0;
  for (int cur = max(idx0 - P, 0); cur < min(idx0 + 2 * nw, i1) - P;) {
    const int s = __popcll(__builtin_amdgcn_ballot_w64(send <= cur));
    const int so = __builtin_amdgcn_readlane(soff, s);
    tix = item - P >= cur ? so : tix;
    cur = __builtin_amdgcn_readlane(send, s);
  }
  tix = (tix + item) & (NBATCH * NBLK - 1);
  const unsigned tstart = now();
  const int phys = *(const int*)(smem + LDS_TBL + tix * 4);
  unsigned tpace;
  int dpace, nfast, dcut;
  Page pg;
  auto issue = [&](int k) {
    const size_t off = (size_t)(unsigned)__builtin_amdgcn_readlane(phys, k) * PAGE_BYTES;
    const auto rk = __builtin_amdgcn_make_buffer_rsrc((void*)(kbase + off), 0, PAGE_SPAN, 0x00020000);
    const auto rv = __builtin_amdgcn_make_buffer_rsrc((void*)(vbase + off), 0, PAGE_SPAN, 0x00020000);
#pragma unroll
    for (int i = 0; i < 4; ++i) pg.k[i] = __builtin_amdgcn_raw_buffer_load_b128(rk, koff + (i & 1) * TOK_BYTES, (i >> 1) * 2 * TOK_BYTES, AUX_NT);
#pragma unroll
    for (int j = 0; j < 4; ++j) pg.v[j] = __builtin_amdgcn_raw_buffer_load_b128(rv, voff + (j & 1) * TOK_BYTES, (j >> 1) * 2 * TOK_BYTES, AUX_NT);
    __builtin_amdgcn_sched_barrier(0);
  };
  unsigned tiss = 0, tarr = 0;
  const int lat8 = 8 * (ADV_LAT + ((((uintptr_t)kbase >> 8) & 3) == 1 ? ADV_SLOW : 0));
  auto pace = [&](int k) {
    int adv = 0;
    if (k > 0) {
      asm volatile("" ::: "memory");
      *(unsigned char*)(smem + CW_LAT + wave) = (unsigned char)min(tarr - tiss, 200u);
      asm volatile("" ::: "memory");
      const u32x2 lt = *(const u32x2*)(smem + CW_LAT);
      adv = __builtin_amdgcn_readfirstlane(*(const int*)(smem + CW_ADV));
      const int sum = __builtin_amdgcn_readfirstlane(__builtin_amdgcn_sad_u8(lt[0], 0, __builtin_amdgcn_sad_u8(lt[1], 0, 0)));
      const int step = min(max((lat8 - sum) * ADV_GAIN, -ADV_DOWN * 16), ADV_UP * 16);
      adv = min(max(adv + ((dpace * step) >> 14), 0), (k * 8 + wave - 6) * ((dpace * ADV_LINE) >> 10));
      *(int*)(smem + CW_ADV) = adv;
    }
    const unsigned tg = tpace + ((k * 8 * dpace - max(k * 8 - nfast, 0) * dcut - adv) >> 8);
    while ((int)((tiss = now()) - tg) < 0) __builtin_amdgcn_s_sleep(1);
  };
  if (wave == 0) *(unsigned*)(smem + CW_ORIGIN) = tstart | 1u;
  if (nw > 0 && wave < 2) issue(0);

  unsigned contrib;
  {
    const int s = part >> 2;
    const int e = __builtin_amdgcn_readlane(send, s) + P, n = __builtin_amdgcn_readlane(cnt, s), ps = min(P, __builtin_amdgcn_readlane(npg, s));
    const int nxt = __builtin_amdgcn_ds_bpermute((lane + 1) * 4, bnd);
    contrib = (unsigned)__builtin_amdgcn_ballot_w64(nxt > bnd && (bnd < ps || (n > 0 && bnd < e && nxt > e - n)));
  }
  const unsigned um = nitem > 0 ? (unsigned)__builtin_amdgcn_ballot_w64(i0 < min(P, npg) || (cnt > 0 && i0 - P < send && i1 - P > send - cnt)) & 0xffu : 0u;
  const int nseq = __popc(um);
  const bool wide = nseq > 2;
  {
    const float span = (float)(npriv * 16 + P * WSH) * (PACE * (1.f / 32.f)) - (wide ? LEAD * 256.f : nseq == 2 ? LEAD2 * 256.f : 0.f);
    dpace = __builtin_amdgcn_readfirstlane((int)(fmaxf(span, 0.f) * __builtin_amdgcn_rcpf((float)max(nitem, 1))));
    nfast = nitem - ENDN - wave;
    dcut = wide ? 0 : __builtin_amdgcn_readfirstlane((int)((float)dpace * (float)(P * WSH) * __builtin_amdgcn_rcpf((float)(npriv * 16 + P * WSH))));
  }
  do {
    asm volatile("" ::: "memory");
    tpace = __builtin_amdgcn_readfirstlane(*(const unsigned*)(smem + CW_ORIGIN));
  } while (!tpace);
  const unsigned tend = tpace + (unsigned)(((npriv * 16 + P * WSH) >> 5) * PACE >> 8);
  tpace += (wave - 1) * dpace >> 8;
  if (nw > 0 && wave >= 2) {
    pace(0);
    issue(0);
  }

  while (__hip_atomic_load((unsigned*)(smem + CW_Q), __ATOMIC_RELAXED, __HIP_MEMORY_SCOPE_WORKGROUP) == 0u) __builtin_amdgcn_s_sleep(1);
  asm volatile("" ::: "memory");
  const unsigned epoch = *(const unsigned*)(smem + LDS_EP + lane * 4);
  int lenv[2];
  u32x4 qf[2][4];
#pragma unroll
  for (int cg = 0; cg < 2; ++cg) {
    const int l = __builtin_amdgcn_ds_bpermute((4 * cg + (c >> 2)) * 4, lraw);
#pragma unroll
    for (int i = 0; i < 4; ++i) qf[cg][i] = *(const u32x4*)(smem + LDS_Q + ((cg * 4 + i) * 64 + lane) * 16);
    if (l <= 0) {
#pragma unroll
      for (int i = 0; i < 4; ++i) qf[cg][i] = u32x4{0, 0, 0, 0};
    }
    lenv[cg] = clamp_len(l) - 4 * g;
  }
  const unsigned meta = (unsigned)(tix & (NBLK - 1)) * PAGE | (item < P ? 0xff0000u : 0x10000u << (tix >> 9));
  const int rk = (wave - nextra) & 7;
  int ncol = wide ? (nseq + 1 - (rk >> 2)) >> 1 : (unsigned)(rk - 1) < 4u ? nseq : 0;
  int mrd, lrd, pcol, phdr;
  unsigned ctv;
  {
    const int u = (lane >> 3) & 3;
    const int rank = ((um >> (lane & 7)) & 1) ? __popc(um & ((1u << (lane & 7)) - 1)) : -1;
    const unsigned long long hit = __builtin_amdgcn_ballot_w64(rank == (wide ? 2 * u + (rk >> 2) : u));
    const unsigned mine8 = (unsigned)(hit >> (8 * u)) & 0xffu;
    const bool on = mine8 != 0 && lane < 32;
    const int col = 4 * __builtin_ctz(mine8 | 0x80u) + (rk & 3);
    mrd = LDS_ML + (col * 40 + 32 + (lane & 7)) * 4;
    lrd = LDS_ML + (col * 40 + (lane & 7) * 4) * 4;
    pcol = col;
    const int slot = WS_SLOT0 + (col * NPART + part) * SLOT_BYTES;
    phdr = on ? slot + 1024 + (lane & 7) * 16 : OOB;
    ctv = (unsigned)__builtin_amdgcn_ds_bpermute(col * 4, (int)epoch) + 1;
  }
  int phdr0 = wide || lane >= 8 ? OOB : phdr;
  int ord = LDS_O + (__builtin_amdgcn_readfirstlane(pcol) * COL_F + 2 * lane) * 4;
  int prow = WS_SLOT0 + (__builtin_amdgcn_readfirstlane(pcol) * NPART + part) * SLOT_BYTES + lane * 16;
  unsigned ct = __builtin_amdgcn_readfirstlane(ctv);
  asm volatile("" : "+v"(mrd), "+v"(lrd), "+v"(ord), "+v"(prow), "+v"(ct), "+v"(phdr0), "+v"(phdr), "+v"(ctv), "+v"(pcol), "+s"(ncol));
  int wm = LDS_ML + (c * 40 + 32 + rk) * 4, wl = LDS_ML + (c * 40 + rk * 4 + g) * 4, wo[2];
#pragma unroll
  for (int cg = 0; cg < 2; ++cg) wo[cg] = ((um >> (4 * cg + (c >> 2))) & 1) ? LDS_O + ((16 * cg + c) * COL_F + rk * HD + 4 * g) * 4 : -1;
  asm volatile("" : "+v"(wm), "+v"(wl), "+v"(wo[0]), "+v"(wo[1]));

  float m[2] = {-INFINITY, -INFINITY}, ms[2] = {0.f, 0.f}, l[2] = {0.f, 0.f};
  f32x4 acc[2][8];
#pragma unroll
  for (int cg = 0; cg < 2; ++cg)
#pragma unroll
    for (int i = 0; i < 8; ++i) acc[cg][i] = f32x4{0.f, 0.f, 0.f, 0.f};
  __builtin_amdgcn_s_setprio(2);

  auto put_stats = [&]() {
#pragma unroll
    for (int cg = 0; cg < 2; ++cg) {
      *(float*)(smem + wm + cg * (16 * 160)) = m[cg] * SC_LOG2;
      *(float*)(smem + wl + cg * (16 * 160)) = l[cg];
    }
    asm volatile("" ::: "memory");
    *(unsigned char*)(smem + CW_STATS + rk) = 1;
  };
  if (nw == 0) put_stats();
  for (int k = 0; k < nw;) {
    u32x4 kt[4];
    u32x2 va[8];
    quad_transpose(pg.k, kt);
    asm volatile("" :: "v"(kt[0]));
    tarr = now();
    const unsigned mt = (unsigned)__builtin_amdgcn_readlane((int)meta, k);
    ++k;
    u32x2 pb[2];
#pragma unroll
    for (int cg = 0; cg < 2; ++cg) {
      if ((mt >> (16 + 4 * cg)) & 0xfu) {
        f32x4 s = {0.f, 0.f, 0.f, 0.f};
#pragma unroll
        for (int i = 0; i < 4; ++i) s = mfma32(kt[i], qf[cg][i], s);
        const int lim = ((mt >> (16 + 4 * cg + (c >> 2))) & 1) ? lenv[cg] - (int)(mt & 0xffffu) : 0;
        unsigned long long live[4];
#pragma unroll
        for (int r = 0; r < 4; ++r) live[r] = __builtin_amdgcn_ballot_w64(r < lim);
        asm volatile("" : "+s"(live[0]), "+s"(live[1]), "+s"(live[2]), "+s"(live[3]));
        float x[4];
#pragma unroll
        for (int r = 0; r < 4; ++r) x[r] = __builtin_amdgcn_inverse_ballot_w64(live[r]) ? s[r] : -INFINITY;
        const float mx = swap32_max(swap16_max(fmx(fmx(x[0], x[1]), fmx(x[2], x[3]))));
        if (__builtin_amdgcn_ballot_w64(mx > m[cg])) {
          m[cg] = fmx(m[cg], mx);
          const float mn = m[cg] == -INFINITY ? 0.f : m[cg] * SC_LOG2;
          const float alpha = __builtin_amdgcn_exp2f(fminf(ms[cg] - mn, 0.f));
          ms[cg] = mn;
          l[cg] *= alpha;
#pragma unroll
          for (int i = 0; i < 8; ++i) acc[cg][i] *= alpha;
        }
#pragma unroll
        for (int r = 0; r < 4; ++r) x[r] = __builtin_amdgcn_exp2f(__builtin_fmaf(x[r], SC_LOG2, -ms[cg]));
        l[cg] += (x[0] + x[1]) + (x[2] + x[3]);
        pb[cg] = u32x2{cvtpk(x[0], x[1]), cvtpk(x[2], x[3])};
      }
    }
    if (k == nw) put_stats();
#pragma unroll
    for (int i = 0; i < 8; ++i) {
      const unsigned sel = (i & 1) ? 0x07060302u : 0x05040100u;
      va[i][0] = __builtin_amdgcn_perm(pg.v[1][i >> 1], pg.v[0][i >> 1], sel);
      va[i][1] = __builtin_amdgcn_perm(pg.v[3][i >> 1], pg.v[2][i >> 1], sel);
    }
#pragma unroll
    for (int cg = 0; cg < 2; ++cg) {
      if ((mt >> (16 + 4 * cg)) & 0xfu) {
#pragma unroll
        for (int i = 0; i < 8; ++i) acc[cg][i] = mfma16(va[i], pb[cg], acc[cg][i]);
      }
    }
    if (k < nw) {
      pace(k);
      issue(k);
    }
  }
  __builtin_amdgcn_s_waitcnt(0x0F70);

#pragma unroll
  for (int cg = 0; cg < 2; ++cg) {
    if (wo[cg] >= 0) {
#pragma unroll
      for (int i = 0; i < 8; ++i) *(f32x4*)(smem + wo[cg] + 64 * i) = acc[cg][i];
    }
  }
  asm volatile("" ::: "memory");
  *(unsigned char*)(smem + CW_ROWS + rk) = 1;
  __builtin_amdgcn_s_setprio(0);

  if (ncol > 0) {
    for (;;) {
      asm volatile("" ::: "memory");
      const u32x2 dn = *(const u32x2*)(smem + CW_ROWS);
      if (__builtin_amdgcn_ballot_w64((dn[0] & (dn[1] | 0x01010100u)) == 0x01010101u)) break;
    }
    asm volatile("" ::: "memory");
    f32x2 o[NWAVE];
#pragma unroll
    for (int w = 0; w < 5; ++w) o[w] = *(const f32x2*)(smem + ord + w * (HD * 4));
    float mw;
    f32x4 lw;
    for (;;) {
      asm volatile("" ::: "memory");
      const unsigned dn = *(const unsigned*)(smem + CW_STATS + 4);
      asm volatile("" ::: "memory");
      mw = *(const float*)(smem + mrd);
      lw = *(const f32x4*)(smem + lrd);
      asm volatile("" : "+v"(mw), "+v"(lw));
      if (__builtin_amdgcn_ballot_w64(dn == 0x01010101u)) break;
    }
    const float M = max8(mw);
    const float f = __builtin_amdgcn_exp2f(mw - M);
    const float L = sum8(f * ((lw[0] + lw[1]) + (lw[2] + lw[3])));
    const u32x4 hdr = {__float_as_uint(M), ctv, __float_as_uint(L), ctv};
    __builtin_amdgcn_raw_buffer_store_b128(hdr, rws, phdr0, 0, AUX_SC1);
    float fw[8];
#pragma unroll
    for (int w = 0; w < 8; ++w) fw[w] = rdlane(f, w);
    asm volatile("" : "+s"(fw[0]), "+s"(fw[1]), "+s"(fw[2]), "+s"(fw[3]), "+s"(fw[4]), "+s"(fw[5]), "+s"(fw[6]), "+s"(fw[7]));
    f32x2 a = (o[0] * fw[0] + o[2] * fw[2] + o[4] * fw[4]) + (o[1] * fw[1] + o[3] * fw[3]);
    asm volatile("" : "+v"(a));
    for (;;) {
      asm volatile("" ::: "memory");
      const unsigned dn = *(const unsigned*)(smem + CW_ROWS + 4);
      asm volatile("" ::: "memory");
#pragma unroll
      for (int w = 5; w < NWAVE; ++w) o[w] = *(const f32x2*)(smem + ord + w * (HD * 4));
      asm volatile("" : "+v"(o[5]), "+v"(o[6]), "+v"(o[7]));
      if (__builtin_amdgcn_ballot_w64(dn == 0x01010101u)) break;
    }
    a = (a + o[5] * fw[5]) + (o[6] * fw[6] + o[7] * fw[7]);
    __builtin_amdgcn_raw_buffer_store_b128(u32x4{__float_as_uint(a[0]), ct, __float_as_uint(a[1]), ct}, rws, prow, 0, AUX_SC1);
    const int phdr1 = !wide && lane < 8 ? OOB : phdr;
#pragma unroll 1
    for (int u = 1; u < ncol; ++u) {
      const int col = __builtin_amdgcn_readlane(pcol, 8 * u);
#pragma unroll
      for (int w = 0; w < NWAVE; ++w) o[w] = *(const f32x2*)(smem + LDS_O + (col * COL_F + 2 * lane) * 4 + w * (HD * 4));
      if (u == ncol - 1) __builtin_amdgcn_raw_buffer_store_b128(hdr, rws, phdr1, 0, AUX_SC1);
      ct = __builtin_amdgcn_readlane(ctv, 8 * u);
      a = wsum8(o, f, 8 * u);
      __builtin_amdgcn_raw_buffer_store_b128(u32x4{__float_as_uint(a[0]), ct, __float_as_uint(a[1]), ct}, rws,
                                             WS_SLOT0 + (col * NPART + part) * SLOT_BYTES + lane * 16, 0, AUX_SC1);
    }
    if (wide && ncol == 1) __builtin_amdgcn_raw_buffer_store_b128(hdr, rws, phdr1, 0, AUX_SC1);
  }

  if (rk == 0) {
    const unsigned tag = (unsigned)__builtin_amdgcn_readlane((int)epoch, part) + 1;
    const bool oddi = (lane >> 3) & 1;
    int orow = (((part >> 2) * NH + 4 * kvh + (part & 3)) * HD + ((lane >> 1) & 3) * 32 + (lane & 1) * 16 + (lane >> 4) * 2 + (oddi ? 8 : 0)) * 2;
    float Mr = -3.0e38f, Lr = 0.f, a0 = 0.f, a1 = 0.f;
    unsigned spin = 0;
    const int ncon = __popc(contrib);
    const int crank = lane < 32 && ((contrib >> lane) & 1) ? __popc(contrib & ((1u << lane) - 1)) : 63;
    const int cpart = __builtin_amdgcn_ds_permute(crank * 4, lane);
    for (int base = 0; base < ncon; base += GS) {
      const int u16 = lane & 15;
      const bool hon = u16 < GS && base + u16 < ncon;
      const int sl = __builtin_amdgcn_ds_bpermute((base + u16) * 4, cpart);
      const int so = hon ? WS_SLOT0 + (part * NPART + sl) * SLOT_BYTES : OOB;
      const unsigned htag = hon ? tag : 0u;
      const float hbias = hon ? 0.f : -INFINITY;
      int doff[GS];
      unsigned etag[GS];
#pragma unroll
      for (int u = 0; u < GS; ++u) {
        doff[u] = __builtin_amdgcn_readlane(so, u) + lane * 16;
        etag[u] = __builtin_amdgcn_readlane(htag, u);
        asm volatile("" : "+v"(doff[u]), "+s"(etag[u]));
      }
      asm volatile("" : "+v"(orow));
      u32x4 h, d[GS];
      for (;;) {
        asm volatile("" ::: "memory");
        const int av = __builtin_amdgcn_readfirstlane(*(const int*)(smem + CW_ADV));
        if (wide || (int)(now() - (tend + POLL_AT - (av >> 8))) >= 0) break;
        __builtin_amdgcn_s_sleep(2);
      }
#pragma unroll 1
      for (; spin < SPIN_CAP; ++spin) {
        asm volatile("" ::: "memory");
        h = __builtin_amdgcn_raw_buffer_load_b128(rws, so + 1024, 0, AUX_SC1);
        if (!__builtin_amdgcn_ballot_w64(__builtin_amdgcn_bitop3_b32(h[1], h[3], htag, 0x7E) != 0)) break;
      }
#pragma unroll
      for (int u = GS - 1; u >= 0; --u) d[u] = __builtin_amdgcn_raw_buffer_load_b128(rws, doff[u], 0, AUX_SC1);
      __builtin_amdgcn_sched_barrier(0);
      const float mu = __uint_as_float(h[0]) + hbias;
      float M = max8(mu);
      M = fmx(fmx(M, dppf<XOR15>(M)), Mr);
      float f = __builtin_amdgcn_exp2f(mu - M), fr = __builtin_amdgcn_exp2f(Mr - M);
      const float l8 = sum8(f * __uint_as_float(h[2]));
      Lr = (l8 + dppf<XOR15>(l8)) + fr * Lr;
      Mr = M;
      if (base + GS >= ncon) {
        const float inv = __builtin_amdgcn_rcpf(Lr);
        f *= inv;
        fr *= inv;
      }
      a0 *= fr;
      a1 *= fr;
      float fu[GS], b0 = 0.f, b1 = 0.f;
#pragma unroll
      for (int u = 0; u < GS; ++u) {
        fu[u] = rdlane(f, u);
        asm volatile("" : "+s"(fu[u]));
      }
      asm volatile("s_nop 1" : "+v"(a0), "+v"(a1), "+v"(b0), "+v"(b1));
#pragma unroll 1
      for (; spin < SPIN_CAP; ++spin) {
        unsigned bad = 0;
#pragma unroll
        for (int u = GS - 1; u >= 0; --u) bad |= __builtin_amdgcn_bitop3_b32(d[u][1], d[u][3], etag[u], 0x7E);
        if (!__builtin_amdgcn_ballot_w64(bad != 0)) break;
        asm volatile("" ::: "memory");
#pragma unroll
        for (int u = GS - 1; u >= 0; --u) d[u] = __builtin_amdgcn_raw_buffer_load_b128(rws, doff[u], 0, AUX_SC1);
      }
#pragma unroll
      for (int u = 0; u < GS; u += 2)
        asm("v_fmac_f32 %0, %4, %6\n\tv_fmac_f32 %1, %4, %7\n\tv_fmac_f32 %2, %5, %8\n\tv_fmac_f32 %3, %5, %9"
            : "+v"(a0), "+v"(a1), "+v"(b0), "+v"(b1)
            : "s"(fu[u]), "s"(fu[u + 1]), "v"(d[u][0]), "v"(d[u][2]), "v"(d[u + 1][0]), "v"(d[u + 1][2]));
      a0 += b0;
      a1 += b1;
    }
    const unsigned pk = cvtpk(a0, a1), pp = dppu<0x128>(pk);
    __builtin_amdgcn_raw_buffer_store_b32(__builtin_amdgcn_perm(pk, pp, oddi ? 0x07060302u : 0x01000504u), rout, orow, 0, AUX_SC1);
    __builtin_amdgcn_raw_buffer_store_b32(tag, rws, lane ? OOB : 0, part * 4, AUX_SC1);
    asm volatile(".Lcode_end:");
  }
}

}

extern "C" void kernel_launch(void* const* d_in, const int* in_sizes, int n_in, void* d_out, int out_size, void* d_ws,
                              size_t ws_size, hipStream_t stream) {
  (void)n_in; (void)out_size; (void)ws_size;
  (void)hipFuncSetAttribute((const void*)attn_fused, hipFuncAttributeMaxDynamicSharedMemorySize, LDS_BYTES);
  hipLaunchKernelGGL(attn_fused, dim3(NKVH * NPART), dim3(NTHR), LDS_BYTES, stream, (const char*)d_in[0], (const char*)d_in[1],
                     (const char*)d_in[2], (const int*)d_in[3], (const int*)d_in[4], (char*)d_ws, (__hip_bfloat16*)d_out, in_sizes[1] / (PAGE_BYTES / 2));
}
```

```cpp
#define K_PULL 6
#define K_ADV_LAT 98
#include <hip/hip_runtime.h>
#include <hip/hip_bf16.h>
#include <stdint.h>
#define STAMP(i)
#define STAMP_DECL
#define STAMP_FLUSH
#define K_UP 150
#define K_LINE 115
#define K_ROT -1
#define K_NEG 60
#define K_DOWN 80
#define K_TAIL 0
#define K_WFAR 21
#define K_GW 8
#define K_FLAG 1
#define K_POLL_AT -20
#define K_ADV_GAIN 11
#define K_ADV_SLOW 25
#define K_ENDN 6
#define K_LEAD2 25
#define K_LEAD 170
#define K_WSH 17
#define K_PACE 480

namespace {

constexpr int NBATCH = 8, NH = 32, NKVH = 8, HD = 128, PAGE = 16, NBLK = 512, LK = NBLK * PAGE;
constexpr int NPART = 32, NWAVE = 8, NTHR = NWAVE * 64, NCOL = 32;
constexpr int PACE = K_PACE;
constexpr int WSH = K_WSH;
constexpr int LEAD = K_LEAD;
constexpr int LEAD2 = K_LEAD2;
constexpr int ENDN = K_ENDN;
constexpr int ADV_LAT = K_ADV_LAT;
constexpr int ADV_SLOW = K_ADV_SLOW;
constexpr int ADV_GAIN = K_ADV_GAIN;
constexpr int ADV_UP = K_UP, ADV_DOWN = K_DOWN, ADV_LINE = K_LINE;
constexpr int POLL_AT = K_POLL_AT;
constexpr int PF_N = 2;
constexpr int TOK_BYTES = NKVH * HD * 2;
constexpr int PAGE_BYTES = PAGE * TOK_BYTES;
constexpr int PAGE_SPAN = (PAGE - 1) * TOK_BYTES + HD * 2;
constexpr float SC_LOG2 = 0.08838834764831845f * 1.4426950408889634f;
constexpr int AUX_NT = 2, AUX_SC1 = 16;
constexpr unsigned SPIN_CAP = 1u << 20;
constexpr int OOB = 0x70000000;
constexpr int GS = 12;

typedef __attribute__((ext_vector_type(8))) __bf16 bf16x8;
typedef __attribute__((ext_vector_type(2))) __bf16 bf16x2;
typedef __attribute__((ext_vector_type(4))) float f32x4;
typedef __attribute__((ext_vector_type(2))) float f32x2;
typedef __attribute__((ext_vector_type(4))) unsigned u32x4;
typedef __attribute__((ext_vector_type(2))) unsigned u32x2;
typedef __attribute__((ext_vector_type(8))) int i32x8;
extern "C" __device__ int llvm_writelane(int, int, int) __asm("llvm.amdgcn.writelane.i32");

constexpr int COL_F = NWAVE * HD + 4;
constexpr int LDS_TBL = 0;
constexpr int LDS_PW = LDS_TBL + NBLK * NBATCH * 4;
constexpr int CW_PREFIX = LDS_PW;
constexpr int CW_Q = LDS_PW + 16;
constexpr int CW_ORIGIN = LDS_PW + 20;
constexpr int CW_STATS = LDS_PW + 24;
constexpr int CW_ROWS = LDS_PW + 32;
constexpr int CW_LAT = LDS_PW + 40;
constexpr int CW_ADV = LDS_PW + 48;
constexpr int LDS_EP = LDS_PW + 64;
constexpr int LDS_Q = LDS_EP + 256;
constexpr int LDS_ML = LDS_Q + 8192;
constexpr int LDS_O = LDS_ML + NCOL * 40 * 4;
constexpr int LDS_BYTES = LDS_O + NCOL * COL_F * 4;

constexpr int WS_HEAD = 2 << 20, WS_SLOT0 = 1024, SLOT_BYTES = 1152;
constexpr int WS_BYTES = WS_SLOT0 + NCOL * NPART * SLOT_BYTES;

__device__ __forceinline__ f32x4 mfma32(u32x4 a, u32x4 b, f32x4 c) {
  return __builtin_amdgcn_mfma_f32_16x16x32_bf16(__builtin_bit_cast(bf16x8, a), __builtin_bit_cast(bf16x8, b), c, 0, 0, 0);
}
__device__ __forceinline__ unsigned cvtpk(float lo, float hi) {
  return __builtin_bit_cast(unsigned, __builtin_convertvector(f32x2{lo, hi}, bf16x2));
}
__device__ __forceinline__ float fmx(float a, float b) { return __builtin_elementwise_maximum(a, b); }
__device__ __forceinline__ float swap16_max(float x) {
  auto s = __builtin_amdgcn_permlane16_swap(__float_as_uint(x), __float_as_uint(x), false, false);
  return fmx(__uint_as_float(s[0]), __uint_as_float(s[1]));
}
__device__ __forceinline__ float swap32_max(float x) {
  auto s = __builtin_amdgcn_permlane32_swap(__float_as_uint(x), __float_as_uint(x), false, false);
  return fmx(__uint_as_float(s[0]), __uint_as_float(s[1]));
}
template <int CTRL> __device__ __forceinline__ unsigned dppu(unsigned x) {
  return (unsigned)__builtin_amdgcn_mov_dpp((int)x, CTRL, 0xf, 0xf, true);
}
template <int CTRL> __device__ __forceinline__ float dppf(float x) { return __uint_as_float(dppu<CTRL>(__float_as_uint(x))); }
constexpr int XOR1 = 0xB1, XOR2 = 0x4E, XOR3 = 0x1B, XOR7 = 0x141, XOR15 = 0x140;
__device__ __forceinline__ float max8(float x) {
  x = fmx(x, dppf<XOR7>(x));
  return fmx(fmx(fmx(x, dppf<XOR1>(x)), dppf<XOR2>(x)), dppf<XOR3>(x));
}
__device__ __forceinline__ float sum8(float x) {
  x += dppf<XOR1>(x); x += dppf<XOR2>(x); return x + dppf<XOR7>(x);
}
__device__ __forceinline__ float rdlane(float x, int l) { return __int_as_float(__builtin_amdgcn_readlane(__float_as_int(x), l)); }
__device__ __forceinline__ int clamp_len(int l) { return (l <= 0 || l > LK) ? LK : l; }
__device__ __forceinline__ unsigned now() { return (unsigned)__builtin_amdgcn_s_memrealtime(); }
__device__ __forceinline__ f32x2 wsum8(const f32x2 (&o)[8], float f, int l0) {
  float s[8];
#pragma unroll
  for (int w = 0; w < 8; ++w) s[w] = rdlane(f, l0 + w);
  asm volatile("" : "+s"(s[0]), "+s"(s[1]), "+s"(s[2]), "+s"(s[3]), "+s"(s[4]), "+s"(s[5]), "+s"(s[6]), "+s"(s[7]));
  const f32x2 a = o[0] * s[0] + o[2] * s[2] + o[4] * s[4] + o[6] * s[6];
  const f32x2 b = o[1] * s[1] + o[3] * s[3] + o[5] * s[5] + o[7] * s[7];
  return a + b;
}

#define CND4(D, A, B, P)                                                                        \
  "v_cndmask_b32_dpp %[" D "0], %[" A "0], %[" B "0], vcc " P " row_mask:0xf bank_mask:0xf\n\t" \
  "v_cndmask_b32_dpp %[" D "1], %[" A "1], %[" B "1], vcc " P " row_mask:0xf bank_mask:0xf\n\t" \
  "v_cndmask_b32_dpp %[" D "2], %[" A "2], %[" B "2], vcc " P " row_mask:0xf bank_mask:0xf\n\t" \
  "v_cndmask_b32_dpp %[" D "3], %[" A "3], %[" B "3], vcc " P " row_mask:0xf bank_mask:0xf\n\t"
#define SWAP_STAGE(MASK, P)                                                                                              \
  asm("s_mov_b32 vcc_lo, " MASK "\n\ts_mov_b32 vcc_hi, " MASK "\n\t" CND4("x", "b", "a", P) "s_not_b64 vcc, vcc\n\t" CND4("y", "a", "b", P) \
      "s_nop 1"                                                                                                          \
      : [x0] "=&v"(x[0]), [x1] "=&v"(x[1]), [x2] "=&v"(x[2]), [x3] "=&v"(x[3]), [y0] "=&v"(y[0]), [y1] "=&v"(y[1]),      \
        [y2] "=&v"(y[2]), [y3] "=&v"(y[3])                                                                               \
      : [a0] "v"(a[0]), [a1] "v"(a[1]), [a2] "v"(a[2]), [a3] "v"(a[3]), [b0] "v"(b[0]), [b1] "v"(b[1]), [b2] "v"(b[2]),  \
        [b3] "v"(b[3])                                                                                                   \
      : "vcc", "scc")
__device__ __forceinline__ void swap1(const u32x4& a, const u32x4& b, u32x4& ox, u32x4& oy) {
  unsigned x[4], y[4];
  SWAP_STAGE("0x55555555", "quad_perm:[1,0,3,2]");
  ox = u32x4{x[0], x[1], x[2], x[3]};
  oy = u32x4{y[0], y[1], y[2], y[3]};
}
__device__ __forceinline__ void swap2(const u32x4& a, const u32x4& b, u32x4& ox, u32x4& oy) {
  unsigned x[4], y[4];
  SWAP_STAGE("0x33333333", "quad_perm:[2,3,0,1]");
  ox = u32x4{x[0], x[1], x[2], x[3]};
  oy = u32x4{y[0], y[1], y[2], y[3]};
}
__device__ __forceinline__ void quad_transpose(const u32x4 (&in)[4], u32x4 (&out)[4]) {
  u32x4 t[4];
  swap1(in[0], in[1], t[0], t[1]);
  swap1(in[2], in[3], t[2], t[3]);
  swap2(t[0], t[2], out[0], out[2]);
  swap2(t[1], t[3], out[1], out[3]);
}

__global__ __launch_bounds__(NTHR, 2) void attn_fused(const char* __restrict__ q, const char* __restrict__ kc,
                                                      const char* __restrict__ vc, const int* __restrict__ bt,
                                                      const int* __restrict__ lens, char* __restrict__ ws,
                                                      __hip_bfloat16* __restrict__ out, int nphys) {
  extern __shared__ __attribute__((aligned(16))) char smem[];
  STAMP_DECL;
  STAMP(0);
  const int tid = threadIdx.x, lane = tid & 63, wave = __builtin_amdgcn_readfirstlane(tid >> 6);
  if (tid < 16) ((unsigned*)(smem + LDS_PW))[tid] = 0u;
  unsigned warm0 = 0;
  if (wave == 0) {
    unsigned long long pc;
    unsigned len;
    asm volatile("s_getpc_b64 %0\n.Lcode_begin:\n\ts_mov_b32 %1, .Lcode_end-.Lcode_begin" : "=s"(pc), "=s"(len));
    const auto rc = __builtin_amdgcn_make_buffer_rsrc((void*)pc, 0, max((int)len, 0), 0x00020000);
    warm0 = __builtin_amdgcn_raw_buffer_load_b32(rc, (lane * 8 + ((blockIdx.x >> 3) & 7)) * 128, 0, 0);
  }
  asm volatile("s_waitcnt lgkmcnt(0)" :: "s"(q), "s"(kc), "s"(vc), "s"(bt), "s"(lens), "s"(ws), "s"(out), "s"(nphys) : "memory");
  const i32x8 slen = *(const __attribute__((address_space(4))) i32x8*)(uintptr_t)lens;
  const int g = lane >> 4, c = lane & 15;
  const int kvh = blockIdx.x & 7, part = blockIdx.x >> 3;

  u32x2 y[NBATCH];
  const bool btw = (unsigned)(wave - 2) < 4u;
  const int bcol = (tid - 128) * 8;
  if (btw) {
    const auto rbt = __builtin_amdgcn_make_buffer_rsrc((void*)bt, 0, NBATCH * NBLK * 4, 0x00020000);
#pragma unroll
    for (int b = 0; b < NBATCH; ++b) y[b] = __builtin_amdgcn_raw_buffer_load_b64(rbt, bcol + (b & 1) * 2048, (b >> 1) * 4096, 0);
  }
  asm volatile("s_barrier" ::: "memory");
  const auto rws = __builtin_amdgcn_make_buffer_rsrc((void*)(ws + (size_t)kvh * WS_HEAD), 0, WS_BYTES, 0x00020000);
  const auto rout = __builtin_amdgcn_make_buffer_rsrc((void*)out, 0, NBATCH * NH * HD * 2, 0x00020000);
  const char* kbase = kc + kvh * (HD * 2);
  const char* vbase = vc + kvh * (HD * 2);
  int koff = ((c >> 2) * 4) * TOK_BYTES + (4 * g + (c & 3)) * 16;
  int voff = 4 * g * TOK_BYTES + c * 16;
  asm volatile("" : "+v"(koff), "+v"(voff), "+s"(kbase), "+s"(vbase));
  if (btw) {
#pragma unroll
    for (int b = 0; b < NBATCH; ++b) *(u32x2*)(smem + LDS_TBL + b * (NBLK * 4) + bcol) = y[b];
    unsigned d0 = y[0][0] ^ y[1][0], d1 = y[0][1] ^ y[1][1];
#pragma unroll
    for (int b = 2; b < NBATCH; ++b) {
      d0 = __builtin_amdgcn_bitop3_b32(d0, y[0][0], y[b][0], 0xF6);
      d1 = __builtin_amdgcn_bitop3_b32(d1, y[0][1], y[b][1], 0xF6);
    }
    unsigned f0, f1;
    asm("s_ff1_i32_b64 %0, %1" : "=s"(f0) : "s"(__builtin_amdgcn_ballot_w64(d0 != 0)));
    asm("s_ff1_i32_b64 %0, %1" : "=s"(f1) : "s"(__builtin_amdgcn_ballot_w64(d1 != 0)));
    asm volatile("" ::: "memory");
    ((unsigned*)(smem + CW_PREFIX))[wave - 2] = (((wave - 2) * 128) | min(2 * f0, 2 * f1 + 1)) + 1;
  }
  if (wave == 6) {
    const auto rq = __builtin_amdgcn_make_buffer_rsrc((void*)(q + 4 * kvh * (HD * 2)), 0, NBATCH * NH * HD * 2 - 4 * kvh * (HD * 2), 0x00020000);
    u32x4 qs[NBATCH];
#pragma unroll
    for (int b = 0; b < NBATCH; ++b) qs[b] = __builtin_amdgcn_raw_buffer_load_b128(rq, lane * 16, b * (NH * HD * 2), 0);
    const unsigned e = __builtin_amdgcn_raw_buffer_load_b32(rws, lane < 32 ? lane * 4 : WS_SLOT0 + (part * NPART + lane - 32) * SLOT_BYTES + 1024, 0, AUX_SC1);
#pragma unroll
    for (int b = 0; b < NBATCH; ++b) *(u32x4*)(smem + LDS_Q + (((b >> 2) * 4 + (c & 3)) * 64 + (c >> 2) * 16 + (b & 3) * 4 + g) * 16) = qs[b];
    *(unsigned*)(smem + LDS_EP + lane * 4) = e;
    asm volatile("" ::: "memory");
    *(unsigned*)(smem + CW_Q) = 1u;
  }
  if (wave == 7) {
    static_assert(PF_N == 2, "32 lanes per page");
    const auto rk = __builtin_amdgcn_make_buffer_rsrc((void*)kc, 0, (unsigned)nphys * PAGE_BYTES, 0x00020000);
    const auto rv = __builtin_amdgcn_make_buffer_rsrc((void*)vc, 0, (unsigned)nphys * PAGE_BYTES, 0x00020000);
    const unsigned page = (unsigned)(((part * PF_N + (lane >> 5)) * 2 + 1) * nphys) / (2 * NPART * PF_N);
    const unsigned off = page * PAGE_BYTES + ((lane >> 1) & 15) * TOK_BYTES + kvh * (HD * 2) + (lane & 1) * 128;
    const unsigned wk = __builtin_amdgcn_raw_buffer_load_b32(rk, off, 0, 0), wv = __builtin_amdgcn_raw_buffer_load_b32(rv, off, 0, 0);
    asm volatile("" :: "v"(wk), "v"(wv));
  }
  asm volatile("" :: "v"(warm0));
  __builtin_amdgcn_s_waitcnt(0x0F70);
  int lraw = 1;
#pragma unroll
  for (int b = 0; b < NBATCH; ++b) lraw = llvm_writelane(slen[b], b, lraw);
  const int npg8 = (clamp_len(lraw) + PAGE - 1) >> 4;
  int nmax = max(npg8, (int)dppu<XOR1>(npg8));
  nmax = max(nmax, (int)dppu<XOR2>(nmax));
  nmax = __builtin_amdgcn_readfirstlane(max(nmax, (int)dppu<XOR7>(nmax)));
  int npg = lane < NBATCH ? npg8 : (1 << 20);
  unsigned farpage = 0x80000000u >> __builtin_clz(nphys);
  asm volatile("" : "+v"(npg), "+s"(nmax), "+s"(farpage));

  unsigned p1;
  do {
    asm volatile("" ::: "memory");
    const u32x4 a = *(const u32x4*)(smem + CW_PREFIX);
    p1 = __builtin_amdgcn_readfirstlane(min(min(a[0], a[1]), min(a[2], a[3])));
  } while (!p1);
  asm volatile("" ::: "memory");
  const int P = __builtin_amdgcn_readfirstlane((int)(p1 - 1 < (unsigned)nmax ? p1 - 1 : (unsigned)nmax));
  const unsigned first = *(const unsigned*)(smem + LDS_TBL + ((lane & 7) * NBLK + min(P, NBLK - 1)) * 4);
  const int cnt = max(npg - P, 0);
  int send = cnt;
  send += (int)dppu<0x111>(send);
  send += (int)dppu<0x112>(send);
  send += (int)dppu<0x114>(send);
  const int npriv = __builtin_amdgcn_readlane(send, NBATCH - 1);
  int bnd;
  {
    const int f = __builtin_ctz((unsigned)__builtin_amdgcn_ballot_w64(first >= farpage) | 0x100u);
    const int nfar = f < NBATCH ? npriv - __builtin_amdgcn_readlane(send - cnt, f) : 0;
    const unsigned wt = npriv * 16 + P * WSH + nfar * (K_WFAR - 16);
    const unsigned x = wt * (unsigned)lane;
    bnd = max((int)__umulhi(x, (unsigned)((1ull << 27) / WSH)), ((int)(x >> 5) - P * (WSH - 16)) >> 4);
    bnd = min(bnd, P + npriv - (int)__umulhi(wt * (unsigned)max(32 - lane, 0), (unsigned)((1ull << 27) / K_WFAR)));
  }
  const int i0 = __builtin_amdgcn_readlane(bnd, part), i1 = __builtin_amdgcn_readlane(bnd, part + 1);
  const int nitem = i1 - i0, ntile = (nitem + 1) >> 1, nextra = ntile & 7;
  const int nw = (ntile >> 3) + (wave < nextra ? 1 : 0);
  const int soff = lane * NBLK - (send - cnt);
  int r = 0;
  if (K_ROT >= 0) {
    const int s = __popcll(__builtin_amdgcn_ballot_w64(send <= i0 - P));
    const int t0 = (i0 + (i0 >= P ? __builtin_amdgcn_readlane(soff, s) : 0)) & (NBATCH * NBLK - 1);
    r = (K_ROT * part - __builtin_amdgcn_readfirstlane(*(const int*)(smem + LDS_TBL + t0 * 4)) - (int)((uintptr_t)kc >> 15)) & 63;
    r = r < nitem ? r : 0;
  }
  const int tile0 = (ntile >> 3) * (wave & ~(K_GW - 1)) + min(wave & ~(K_GW - 1), nextra) + (wave & (K_GW - 1));
  const int pos = 2 * (tile0 + K_GW * (lane >> 1)) + (lane & 1);
  const int item = i0 + (pos + r >= nitem ? pos + r - nitem : pos + r);
  int tix = 0;
  for (int cur = max(i0 - P, 0); cur < i1 - P;) {
    const int s = __popcll(__builtin_amdgcn_ballot_w64(send <= cur));
    const int so = __builtin_amdgcn_readlane(soff, s);
    tix = item - P >= cur ? so : tix;
    cur = __builtin_amdgcn_readlane(send, s);
  }
  tix = (tix + item) & (NBATCH * NBLK - 1);
  const unsigned tstart = now();
  const int phys = *(const int*)(smem + LDS_TBL + tix * 4);
  STAMP(2);
  unsigned tpace;
  int dpace, nfast, dcut;
  u32x4 buf[2][4];
  auto issue = [&](const char* base, int off, int k) {
#pragma unroll
    for (int s = 0; s < 2; ++s) {
      const size_t po = (size_t)(unsigned)__builtin_amdgcn_readlane(phys, 2 * k + s) * PAGE_BYTES;
      const auto rp = __builtin_amdgcn_make_buffer_rsrc((void*)(base + po), 0, s == 0 || 2 * (tile0 + K_GW * k) + 1 < nitem ? PAGE_SPAN : 0, 0x00020000);
#pragma unroll
      for (int i = 0; i < 4; ++i) buf[s][i] = __builtin_amdgcn_raw_buffer_load_b128(rp, off + (i & 1) * TOK_BYTES, (i >> 1) * 2 * TOK_BYTES, AUX_NT);
    }
    __builtin_amdgcn_sched_barrier(0);
  };
  unsigned tiss = 0, tarr = 0;
  int lat8;
  auto pace = [&](int n, bool steer) {
    int adv = 0;
    if (steer) {
      asm volatile("" ::: "memory");
      *(unsigned char*)(smem + CW_LAT + wave) = (unsigned char)min(tarr - tiss, 200u);
      asm volatile("" ::: "memory");
      const u32x2 lt = *(const u32x2*)(smem + CW_LAT);
      adv = __builtin_amdgcn_readfirstlane(*(const int*)(smem + CW_ADV));
      const int sum = __builtin_amdgcn_readfirstlane(__builtin_amdgcn_sad_u8(lt[0], 0, __builtin_amdgcn_sad_u8(lt[1], 0, 0)));
      const int line = max(n - 6, 0) * ((dpace * ADV_LINE) >> 10);
      const int step = min(max((lat8 - sum + (((line - adv) * K_PULL) >> 10)) * ADV_GAIN, -ADV_DOWN * 16), ADV_UP * 16);
      adv = min(max(adv + ((dpace * step) >> 14), -n * ((dpace * K_NEG) >> 10)), line);
      *(int*)(smem + CW_ADV) = adv;
    }
    const unsigned tg = tpace + (((n - 1) * dpace - max(n - nfast, 0) * dcut - adv) >> 8);
    while ((int)((tiss = now()) - tg) < 0) __builtin_amdgcn_s_sleep(1);
  };
  if (wave == 0) *(unsigned*)(smem + CW_ORIGIN) = tstart | 1u;
  if (nw > 0 && wave < 2) issue(kbase, koff, 0);

  lat8 = 8 * (ADV_LAT + ((((uintptr_t)kbase >> 8) & 3) == 1 ? ADV_SLOW : 0) +
              (__builtin_amdgcn_readlane(phys, nw & ~1) >> (31 - __builtin_clz(nphys)) ? K_TAIL : 0));
  unsigned contrib;
  {
    const int s = part >> 2;
    const int e = __builtin_amdgcn_readlane(send, s) + P, n = __builtin_amdgcn_readlane(cnt, s), ps = min(P, __builtin_amdgcn_readlane(npg, s));
    const int nxt = __builtin_amdgcn_ds_bpermute((lane + 1) * 4, bnd);
    contrib = (unsigned)__builtin_amdgcn_ballot_w64(nxt > bnd && (bnd < ps || (n > 0 && bnd < e && nxt > e - n)));
  }
  const unsigned um = nitem > 0 ? (unsigned)__builtin_amdgcn_ballot_w64(i0 < min(P, npg) || (cnt > 0 && i0 - P < send && i1 - P > send - cnt)) & 0xffu : 0u;
  const int nseq = __popc(um);
  const bool wide = nseq > 2;
  {
    const float span = (float)(npriv * 16 + P * WSH) * (PACE * (1.f / 32.f)) - (wide ? LEAD * 256.f : nseq == 2 ? LEAD2 * 256.f : 0.f);
    dpace = __builtin_amdgcn_readfirstlane((int)(fmaxf(span, 0.f) * __builtin_amdgcn_rcpf((float)max(2 * ntile, 1))));
    nfast = 2 * ntile - ENDN;
    dcut = wide ? 0 : __builtin_amdgcn_readfirstlane((int)((float)dpace * (float)(P * WSH) * __builtin_amdgcn_rcpf((float)(npriv * 16 + P * WSH))));
  }
  do {
    asm volatile("" ::: "memory");
    tpace = __builtin_amdgcn_readfirstlane(*(const unsigned*)(smem + CW_ORIGIN));
  } while (!tpace);
  const unsigned tend = tpace + (unsigned)(((npriv * 16 + P * WSH) >> 5) * PACE >> 8);
  if (nw > 0 && wave >= 2) {
    pace(wave, false);
    issue(kbase, koff, 0);
  }

  while (__hip_atomic_load((unsigned*)(smem + CW_Q), __ATOMIC_RELAXED, __HIP_MEMORY_SCOPE_WORKGROUP) == 0u) __builtin_amdgcn_s_sleep(1);
  asm volatile("" ::: "memory");
  const unsigned epoch = *(const unsigned*)(smem + LDS_EP + lane * 4);
  int lenv[2];
  u32x4 qf[2][4];
#pragma unroll
  for (int cg = 0; cg < 2; ++cg) {
    const int l = __builtin_amdgcn_ds_bpermute((4 * cg + (c >> 2)) * 4, lraw);
#pragma unroll
    for (int i = 0; i < 4; ++i) qf[cg][i] = *(const u32x4*)(smem + LDS_Q + ((cg * 4 + i) * 64 + lane) * 16);
    if (l <= 0) {
#pragma unroll
      for (int i = 0; i < 4; ++i) qf[cg][i] = u32x4{0, 0, 0, 0};
    }
    lenv[cg] = clamp_len(l) - 4 * g;
  }
  const unsigned meta = pos < nitem ? (unsigned)(tix & (NBLK - 1)) * PAGE | (item < P ? 0xff0000u : 0x10000u << (tix >> 9)) : 0u;
  const int rk = (wave - nextra) & 7;
  int ncol = wide ? (nseq + 1 - (rk >> 2)) >> 1 : (unsigned)(rk - 1) < 4u ? nseq : 0;
  int mrd, lrd, pcol, phdr;
  unsigned ctv;
  {
    const int u = (lane >> 3) & 3;
    const int rank = ((um >> (lane & 7)) & 1) ? __popc(um & ((1u << (lane & 7)) - 1)) : -1;
    const unsigned long long hit = __builtin_amdgcn_ballot_w64(rank == (wide ? 2 * u + (rk >> 2) : u));
    const unsigned mine8 = (unsigned)(hit >> (8 * u)) & 0xffu;
    const bool on = mine8 != 0 && lane < 32;
    const int col = 4 * __builtin_ctz(mine8 | 0x80u) + (rk & 3);
    mrd = LDS_ML + (col * 40 + 32 + (lane & 7)) * 4;
    lrd = LDS_ML + (col * 40 + (lane & 7) * 4) * 4;
    pcol = col;
    const int slot = WS_SLOT0 + (col * NPART + part) * SLOT_BYTES;
    phdr = on ? slot + 1024 + (lane & 7) * 16 : OOB;
    ctv = (unsigned)__builtin_amdgcn_ds_bpermute(col * 4, (int)epoch) + 1;
  }
  int phdr0 = wide || lane >= 8 ? OOB : phdr;
  int ord = LDS_O + (__builtin_amdgcn_readfirstlane(pcol) * COL_F + 2 * lane) * 4;
  int prow = WS_SLOT0 + (__builtin_amdgcn_readfirstlane(pcol) * NPART + part) * SLOT_BYTES + lane * 16;
  unsigned ct = __builtin_amdgcn_readfirstlane(ctv);
  asm volatile("" : "+v"(mrd), "+v"(lrd), "+v"(ord), "+v"(prow), "+v"(ct), "+v"(phdr0), "+v"(phdr), "+v"(ctv), "+v"(pcol), "+s"(ncol));
  int wm = LDS_ML + (c * 40 + 32 + rk) * 4, wl = LDS_ML + (c * 40 + rk * 4 + g) * 4, wo[2];
#pragma unroll
  for (int cg = 0; cg < 2; ++cg) wo[cg] = ((um >> (4 * cg + (c >> 2))) & 1) ? LDS_O + ((16 * cg + c) * COL_F + rk * HD + 4 * g) * 4 : -1;
  asm volatile("" : "+v"(wm), "+v"(wl), "+v"(wo[0]), "+v"(wo[1]));

  float m[2] = {-INFINITY, -INFINITY}, ms[2] = {0.f, 0.f}, l[2] = {0.f, 0.f};
  f32x4 acc[2][8];
#pragma unroll
  for (int cg = 0; cg < 2; ++cg)
#pragma unroll
    for (int i = 0; i < 8; ++i) acc[cg][i] = f32x4{0.f, 0.f, 0.f, 0.f};
  __builtin_amdgcn_s_setprio(2);

  auto put_stats = [&]() {
#pragma unroll
    for (int cg = 0; cg < 2; ++cg) {
      *(float*)(smem + wm + cg * (16 * 160)) = m[cg] * SC_LOG2;
      *(float*)(smem + wl + cg * (16 * 160)) = l[cg];
    }
  };
  auto flag_stats = [&]() {
    asm volatile("" ::: "memory");
    *(unsigned char*)(smem + CW_STATS + rk) = 1;
  };
  if (nw == 0) {
    put_stats();
    flag_stats();
  }
  const int sc0 = LDS_O + ((lane >> 5) * COL_F + rk * HD) * 4 + (lane & 31) * 16;
  for (int k0 = 0; k0 < nw; k0 += 4) {
    const int k1 = min(k0 + 4, nw), nround = min(ntile - 8 * k0, 32);
    float mx[2] = {-INFINITY, -INFINITY};
    for (int k = k0; k < k1;) {
      u32x4 kt[2][4];
      quad_transpose(buf[0], kt[0]);
      asm volatile("" :: "v"(kt[0][0]));
      tarr = now();
      quad_transpose(buf[1], kt[1]);
      const unsigned mt[2] = {(unsigned)__builtin_amdgcn_readlane((int)meta, 2 * k), (unsigned)__builtin_amdgcn_readlane((int)meta, 2 * k + 1)};
#pragma unroll
      for (int cg = 0; cg < 2; ++cg) {
        if (((mt[0] | mt[1]) >> (16 + 4 * cg)) & 0xfu) {
#pragma unroll
          for (int s = 0; s < 2; ++s) {
            f32x4 x = {0.f, 0.f, 0.f, 0.f};
#pragma unroll
            for (int i = 0; i < 4; ++i) x = mfma32(kt[s][i], qf[cg][i], x);
            const int lim = ((mt[s] >> (16 + 4 * cg + (c >> 2))) & 1) ? lenv[cg] - (int)(mt[s] & 0xffffu) : 0;
#pragma unroll
            for (int e = 0; e < 4; ++e) x[e] = e < lim ? x[e] : -INFINITY;
            mx[cg] = fmx(fmx(fmx(mx[cg], x[0]), x[1]), fmx(x[2], x[3]));
            *(f32x4*)(smem + sc0 + (((k - k0) * 2 + cg) * 4 + 2 * s) * (COL_F * 4)) = x;
          }
        }
      }
      ++k;
      if (k < k1) {
        pace(16 * k0 + 8 * (k - k0) + wave, true);
        issue(kbase, koff, k);
      }
    }
    pace(16 * k0 + nround + wave, true);
    issue(vbase, voff, k0);
#pragma unroll
    for (int cg = 0; cg < 2; ++cg) {
      m[cg] = fmx(m[cg], swap32_max(swap16_max(mx[cg])));
      const float mn = m[cg] == -INFINITY ? 0.f : m[cg] * SC_LOG2;
      if (k0 > 0) {
        const float alpha = __builtin_amdgcn_exp2f(fminf(ms[cg] - mn, 0.f));
        l[cg] *= alpha;
#pragma unroll
        for (int i = 0; i < 8; ++i) acc[cg][i] *= alpha;
      }
      ms[cg] = mn;
    }
    for (int k = k0; k < k1;) {
      const unsigned mu = (unsigned)__builtin_amdgcn_readlane((int)meta, 2 * k) | (unsigned)__builtin_amdgcn_readlane((int)meta, 2 * k + 1);
      u32x4 pb[2];
#pragma unroll
      for (int cg = 0; cg < 2; ++cg) {
        if ((mu >> (16 + 4 * cg)) & 0xfu) {
#pragma unroll
          for (int s = 0; s < 2; ++s) {
            f32x4 x = *(const f32x4*)(smem + sc0 + (((k - k0) * 2 + cg) * 4 + 2 * s) * (COL_F * 4));
#pragma unroll
            for (int e = 0; e < 4; ++e) x[e] = __builtin_amdgcn_exp2f(__builtin_fmaf(x[e], SC_LOG2, -ms[cg]));
            l[cg] += (x[0] + x[1]) + (x[2] + x[3]);
            pb[cg][2 * s] = cvtpk(x[0], x[1]);
            pb[cg][2 * s + 1] = cvtpk(x[2], x[3]);
          }
        }
      }
      ++k;
      if (k == nw) put_stats();
      u32x4 va[8];
#pragma unroll
      for (int s = 0; s < 2; ++s) {
#pragma unroll
        for (int i = 0; i < 8; ++i) {
          const unsigned sel = (i & 1) ? 0x07060302u : 0x05040100u;
          va[i][2 * s] = __builtin_amdgcn_perm(buf[s][1][i >> 1], buf[s][0][i >> 1], sel);
          va[i][2 * s + 1] = __builtin_amdgcn_perm(buf[s][3][i >> 1], buf[s][2][i >> 1], sel);
        }
        if (s == 0) {
          asm volatile("" :: "v"(va[7][1]));
          tarr = now();
          if (K_FLAG == 0 && k == nw) flag_stats();
        }
      }
      if (K_FLAG == 1) {
        asm volatile("" :: "v"(va[7][3]));
        if (k == nw) flag_stats();
      }
#pragma unroll
      for (int cg = 0; cg < 2; ++cg) {
        if ((mu >> (16 + 4 * cg)) & 0xfu) {
#pragma unroll
          for (int i = 0; i < 8; ++i) acc[cg][i] = mfma32(va[i], pb[cg], acc[cg][i]);
        }
      }
      if (k < nw) {
        const bool more_v = k < k1;
        pace(more_v ? 16 * k0 + nround + 8 * (k - k0) + wave : 16 * k1 + wave, true);
        issue(more_v ? vbase : kbase, more_v ? voff : koff, k);
      }
    }
  }
  __builtin_amdgcn_s_waitcnt(0x0F70);
  STAMP(4);

#pragma unroll
  for (int cg = 0; cg < 2; ++cg) {
    if (wo[cg] >= 0) {
#pragma unroll
      for (int i = 0; i < 8; ++i) *(f32x4*)(smem + wo[cg] + 64 * i) = acc[cg][i];
    }
  }
  asm volatile("" ::: "memory");
  *(unsigned char*)(smem + CW_ROWS + rk) = 1;
  __builtin_amdgcn_s_setprio(0);

  if (ncol > 0) {
    for (;;) {
      asm volatile("" ::: "memory");
      const u32x2 dn = *(const u32x2*)(smem + CW_ROWS);
      if (__builtin_amdgcn_ballot_w64((dn[0] & (dn[1] | 0x01010100u)) == 0x01010101u)) break;
    }
    asm volatile("" ::: "memory");
    f32x2 o[NWAVE];
#pragma unroll
    for (int w = 0; w < 5; ++w) o[w] = *(const f32x2*)(smem + ord + w * (HD * 4));
    float mw;
    f32x4 lw;
    for (;;) {
      asm volatile("" ::: "memory");
      const unsigned dn = *(const unsigned*)(smem + CW_STATS + 4);
      asm volatile("" ::: "memory");
      mw = *(const float*)(smem + mrd);
      lw = *(const f32x4*)(smem + lrd);
      asm volatile("" : "+v"(mw), "+v"(lw));
      if (__builtin_amdgcn_ballot_w64(dn == 0x01010101u)) break;
    }
    const float M = max8(mw);
    const float f = __builtin_amdgcn_exp2f(mw - M);
    const float L = sum8(f * ((lw[0] + lw[1]) + (lw[2] + lw[3])));
    const u32x4 hdr = {__float_as_uint(M), ctv, __float_as_uint(L), ctv};
    __builtin_amdgcn_raw_buffer_store_b128(hdr, rws, phdr0, 0, AUX_SC1);
    STAMP(5);
    float fw[8];
#pragma unroll
    for (int w = 0; w < 8; ++w) fw[w] = rdlane(f, w);
    asm volatile("" : "+s"(fw[0]), "+s"(fw[1]), "+s"(fw[2]), "+s"(fw[3]), "+s"(fw[4]), "+s"(fw[5]), "+s"(fw[6]), "+s"(fw[7]));
    f32x2 a = (o[0] * fw[0] + o[2] * fw[2] + o[4] * fw[4]) + (o[1] * fw[1] + o[3] * fw[3]);
    asm volatile("" : "+v"(a));
    for (;;) {
      asm volatile("" ::: "memory");
      const unsigned dn = *(const unsigned*)(smem + CW_ROWS + 4);
      asm volatile("" ::: "memory");
#pragma unroll
      for (int w = 5; w < NWAVE; ++w) o[w] = *(const f32x2*)(smem + ord + w * (HD * 4));
      asm volatile("" : "+v"(o[5]), "+v"(o[6]), "+v"(o[7]));
      if (__builtin_amdgcn_ballot_w64(dn == 0x01010101u)) break;
    }
    a = (a + o[5] * fw[5]) + (o[6] * fw[6] + o[7] * fw[7]);
    __builtin_amdgcn_raw_buffer_store_b128(u32x4{__float_as_uint(a[0]), ct, __float_as_uint(a[1]), ct}, rws, prow, 0, AUX_SC1);
    const int phdr1 = !wide && lane < 8 ? OOB : phdr;
#pragma unroll 1
    for (int u = 1; u < ncol; ++u) {
      const int col = __builtin_amdgcn_readlane(pcol, 8 * u);
#pragma unroll
      for (int w = 0; w < NWAVE; ++w) o[w] = *(const f32x2*)(smem + LDS_O + (col * COL_F + 2 * lane) * 4 + w * (HD * 4));
      if (u == ncol - 1) __builtin_amdgcn_raw_buffer_store_b128(hdr, rws, phdr1, 0, AUX_SC1);
      ct = __builtin_amdgcn_readlane(ctv, 8 * u);
      a = wsum8(o, f, 8 * u);
      __builtin_amdgcn_raw_buffer_store_b128(u32x4{__float_as_uint(a[0]), ct, __float_as_uint(a[1]), ct}, rws,
                                             WS_SLOT0 + (col * NPART + part) * SLOT_BYTES + lane * 16, 0, AUX_SC1);
    }
    if (wide && ncol == 1) __builtin_amdgcn_raw_buffer_store_b128(hdr, rws, phdr1, 0, AUX_SC1);
    STAMP(6);
  }

  if (rk == 0) {
    const unsigned tag = (unsigned)__builtin_amdgcn_readlane((int)epoch, part) + 1;
    const bool oddi = (lane >> 3) & 1;
    int orow = (((part >> 2) * NH + 4 * kvh + (part & 3)) * HD + ((lane >> 1) & 3) * 32 + (lane & 1) * 16 + (lane >> 4) * 2 + (oddi ? 8 : 0)) * 2;
    float Mr = -3.0e38f, Lr = 0.f, a0 = 0.f, a1 = 0.f;
    unsigned spin = 0;
    const int ncon = __popc(contrib);
    const int crank = lane < 32 && ((contrib >> lane) & 1) ? __popc(contrib & ((1u << lane) - 1)) : 63;
    const int cpart = __builtin_amdgcn_ds_permute(crank * 4, lane);
    for (int base = 0; base < ncon; base += GS) {
      const int u16 = lane & 15;
      const bool hon = u16 < GS && base + u16 < ncon;
      const int sl = __builtin_amdgcn_ds_bpermute((base + u16) * 4, cpart);
      const int so = hon ? WS_SLOT0 + (part * NPART + sl) * SLOT_BYTES : OOB;
      const unsigned htag = hon ? tag : 0u;
      const float hbias = hon ? 0.f : -INFINITY;
      int doff[GS];
      unsigned etag[GS];
#pragma unroll
      for (int u = 0; u < GS; ++u) {
        doff[u] = __builtin_amdgcn_readlane(so, u) + lane * 16;
        etag[u] = __builtin_amdgcn_readlane(htag, u);
        asm volatile("" : "+v"(doff[u]), "+s"(etag[u]));
      }
      asm volatile("" : "+v"(orow));
      u32x4 h, d[GS];
      const int eoff = lane == 0 && base + GS >= ncon ? part * 4 : OOB;
      for (;;) {
        asm volatile("" ::: "memory");
        const int av = __builtin_amdgcn_readfirstlane(*(const int*)(smem + CW_ADV));
        if (wide || (int)(now() - (tend + POLL_AT - (av >> 8))) >= 0) break;
        __builtin_amdgcn_s_sleep(2);
      }
#pragma unroll 1
      for (; spin < SPIN_CAP; ++spin) {
        asm volatile("" ::: "memory");
        h = __builtin_amdgcn_raw_buffer_load_b128(rws, so + 1024, 0, AUX_SC1);
        if (!__builtin_amdgcn_ballot_w64(__builtin_amdgcn_bitop3_b32(h[1], h[3], htag, 0x7E) != 0)) break;
      }
      STAMP(3);
#pragma unroll
      for (int u = GS - 1; u >= 0; --u) d[u] = __builtin_amdgcn_raw_buffer_load_b128(rws, doff[u], 0, AUX_SC1);
      __builtin_amdgcn_raw_buffer_store_b32(tag, rws, eoff, 0, AUX_SC1);
      __builtin_amdgcn_sched_barrier(0);
      const float mu = __uint_as_float(h[0]) + hbias;
      float M = max8(mu);
      M = fmx(fmx(M, dppf<XOR15>(M)), Mr);
      float f = __builtin_amdgcn_exp2f(mu - M), fr = __builtin_amdgcn_exp2f(Mr - M);
      const float l8 = sum8(f * __uint_as_float(h[2]));
      Lr = (l8 + dppf<XOR15>(l8)) + fr * Lr;
      Mr = M;
      if (base + GS >= ncon) {
        const float inv = __builtin_amdgcn_rcpf(Lr);
        f *= inv;
        fr *= inv;
      }
      a0 *= fr;
      a1 *= fr;
      float fu[GS], b0 = 0.f, b1 = 0.f;
#pragma unroll
      for (int u = 0; u < GS; ++u) {
        fu[u] = rdlane(f, u);
        asm volatile("" : "+s"(fu[u]));
      }
      asm volatile("s_nop 1" : "+v"(a0), "+v"(a1), "+v"(b0), "+v"(b1));
#pragma unroll 1
      for (; spin < SPIN_CAP; ++spin) {
        unsigned bad = 0;
#pragma unroll
        for (int u = GS - 1; u >= 0; --u) bad |= __builtin_amdgcn_bitop3_b32(d[u][1], d[u][3], etag[u], 0x7E);
        if (!__builtin_amdgcn_ballot_w64(bad != 0)) break;
        asm volatile("" ::: "memory");
#pragma unroll
        for (int u = GS - 1; u >= 0; --u) d[u] = __builtin_amdgcn_raw_buffer_load_b128(rws, doff[u], 0, AUX_SC1);
        __builtin_amdgcn_raw_buffer_store_b32(tag, rws, eoff, 0, AUX_SC1);
      }
#pragma unroll
      for (int u = 0; u < GS; u += 2)
        asm("v_fmac_f32 %0, %4, %6\n\tv_fmac_f32 %1, %4, %7\n\tv_fmac_f32 %2, %5, %8\n\tv_fmac_f32 %3, %5, %9"
            : "+v"(a0), "+v"(a1), "+v"(b0), "+v"(b1)
            : "s"(fu[u]), "s"(fu[u + 1]), "v"(d[u][0]), "v"(d[u][2]), "v"(d[u + 1][0]), "v"(d[u + 1][2]));
      a0 += b0;
      a1 += b1;
    }
    const unsigned pk = cvtpk(a0, a1), pp = dppu<0x128>(pk);
    __builtin_amdgcn_raw_buffer_store_b32(__builtin_amdgcn_perm(pk, pp, oddi ? 0x07060302u : 0x01000504u), rout, orow, 0, AUX_SC1);
    asm volatile(".Lcode_end:");
  }
  STAMP(7);
  STAMP_FLUSH;
}

}

extern "C" void kernel_launch(void* const* d_in, const int* in_sizes, int n_in, void* d_out, int out_size, void* d_ws,
                              size_t ws_size, hipStream_t stream) {
  (void)n_in; (void)out_size; (void)ws_size;
  (void)hipFuncSetAttribute((const void*)attn_fused, hipFuncAttributeMaxDynamicSharedMemorySize, LDS_BYTES);
  hipLaunchKernelGGL(attn_fused, dim3(NKVH * NPART), dim3(NTHR), LDS_BYTES, stream, (const char*)d_in[0], (const char*)d_in[1],
                     (const char*)d_in[2], (const int*)d_in[3], (const int*)d_in[4], (char*)d_ws, (__hip_bfloat16*)d_out, in_sizes[1] / (PAGE_BYTES / 2));
}
```

```cpp
#include <hip/hip_runtime.h>
#include <hip/hip_bf16.h>
#include <stdint.h>

namespace {

constexpr int NBATCH = 8, NH = 32, NKVH = 8, HD = 128, PAGE = 16, NBLK = 512, LK = NBLK * PAGE;
constexpr int NPART = 32, NWAVE = 8, NTHR = NWAVE * 64, NCOL = 32;
constexpr int WSH = 17, WFAR = 21;
static_assert(WFAR >= WSH && WSH >= 16, "split points are a min / max of three lines");
constexpr int PACE = 480;
constexpr int LEAD = 170;
constexpr int LEAD2 = 25;
constexpr int ENDN = 6;
constexpr int ADV_LAT = 98;
constexpr int ADV_SLOW = 25;
constexpr int ADV_PULL = 6;
constexpr int ADV_GAIN = 11;
constexpr int ADV_UP = 150, ADV_DOWN = 80;
constexpr int ADV_MAX = 115, ADV_MIN = 60;
constexpr int POLL_AT = -20;
constexpr int PF_N = 2;
constexpr int TOK_BYTES = NKVH * HD * 2;
constexpr int PAGE_BYTES = PAGE * TOK_BYTES;
constexpr int PAGE_SPAN = (PAGE - 1) * TOK_BYTES + HD * 2;
constexpr float SC_LOG2 = 0.08838834764831845f * 1.4426950408889634f;
constexpr int AUX_NT = 2, AUX_SC1 = 16;
constexpr unsigned SPIN_CAP = 1u << 20;
constexpr int OOB = 0x70000000;
constexpr int GS = 12;

typedef __attribute__((ext_vector_type(8))) __bf16 bf16x8;
typedef __attribute__((ext_vector_type(2))) __bf16 bf16x2;
typedef __attribute__((ext_vector_type(4))) float f32x4;
typedef __attribute__((ext_vector_type(2))) float f32x2;
typedef __attribute__((ext_vector_type(4))) unsigned u32x4;
typedef __attribute__((ext_vector_type(2))) unsigned u32x2;
typedef __attribute__((ext_vector_type(8))) int i32x8;
extern "C" __device__ int llvm_writelane(int, int, int) __asm("llvm.amdgcn.writelane.i32");

constexpr int COL_F = NWAVE * HD + 4;
constexpr int LDS_TBL = 0;
constexpr int LDS_PW = LDS_TBL + NBLK * NBATCH * 4;
constexpr int CW_PREFIX = LDS_PW;
constexpr int CW_Q = LDS_PW + 16;
constexpr int CW_ORIGIN = LDS_PW + 20;
constexpr int CW_STATS = LDS_PW + 24;
constexpr int CW_ROWS = LDS_PW + 32;
constexpr int CW_LAT = LDS_PW + 40;
constexpr int CW_ADV = LDS_PW + 48;
constexpr int LDS_EP = LDS_PW + 64;
constexpr int LDS_Q = LDS_EP + 256;
constexpr int LDS_ML = LDS_Q + 8192;
constexpr int LDS_O = LDS_ML + NCOL * 40 * 4;
constexpr int LDS_BYTES = LDS_O + NCOL * COL_F * 4;

constexpr int WS_HEAD = 2 << 20, WS_SLOT0 = 1024, SLOT_BYTES = 1152;
constexpr int WS_BYTES = WS_SLOT0 + NCOL * NPART * SLOT_BYTES;

__device__ __forceinline__ f32x4 mfma32(u32x4 a, u32x4 b, f32x4 c) {
  return __builtin_amdgcn_mfma_f32_16x16x32_bf16(__builtin_bit_cast(bf16x8, a), __builtin_bit_cast(bf16x8, b), c, 0, 0, 0);
}
__device__ __forceinline__ unsigned cvtpk(float lo, float hi) {
  return __builtin_bit_cast(unsigned, __builtin_convertvector(f32x2{lo, hi}, bf16x2));
}
__device__ __forceinline__ float fmx(float a, float b) { return __builtin_elementwise_maximum(a, b); }
__device__ __forceinline__ float swap16_max(float x) {
  auto s = __builtin_amdgcn_permlane16_swap(__float_as_uint(x), __float_as_uint(x), false, false);
  return fmx(__uint_as_float(s[0]), __uint_as_float(s[1]));
}
__device__ __forceinline__ float swap32_max(float x) {
  auto s = __builtin_amdgcn_permlane32_swap(__float_as_uint(x), __float_as_uint(x), false, false);
  return fmx(__uint_as_float(s[0]), __uint_as_float(s[1]));
}
template <int CTRL> __device__ __forceinline__ unsigned dppu(unsigned x) {
  return (unsigned)__builtin_amdgcn_mov_dpp((int)x, CTRL, 0xf, 0xf, true);
}
template <int CTRL> __device__ __forceinline__ float dppf(float x) { return __uint_as_float(dppu<CTRL>(__float_as_uint(x))); }
constexpr int XOR1 = 0xB1, XOR2 = 0x4E, XOR3 = 0x1B, XOR7 = 0x141, XOR15 = 0x140;
__device__ __forceinline__ float max8(float x) {
  x = fmx(x, dppf<XOR7>(x));
  return fmx(fmx(fmx(x, dppf<XOR1>(x)), dppf<XOR2>(x)), dppf<XOR3>(x));
}
__device__ __forceinline__ float sum8(float x) {
  x += dppf<XOR1>(x); x += dppf<XOR2>(x); return x + dppf<XOR7>(x);
}
__device__ __forceinline__ float rdlane(float x, int l) { return __int_as_float(__builtin_amdgcn_readlane(__float_as_int(x), l)); }
__device__ __forceinline__ int clamp_len(int l) { return (l <= 0 || l > LK) ? LK : l; }
__device__ __forceinline__ unsigned now() { return (unsigned)__builtin_amdgcn_s_memrealtime(); }
__device__ __forceinline__ f32x2 wsum8(const f32x2 (&o)[8], float f, int l0) {
  float s[8];
#pragma unroll
  for (int w = 0; w < 8; ++w) s[w] = rdlane(f, l0 + w);
  asm volatile("" : "+s"(s[0]), "+s"(s[1]), "+s"(s[2]), "+s"(s[3]), "+s"(s[4]), "+s"(s[5]), "+s"(s[6]), "+s"(s[7]));
  const f32x2 a = o[0] * s[0] + o[2] * s[2] + o[4] * s[4] + o[6] * s[6];
  const f32x2 b = o[1] * s[1] + o[3] * s[3] + o[5] * s[5] + o[7] * s[7];
  return a + b;
}

#define CND4(D, A, B, P)                                                                        \
  "v_cndmask_b32_dpp %[" D "0], %[" A "0], %[" B "0], vcc " P " row_mask:0xf bank_mask:0xf\n\t" \
  "v_cndmask_b32_dpp %[" D "1], %[" A "1], %[" B "1], vcc " P " row_mask:0xf bank_mask:0xf\n\t" \
  "v_cndmask_b32_dpp %[" D "2], %[" A "2], %[" B "2], vcc " P " row_mask:0xf bank_mask:0xf\n\t" \
  "v_cndmask_b32_dpp %[" D "3], %[" A "3], %[" B "3], vcc " P " row_mask:0xf bank_mask:0xf\n\t"
#define SWAP_STAGE(MASK, P)                                                                                              \
  asm("s_mov_b32 vcc_lo, " MASK "\n\ts_mov_b32 vcc_hi, " MASK "\n\t" CND4("x", "b", "a", P) "s_not_b64 vcc, vcc\n\t" CND4("y", "a", "b", P) \
      "s_nop 1"                                                                                                          \
      : [x0] "=&v"(x[0]), [x1] "=&v"(x[1]), [x2] "=&v"(x[2]), [x3] "=&v"(x[3]), [y0] "=&v"(y[0]), [y1] "=&v"(y[1]),      \
        [y2] "=&v"(y[2]), [y3] "=&v"(y[3])                                                                               \
      : [a0] "v"(a[0]), [a1] "v"(a[1]), [a2] "v"(a[2]), [a3] "v"(a[3]), [b0] "v"(b[0]), [b1] "v"(b[1]), [b2] "v"(b[2]),  \
        [b3] "v"(b[3])                                                                                                   \
      : "vcc", "scc")
__device__ __forceinline__ void swap1(const u32x4& a, const u32x4& b, u32x4& ox, u32x4& oy) {
  unsigned x[4], y[4];
  SWAP_STAGE("0x55555555", "quad_perm:[1,0,3,2]");
  ox = u32x4{x[0], x[1], x[2], x[3]};
  oy = u32x4{y[0], y[1], y[2], y[3]};
}
__device__ __forceinline__ void swap2(const u32x4& a, const u32x4& b, u32x4& ox, u32x4& oy) {
  unsigned x[4], y[4];
  SWAP_STAGE("0x33333333", "quad_perm:[2,3,0,1]");
  ox = u32x4{x[0], x[1], x[2], x[3]};
  oy = u32x4{y[0], y[1], y[2], y[3]};
}
__device__ __forceinline__ void quad_transpose(const u32x4 (&in)[4], u32x4 (&out)[4]) {
  u32x4 t[4];
  swap1(in[0], in[1], t[0], t[1]);
  swap1(in[2], in[3], t[2], t[3]);
  swap2(t[0], t[2], out[0], out[2]);
  swap2(t[1], t[3], out[1], out[3]);
}

__global__ __launch_bounds__(NTHR, 2) void attn_fused(const char* __restrict__ q, const char* __restrict__ kc,
                                                      const char* __restrict__ vc, const int* __restrict__ bt,
                                                      const int* __restrict__ lens, char* __restrict__ ws,
                                                      __hip_bfloat16* __restrict__ out, int nphys) {
  extern __shared__ __attribute__((aligned(16))) char smem[];
  const int tid = threadIdx.x, lane = tid & 63, wave = __builtin_amdgcn_readfirstlane(tid >> 6);
  if (tid < 16) ((unsigned*)(smem + LDS_PW))[tid] = 0u;
  unsigned warm0 = 0;
  if (wave == 0) {
    unsigned long long pc;
    unsigned len;
    asm volatile("s_getpc_b64 %0\n.Lcode_begin:\n\ts_mov_b32 %1, .Lcode_end-.Lcode_begin" : "=s"(pc), "=s"(len));
    const auto rc = __builtin_amdgcn_make_buffer_rsrc((void*)pc, 0, max((int)len, 0), 0x00020000);
    warm0 = __builtin_amdgcn_raw_buffer_load_b32(rc, (lane * 8 + ((blockIdx.x >> 3) & 7)) * 128, 0, 0);
  }
  asm volatile("s_waitcnt lgkmcnt(0)" :: "s"(q), "s"(kc), "s"(vc), "s"(bt), "s"(lens), "s"(ws), "s"(out), "s"(nphys) : "memory");
  const i32x8 slen = *(const __attribute__((address_space(4))) i32x8*)(uintptr_t)lens;
  const int g = lane >> 4, c = lane & 15;
  const int kvh = blockIdx.x & 7, part = blockIdx.x >> 3;

  u32x2 y[NBATCH];
  const bool btw = (unsigned)(wave - 2) < 4u;
  const int bcol = (tid - 128) * 8;
  if (btw) {
    const auto rbt = __builtin_amdgcn_make_buffer_rsrc((void*)bt, 0, NBATCH * NBLK * 4, 0x00020000);
#pragma unroll
    for (int b = 0; b < NBATCH; ++b) y[b] = __builtin_amdgcn_raw_buffer_load_b64(rbt, bcol + (b & 1) * 2048, (b >> 1) * 4096, 0);
  }
  asm volatile("s_barrier" ::: "memory");
  const auto rws = __builtin_amdgcn_make_buffer_rsrc((void*)(ws + (size_t)kvh * WS_HEAD), 0, WS_BYTES, 0x00020000);
  const auto rout = __builtin_amdgcn_make_buffer_rsrc((void*)out, 0, NBATCH * NH * HD * 2, 0x00020000);
  const char* kbase = kc + kvh * (HD * 2);
  const char* vbase = vc + kvh * (HD * 2);
  int koff = ((c >> 2) * 4) * TOK_BYTES + (4 * g + (c & 3)) * 16;
  int voff = 4 * g * TOK_BYTES + c * 16;
  asm volatile("" : "+v"(koff), "+v"(voff), "+s"(kbase), "+s"(vbase));
  if (btw) {
#pragma unroll
    for (int b = 0; b < NBATCH; ++b) *(u32x2*)(smem + LDS_TBL + b * (NBLK * 4) + bcol) = y[b];
    unsigned d0 = y[0][0] ^ y[1][0], d1 = y[0][1] ^ y[1][1];
#pragma unroll
    for (int b = 2; b < NBATCH; ++b) {
      d0 = __builtin_amdgcn_bitop3_b32(d0, y[0][0], y[b][0], 0xF6);
      d1 = __builtin_amdgcn_bitop3_b32(d1, y[0][1], y[b][1], 0xF6);
    }
    unsigned f0, f1;
    asm("s_ff1_i32_b64 %0, %1" : "=s"(f0) : "s"(__builtin_amdgcn_ballot_w64(d0 != 0)));
    asm("s_ff1_i32_b64 %0, %1" : "=s"(f1) : "s"(__builtin_amdgcn_ballot_w64(d1 != 0)));
    asm volatile("" ::: "memory");
    ((unsigned*)(smem + CW_PREFIX))[wave - 2] = (((wave - 2) * 128) | min(2 * f0, 2 * f1 + 1)) + 1;
  }
  if (wave == 6) {
    const auto rq = __builtin_amdgcn_make_buffer_rsrc((void*)(q + 4 * kvh * (HD * 2)), 0, NBATCH * NH * HD * 2 - 4 * kvh * (HD * 2), 0x00020000);
    u32x4 qs[NBATCH];
#pragma unroll
    for (int b = 0; b < NBATCH; ++b) qs[b] = __builtin_amdgcn_raw_buffer_load_b128(rq, lane * 16, b * (NH * HD * 2), 0);
    const unsigned e = __builtin_amdgcn_raw_buffer_load_b32(rws, lane < 32 ? lane * 4 : WS_SLOT0 + (part * NPART + lane - 32) * SLOT_BYTES + 1024, 0, AUX_SC1);
#pragma unroll
    for (int b = 0; b < NBATCH; ++b) *(u32x4*)(smem + LDS_Q + (((b >> 2) * 4 + (c & 3)) * 64 + (c >> 2) * 16 + (b & 3) * 4 + g) * 16) = qs[b];
    *(unsigned*)(smem + LDS_EP + lane * 4) = e;
    asm volatile("" ::: "memory");
    *(unsigned*)(smem + CW_Q) = 1u;
  }
  if (wave == 7) {
    static_assert(PF_N == 2, "32 lanes per page");
    const auto rk = __builtin_amdgcn_make_buffer_rsrc((void*)kc, 0, (unsigned)nphys * PAGE_BYTES, 0x00020000);
    const auto rv = __builtin_amdgcn_make_buffer_rsrc((void*)vc, 0, (unsigned)nphys * PAGE_BYTES, 0x00020000);
    const unsigned page = (unsigned)(((part * PF_N + (lane >> 5)) * 2 + 1) * nphys) / (2 * NPART * PF_N);
    const unsigned off = page * PAGE_BYTES + ((lane >> 1) & 15) * TOK_BYTES + kvh * (HD * 2) + (lane & 1) * 128;
    const unsigned wk = __builtin_amdgcn_raw_buffer_load_b32(rk, off, 0, 0), wv = __builtin_amdgcn_raw_buffer_load_b32(rv, off, 0, 0);
    asm volatile("" :: "v"(wk), "v"(wv));
  }
  asm volatile("" :: "v"(warm0));
  __builtin_amdgcn_s_waitcnt(0x0F70);
  int lraw = 1;
#pragma unroll
  for (int b = 0; b < NBATCH; ++b) lraw = llvm_writelane(slen[b], b, lraw);
  const int npg8 = (clamp_len(lraw) + PAGE - 1) >> 4;
  int nmax = max(npg8, (int)dppu<XOR1>(npg8));
  nmax = max(nmax, (int)dppu<XOR2>(nmax));
  nmax = __builtin_amdgcn_readfirstlane(max(nmax, (int)dppu<XOR7>(nmax)));
  int npg = lane < NBATCH ? npg8 : (1 << 20);
  unsigned farpage = 0x80000000u >> __builtin_clz(nphys);
  int lastix = LDS_TBL + ((lane & 7) * NBLK + npg8 - 1) * 4;
  int rest = max(32 - lane, 0), soff0 = lane * NBLK, item0 = 16 * (lane >> 1) + 2 * wave + (lane & 1);
  asm volatile("" : "+v"(npg), "+s"(nmax), "+s"(farpage), "+v"(lastix), "+v"(rest), "+v"(soff0), "+v"(item0));

  unsigned p1, last;
  do {
    asm volatile("" ::: "memory");
    const u32x4 a = *(const u32x4*)(smem + CW_PREFIX);
    asm volatile("" ::: "memory");
    last = *(const unsigned*)(smem + lastix);
    asm volatile("" : "+v"(last));
    p1 = __builtin_amdgcn_readfirstlane(min(min(a[0], a[1]), min(a[2], a[3])));
  } while (!p1);
  asm volatile("" ::: "memory");
  int P;
  asm("s_min_u32 %0, %1, %2" : "=s"(P) : "s"(p1 - 1), "s"(nmax) : "scc");
  const int cnt = max(npg - P, 0);
  int send = cnt;
  send += (int)dppu<0x111>(send);
  send += (int)dppu<0x112>(send);
  send += (int)dppu<0x114>(send);
  const int npriv = __builtin_amdgcn_readlane(send, NBATCH - 1);
  int bnd;
  {
    const int f = __builtin_ctz((unsigned)__builtin_amdgcn_ballot_w64(last >= farpage) | 0x100u);
    const int nfar = f < NBATCH ? npriv - __builtin_amdgcn_readlane(send - cnt, f) : 0;
    const unsigned wt = npriv * 16 + P * WSH + nfar * (WFAR - 16);
    const unsigned x = wt * (unsigned)lane;
    bnd = max((int)__umulhi(x, (unsigned)((1ull << 27) / WSH)), ((int)(x >> 5) - P * (WSH - 16)) >> 4);
    bnd = min(bnd, P + npriv - (int)__umulhi(wt * (unsigned)rest, (unsigned)((1ull << 27) / WFAR)));
  }
  const int i0 = __builtin_amdgcn_readlane(bnd, part), i1 = __builtin_amdgcn_readlane(bnd, part + 1);
  const int nitem = i1 - i0, ntile = (nitem + 1) >> 1, nextra = ntile & 7;
  const int nw = (ntile >> 3) + (wave < nextra ? 1 : 0);
  const int soff = soff0 - (send - cnt);
  const int item = i0 + item0;
  int tix = 0;
  for (int cur = max(i0 - P, 0); cur < i1 - P;) {
    const int s = __popcll(__builtin_amdgcn_ballot_w64(send <= cur));
    const int so = __builtin_amdgcn_readlane(soff, s);
    tix = item - P >= cur ? so : tix;
    cur = __builtin_amdgcn_readlane(send, s);
  }
  tix = (tix + item) & (NBATCH * NBLK - 1);
  const unsigned tstart = now();
  const int phys = *(const int*)(smem + LDS_TBL + tix * 4);
  int early = wave < 2 ? nitem - 2 * wave : 0;
  asm volatile("" : "+s"(early));
  unsigned tpace;
  int dpace, nfast, dcut;
  u32x4 buf[2][4];
  auto issue = [&](const char* base, int off, int k) {
#pragma unroll
    for (int s = 0; s < 2; ++s) {
      const bool on = s == 0 || 16 * k + 2 * wave + 1 < nitem;
      const size_t po = (size_t)(unsigned)__builtin_amdgcn_readlane(phys, on ? 2 * k + s : 2 * k) * PAGE_BYTES;
      const auto rp = __builtin_amdgcn_make_buffer_rsrc((void*)(base + po), 0, on ? PAGE_SPAN : 0, 0x00020000);
#pragma unroll
      for (int i = 0; i < 4; ++i) buf[s][i] = __builtin_amdgcn_raw_buffer_load_b128(rp, off + (i & 1) * TOK_BYTES, (i >> 1) * 2 * TOK_BYTES, AUX_NT);
    }
    __builtin_amdgcn_sched_barrier(0);
  };
  unsigned tiss = 0, tarr = 0;
  const int lat8 = 8 * (ADV_LAT + ((((uintptr_t)kbase >> 8) & 3) == 1 ? ADV_SLOW : 0));
  auto pace = [&](int n, bool steer) {
    int adv = 0;
    if (steer) {
      asm volatile("" ::: "memory");
      *(unsigned char*)(smem + CW_LAT + wave) = (unsigned char)min(tarr - tiss, 200u);
      asm volatile("" ::: "memory");
      const u32x2 lt = *(const u32x2*)(smem + CW_LAT);
      adv = __builtin_amdgcn_readfirstlane(*(const int*)(smem + CW_ADV));
      const int sum = __builtin_amdgcn_readfirstlane(__builtin_amdgcn_sad_u8(lt[0], 0, __builtin_amdgcn_sad_u8(lt[1], 0, 0)));
      const int line = max(n - 6, 0) * ((dpace * ADV_MAX) >> 10);
      const int step = min(max((lat8 - sum + (((line - adv) * ADV_PULL) >> 10)) * ADV_GAIN, -ADV_DOWN * 16), ADV_UP * 16);
      adv = min(max(adv + ((dpace * step) >> 14), -n * ((dpace * ADV_MIN) >> 10)), line);
      *(int*)(smem + CW_ADV) = adv;
    }
    const unsigned tg = tpace + (((n - 1) * dpace - max(n - nfast, 0) * dcut - adv) >> 8);
    while ((int)((tiss = now()) - tg) < 0) __builtin_amdgcn_s_sleep(1);
  };
  if (early > 0) issue(kbase, koff, 0);
  if (wave == 0) *(unsigned*)(smem + CW_ORIGIN) = tstart | 1u;

  unsigned contrib;
  {
    const int s = part >> 2;
    const int e = __builtin_amdgcn_readlane(send, s) + P, n = __builtin_amdgcn_readlane(cnt, s), ps = min(P, __builtin_amdgcn_readlane(npg, s));
    const int nxt = __builtin_amdgcn_ds_bpermute((lane + 1) * 4, bnd);
    contrib = (unsigned)__builtin_amdgcn_ballot_w64(nxt > bnd && (bnd < ps || (n > 0 && bnd < e && nxt > e - n)));
  }
  const unsigned um = nitem > 0 ? (unsigned)__builtin_amdgcn_ballot_w64(i0 < min(P, npg) || (cnt > 0 && i0 - P < send && i1 - P > send - cnt)) & 0xffu : 0u;
  const int nseq = __popc(um);
  const bool wide = nseq > 2;
  {
    const float span = (float)(npriv * 16 + P * WSH) * (PACE * (1.f / 32.f)) - (wide ? LEAD * 256.f : nseq == 2 ? LEAD2 * 256.f : 0.f);
    dpace = __builtin_amdgcn_readfirstlane((int)(fmaxf(span, 0.f) * __builtin_amdgcn_rcpf((float)max(2 * ntile, 1))));
    nfast = 2 * ntile - ENDN;
    dcut = wide ? 0 : __builtin_amdgcn_readfirstlane((int)((float)dpace * (float)(P * WSH) * __builtin_amdgcn_rcpf((float)(npriv * 16 + P * WSH))));
  }
  do {
    asm volatile("" ::: "memory");
    tpace = __builtin_amdgcn_readfirstlane(*(const unsigned*)(smem + CW_ORIGIN));
  } while (!tpace);
  const unsigned tend = tpace + (unsigned)(((npriv * 16 + P * WSH) >> 5) * PACE >> 8);
  if (nw > 0 && wave >= 2) {
    pace(wave, false);
    issue(kbase, koff, 0);
  }

  while (__hip_atomic_load((unsigned*)(smem + CW_Q), __ATOMIC_RELAXED, __HIP_MEMORY_SCOPE_WORKGROUP) == 0u) __builtin_amdgcn_s_sleep(1);
  asm volatile("" ::: "memory");
  const unsigned epoch = *(const unsigned*)(smem + LDS_EP + lane * 4);
  int lenv[2];
  u32x4 qf[2][4];
#pragma unroll
  for (int cg = 0; cg < 2; ++cg) {
    const int l = __builtin_amdgcn_ds_bpermute((4 * cg + (c >> 2)) * 4, lraw);
#pragma unroll
    for (int i = 0; i < 4; ++i) qf[cg][i] = *(const u32x4*)(smem + LDS_Q + ((cg * 4 + i) * 64 + lane) * 16);
    if (l <= 0) {
#pragma unroll
      for (int i = 0; i < 4; ++i) qf[cg][i] = u32x4{0, 0, 0, 0};
    }
    lenv[cg] = clamp_len(l) - 4 * g;
  }
  const unsigned meta = item < i1 ? (unsigned)(tix & (NBLK - 1)) * PAGE | (item < P ? 0xff0000u : 0x10000u << (tix >> 9)) : 0u;
  const int rk = (wave - nextra) & 7;
  int ncol = wide ? (nseq + 1 - (rk >> 2)) >> 1 : (unsigned)(rk - 1) < 4u ? nseq : 0;
  int mrd, lrd, pcol, phdr;
  unsigned ctv;
  {
    const int u = (lane >> 3) & 3;
    const int rank = ((um >> (lane & 7)) & 1) ? __popc(um & ((1u << (lane & 7)) - 1)) : -1;
    const unsigned long long hit = __builtin_amdgcn_ballot_w64(rank == (wide ? 2 * u + (rk >> 2) : u));
    const unsigned mine8 = (unsigned)(hit >> (8 * u)) & 0xffu;
    const bool on = mine8 != 0 && lane < 32;
    const int col = 4 * __builtin_ctz(mine8 | 0x80u) + (rk & 3);
    mrd = LDS_ML + (col * 40 + 32 + (lane & 7)) * 4;
    lrd = LDS_ML + (col * 40 + (lane & 7) * 4) * 4;
    pcol = col;
    const int slot = WS_SLOT0 + (col * NPART + part) * SLOT_BYTES;
    phdr = on ? slot + 1024 + (lane & 7) * 16 : OOB;
    ctv = (unsigned)__builtin_amdgcn_ds_bpermute(col * 4, (int)epoch) + 1;
  }
  int phdr0 = wide || lane >= 8 ? OOB : phdr;
  int ord = LDS_O + (__builtin_amdgcn_readfirstlane(pcol) * COL_F + 2 * lane) * 4;
  int prow = WS_SLOT0 + (__builtin_amdgcn_readfirstlane(pcol) * NPART + part) * SLOT_BYTES + lane * 16;
  unsigned ct = __builtin_amdgcn_readfirstlane(ctv);
  asm volatile("" : "+v"(mrd), "+v"(lrd), "+v"(ord), "+v"(prow), "+v"(ct), "+v"(phdr0), "+v"(phdr), "+v"(ctv), "+v"(pcol), "+s"(ncol));
  int wm = LDS_ML + (c * 40 + 32 + rk) * 4, wl = LDS_ML + (c * 40 + rk * 4 + g) * 4, wo[2];
#pragma unroll
  for (int cg = 0; cg < 2; ++cg) wo[cg] = ((um >> (4 * cg + (c >> 2))) & 1) ? LDS_O + ((16 * cg + c) * COL_F + rk * HD + 4 * g) * 4 : -1;
  asm volatile("" : "+v"(wm), "+v"(wl), "+v"(wo[0]), "+v"(wo[1]));

  float m[2] = {-INFINITY, -INFINITY}, ms[2] = {0.f, 0.f}, l[2] = {0.f, 0.f};
  f32x4 acc[2][8];
#pragma unroll
  for (int cg = 0; cg < 2; ++cg)
#pragma unroll
    for (int i = 0; i < 8; ++i) acc[cg][i] = f32x4{0.f, 0.f, 0.f, 0.f};
  __builtin_amdgcn_s_setprio(2);

  auto put_stats = [&]() {
#pragma unroll
    for (int cg = 0; cg < 2; ++cg) {
      *(float*)(smem + wm + cg * (16 * 160)) = m[cg] * SC_LOG2;
      *(float*)(smem + wl + cg * (16 * 160)) = l[cg];
    }
  };
  auto flag_stats = [&]() {
    asm volatile("" ::: "memory");
    *(unsigned char*)(smem + CW_STATS + rk) = 1;
  };
  if (nw == 0) {
    put_stats();
    flag_stats();
  }
  const int sc0 = LDS_O + ((lane >> 5) * COL_F + rk * HD) * 4 + (lane & 31) * 16;
  for (int k0 = 0; k0 < nw; k0 += 4) {
    const int k1 = min(k0 + 4, nw), nround = min(ntile - 8 * k0, 32);
    float mx[2] = {-INFINITY, -INFINITY};
    for (int k = k0; k < k1;) {
      u32x4 kt[2][4];
      quad_transpose(buf[0], kt[0]);
      asm volatile("" :: "v"(kt[0][0]));
      tarr = now();
      quad_transpose(buf[1], kt[1]);
      const unsigned mt[2] = {(unsigned)__builtin_amdgcn_readlane((int)meta, 2 * k), (unsigned)__builtin_amdgcn_readlane((int)meta, 2 * k + 1)};
#pragma unroll
      for (int cg = 0; cg < 2; ++cg) {
        if (((mt[0] | mt[1]) >> (16 + 4 * cg)) & 0xfu) {
#pragma unroll
          for (int s = 0; s < 2; ++s) {
            f32x4 x = {0.f, 0.f, 0.f, 0.f};
#pragma unroll
            for (int i = 0; i < 4; ++i) x = mfma32(kt[s][i], qf[cg][i], x);
            const int lim = ((mt[s] >> (16 + 4 * cg + (c >> 2))) & 1) ? lenv[cg] - (int)(mt[s] & 0xffffu) : 0;
#pragma unroll
            for (int e = 0; e < 4; ++e) x[e] = e < lim ? x[e] : -INFINITY;
            mx[cg] = fmx(fmx(fmx(mx[cg], x[0]), x[1]), fmx(x[2], x[3]));
            *(f32x4*)(smem + sc0 + (((k - k0) * 2 + cg) * 4 + 2 * s) * (COL_F * 4)) = x;
          }
        }
      }
      ++k;
      if (k < k1) {
        pace(16 * k0 + 8 * (k - k0) + wave, true);
        issue(kbase, koff, k);
      }
    }
    pace(16 * k0 + nround + wave, true);
    issue(vbase, voff, k0);
#pragma unroll
    for (int cg = 0; cg < 2; ++cg) {
      m[cg] = fmx(m[cg], swap32_max(swap16_max(mx[cg])));
      const float mn = m[cg] == -INFINITY ? 0.f : m[cg] * SC_LOG2;
      if (k0 > 0) {
        const float alpha = __builtin_amdgcn_exp2f(fminf(ms[cg] - mn, 0.f));
        l[cg] *= alpha;
#pragma unroll
        for (int i = 0; i < 8; ++i) acc[cg][i] *= alpha;
      }
      ms[cg] = mn;
    }
    for (int k = k0; k < k1;) {
      const unsigned mu = (unsigned)__builtin_amdgcn_readlane((int)meta, 2 * k) | (unsigned)__builtin_amdgcn_readlane((int)meta, 2 * k + 1);
      u32x4 pb[2];
#pragma unroll
      for (int cg = 0; cg < 2; ++cg) {
        if ((mu >> (16 + 4 * cg)) & 0xfu) {
#pragma unroll
          for (int s = 0; s < 2; ++s) {
            f32x4 x = *(const f32x4*)(smem + sc0 + (((k - k0) * 2 + cg) * 4 + 2 * s) * (COL_F * 4));
#pragma unroll
            for (int e = 0; e < 4; ++e) x[e] = __builtin_amdgcn_exp2f(__builtin_fmaf(x[e], SC_LOG2, -ms[cg]));
            l[cg] += (x[0] + x[1]) + (x[2] + x[3]);
            pb[cg][2 * s] = cvtpk(x[0], x[1]);
            pb[cg][2 * s + 1] = cvtpk(x[2], x[3]);
          }
        }
      }
      ++k;
      if (k == nw) put_stats();
      u32x4 va[8];
#pragma unroll
      for (int s = 0; s < 2; ++s) {
#pragma unroll
        for (int i = 0; i < 8; ++i) {
          const unsigned sel = (i & 1) ? 0x07060302u : 0x05040100u;
          va[i][2 * s] = __builtin_amdgcn_perm(buf[s][1][i >> 1], buf[s][0][i >> 1], sel);
          va[i][2 * s + 1] = __builtin_amdgcn_perm(buf[s][3][i >> 1], buf[s][2][i >> 1], sel);
        }
        if (s == 0) {
          asm volatile("" :: "v"(va[7][1]));
          tarr = now();
        }
      }
      asm volatile("" :: "v"(va[7][3]));
      if (k == nw) flag_stats();
#pragma unroll
      for (int cg = 0; cg < 2; ++cg) {
        if ((mu >> (16 + 4 * cg)) & 0xfu) {
#pragma unroll
          for (int i = 0; i < 8; ++i) acc[cg][i] = mfma32(va[i], pb[cg], acc[cg][i]);
        }
      }
      if (k < nw) {
        const bool more_v = k < k1;
        pace(more_v ? 16 * k0 + nround + 8 * (k - k0) + wave : 16 * k1 + wave, true);
        issue(more_v ? vbase : kbase, more_v ? voff : koff, k);
      }
    }
  }
  __builtin_amdgcn_s_waitcnt(0x0F70);

#pragma unroll
  for (int cg = 0; cg < 2; ++cg) {
    if (wo[cg] >= 0) {
#pragma unroll
      for (int i = 0; i < 8; ++i) *(f32x4*)(smem + wo[cg] + 64 * i) = acc[cg][i];
    }
  }
  asm volatile("" ::: "memory");
  *(unsigned char*)(smem + CW_ROWS + rk) = 1;
  __builtin_amdgcn_s_setprio(0);

  if (ncol > 0) {
    for (;;) {
      asm volatile("" ::: "memory");
      const u32x2 dn = *(const u32x2*)(smem + CW_ROWS);
      if (__builtin_amdgcn_ballot_w64((dn[0] & (dn[1] | 0x01010100u)) == 0x01010101u)) break;
    }
    asm volatile("" ::: "memory");
    f32x2 o[NWAVE];
#pragma unroll
    for (int w = 0; w < 5; ++w) o[w] = *(const f32x2*)(smem + ord + w * (HD * 4));
    float mw;
    f32x4 lw;
    for (;;) {
      asm volatile("" ::: "memory");
      const unsigned dn = *(const unsigned*)(smem + CW_STATS + 4);
      asm volatile("" ::: "memory");
      mw = *(const float*)(smem + mrd);
      lw = *(const f32x4*)(smem + lrd);
      asm volatile("" : "+v"(mw), "+v"(lw));
      if (__builtin_amdgcn_ballot_w64(dn == 0x01010101u)) break;
    }
    const float M = max8(mw);
    const float f = __builtin_amdgcn_exp2f(mw - M);
    const float L = sum8(f * ((lw[0] + lw[1]) + (lw[2] + lw[3])));
    const u32x4 hdr = {__float_as_uint(M), ctv, __float_as_uint(L), ctv};
    __builtin_amdgcn_raw_buffer_store_b128(hdr, rws, phdr0, 0, AUX_SC1);
    float fw[8];
#pragma unroll
    for (int w = 0; w < 8; ++w) fw[w] = rdlane(f, w);
    asm volatile("" : "+s"(fw[0]), "+s"(fw[1]), "+s"(fw[2]), "+s"(fw[3]), "+s"(fw[4]), "+s"(fw[5]), "+s"(fw[6]), "+s"(fw[7]));
    f32x2 a = (o[0] * fw[0] + o[2] * fw[2] + o[4] * fw[4]) + (o[1] * fw[1] + o[3] * fw[3]);
    asm volatile("" : "+v"(a));
    for (;;) {
      asm volatile("" ::: "memory");
      const unsigned dn = *(const unsigned*)(smem + CW_ROWS + 4);
      asm volatile("" ::: "memory");
#pragma unroll
      for (int w = 5; w < NWAVE; ++w) o[w] = *(const f32x2*)(smem + ord + w * (HD * 4));
      asm volatile("" : "+v"(o[5]), "+v"(o[6]), "+v"(o[7]));
      if (__builtin_amdgcn_ballot_w64(dn == 0x01010101u)) break;
    }
    a = (a + o[5] * fw[5]) + (o[6] * fw[6] + o[7] * fw[7]);
    __builtin_amdgcn_raw_buffer_store_b128(u32x4{__float_as_uint(a[0]), ct, __float_as_uint(a[1]), ct}, rws, prow, 0, AUX_SC1);
    const int phdr1 = !wide && lane < 8 ? OOB : phdr;
#pragma unroll 1
    for (int u = 1; u < ncol; ++u) {
      const int col = __builtin_amdgcn_readlane(pcol, 8 * u);
#pragma unroll
      for (int w = 0; w < NWAVE; ++w) o[w] = *(const f32x2*)(smem + LDS_O + (col * COL_F + 2 * lane) * 4 + w * (HD * 4));
      if (u == ncol - 1) __builtin_amdgcn_raw_buffer_store_b128(hdr, rws, phdr1, 0, AUX_SC1);
      ct = __builtin_amdgcn_readlane(ctv, 8 * u);
      a = wsum8(o, f, 8 * u);
      __builtin_amdgcn_raw_buffer_store_b128(u32x4{__float_as_uint(a[0]), ct, __float_as_uint(a[1]), ct}, rws,
                                             WS_SLOT0 + (col * NPART + part) * SLOT_BYTES + lane * 16, 0, AUX_SC1);
    }
    if (wide && ncol == 1) __builtin_amdgcn_raw_buffer_store_b128(hdr, rws, phdr1, 0, AUX_SC1);
  }

  if (rk == 0) {
    const unsigned tag = (unsigned)__builtin_amdgcn_readlane((int)epoch, part) + 1;
    const bool oddi = (lane >> 3) & 1;
    int orow = (((part >> 2) * NH + 4 * kvh + (part & 3)) * HD + ((lane >> 1) & 3) * 32 + (lane & 1) * 16 + (lane >> 4) * 2 + (oddi ? 8 : 0)) * 2;
    float Mr = -3.0e38f, Lr = 0.f, a0 = 0.f, a1 = 0.f;
    unsigned spin = 0;
    const int ncon = __popc(contrib);
    const int crank = lane < 32 && ((contrib >> lane) & 1) ? __popc(contrib & ((1u << lane) - 1)) : 63;
    const int cpart = __builtin_amdgcn_ds_permute(crank * 4, lane);
    for (int base = 0; base < ncon; base += GS) {
      const int u16 = lane & 15;
      const bool hon = u16 < GS && base + u16 < ncon;
      const int sl = __builtin_amdgcn_ds_bpermute((base + u16) * 4, cpart);
      const int so = hon ? WS_SLOT0 + (part * NPART + sl) * SLOT_BYTES : OOB;
      const unsigned htag = hon ? tag : 0u;
      const float hbias = hon ? 0.f : -INFINITY;
      int doff[GS];
      unsigned etag[GS];
#pragma unroll
      for (int u = 0; u < GS; ++u) {
        doff[u] = __builtin_amdgcn_readlane(so, u) + lane * 16;
        etag[u] = __builtin_amdgcn_readlane(htag, u);
        asm volatile("" : "+v"(doff[u]), "+s"(etag[u]));
      }
      asm volatile("" : "+v"(orow));
      u32x4 h, d[GS];
      const int eoff = lane == 0 && base + GS >= ncon ? part * 4 : OOB;
      for (;;) {
        asm volatile("" ::: "memory");
        const int av = __builtin_amdgcn_readfirstlane(*(const int*)(smem + CW_ADV));
        if (wide || (int)(now() - (tend + POLL_AT - (av >> 8))) >= 0) break;
        __builtin_amdgcn_s_sleep(2);
      }
#pragma unroll 1
      for (; spin < SPIN_CAP; ++spin) {
        asm volatile("" ::: "memory");
        h = __builtin_amdgcn_raw_buffer_load_b128(rws, so + 1024, 0, AUX_SC1);
        if (!__builtin_amdgcn_ballot_w64(__builtin_amdgcn_bitop3_b32(h[1], h[3], htag, 0x7E) != 0)) break;
      }
#pragma unroll
      for (int u = GS - 1; u >= 0; --u) d[u] = __builtin_amdgcn_raw_buffer_load_b128(rws, doff[u], 0, AUX_SC1);
      __builtin_amdgcn_raw_buffer_store_b32(tag, rws, eoff, 0, AUX_SC1);
      __builtin_amdgcn_sched_barrier(0);
      const float mu = __uint_as_float(h[0]) + hbias;
      float M = max8(mu);
      M = fmx(fmx(M, dppf<XOR15>(M)), Mr);
      float f = __builtin_amdgcn_exp2f(mu - M), fr = __builtin_amdgcn_exp2f(Mr - M);
      const float l8 = sum8(f * __uint_as_float(h[2]));
      Lr = (l8 + dppf<XOR15>(l8)) + fr * Lr;
      Mr = M;
      if (base + GS >= ncon) {
        const float inv = __builtin_amdgcn_rcpf(Lr);
        f *= inv;
        fr *= inv;
      }
      a0 *= fr;
      a1 *= fr;
      float fu[GS], b0 = 0.f, b1 = 0.f;
#pragma unroll
      for (int u = 0; u < GS; ++u) {
        fu[u] = rdlane(f, u);
        asm volatile("" : "+s"(fu[u]));
      }
      asm volatile("s_nop 1" : "+v"(a0), "+v"(a1), "+v"(b0), "+v"(b1));
#pragma unroll 1
      for (; spin < SPIN_CAP; ++spin) {
        unsigned bad = 0;
#pragma unroll
        for (int u = GS - 1; u >= 0; --u) bad |= __builtin_amdgcn_bitop3_b32(d[u][1], d[u][3], etag[u], 0x7E);
        if (!__builtin_amdgcn_ballot_w64(bad != 0)) break;
        asm volatile("" ::: "memory");
#pragma unroll
        for (int u = GS - 1; u >= 0; --u) d[u] = __builtin_amdgcn_raw_buffer_load_b128(rws, doff[u], 0, AUX_SC1);
        __builtin_amdgcn_raw_buffer_store_b32(tag, rws, eoff, 0, AUX_SC1);
      }
#pragma unroll
      for (int u = 0; u < GS; u += 2)
        asm("v_fmac_f32 %0, %4, %6\n\tv_fmac_f32 %1, %4, %7\n\tv_fmac_f32 %2, %5, %8\n\tv_fmac_f32 %3, %5, %9"
            : "+v"(a0), "+v"(a1), "+v"(b0), "+v"(b1)
            : "s"(fu[u]), "s"(fu[u + 1]), "v"(d[u][0]), "v"(d[u][2]), "v"(d[u + 1][0]), "v"(d[u + 1][2]));
      a0 += b0;
      a1 += b1;
    }
    const unsigned pk = cvtpk(a0, a1), pp = dppu<0x128>(pk);
    __builtin_amdgcn_raw_buffer_store_b32(__builtin_amdgcn_perm(pk, pp, oddi ? 0x07060302u : 0x01000504u), rout, orow, 0, AUX_SC1);
    asm volatile(".Lcode_end:");
  }
}

}

extern "C" void kernel_launch(void* const* d_in, const int* in_sizes, int n_in, void* d_out, int out_size, void* d_ws,
                              size_t ws_size, hipStream_t stream) {
  (void)n_in; (void)out_size; (void)ws_size;
  (void)hipFuncSetAttribute((const void*)attn_fused, hipFuncAttributeMaxDynamicSharedMemorySize, LDS_BYTES);
  hipLaunchKernelGGL(attn_fused, dim3(NKVH * NPART), dim3(NTHR), LDS_BYTES, stream, (const char*)d_in[0], (const char*)d_in[1],
                     (const char*)d_in[2], (const int*)d_in[3], (const int*)d_in[4], (char*)d_ws, (__hip_bfloat16*)d_out, in_sizes[1] / (PAGE_BYTES / 2));
}
```

```cpp
#include <hip/hip_runtime.h>
#include <hip/hip_bf16.h>
#include <stdint.h>

namespace {

constexpr int NBATCH = 8, NH = 32, NKVH = 8, HD = 128, PAGE = 16, NBLK = 512, LK = NBLK * PAGE;
constexpr int NPART = 32, NWAVE = 8, NTHR = NWAVE * 64, NCOL = 32;
constexpr int WSH = 17, WFAR = 22;
static_assert(WFAR >= WSH && WSH >= 16, "split points are a min / max of three lines");
constexpr int PACE = 480;
constexpr int LEAD = 170;
constexpr int LEAD2 = 25;
constexpr int ENDN = 6;
constexpr int ADV_LAT = 105;
constexpr int ADV_SLOW = 25;
constexpr int ADV_PULL = 6;
constexpr int ADV_GAIN = 8;
constexpr int ADV_UP = 150, ADV_DOWN = 80;
constexpr int ADV_MAX = 115, ADV_MIN = 60;
constexpr int POLL_AT = -20;
constexpr int PF_N = 2;
constexpr int TOK_BYTES = NKVH * HD * 2;
constexpr int PAGE_BYTES = PAGE * TOK_BYTES;
constexpr int PAGE_SPAN = (PAGE - 1) * TOK_BYTES + HD * 2;
constexpr float SC_LOG2 = 0.08838834764831845f * 1.4426950408889634f;
constexpr int AUX_NT = 2, AUX_SC1 = 16;
constexpr unsigned SPIN_CAP = 1u << 20;
constexpr int OOB = 0x70000000;
constexpr int GS = 12;

typedef __attribute__((ext_vector_type(8))) __bf16 bf16x8;
typedef __attribute__((ext_vector_type(2))) __bf16 bf16x2;
typedef __attribute__((ext_vector_type(4))) float f32x4;
typedef __attribute__((ext_vector_type(2))) float f32x2;
typedef __attribute__((ext_vector_type(4))) unsigned u32x4;
typedef __attribute__((ext_vector_type(2))) unsigned u32x2;
typedef __attribute__((ext_vector_type(8))) int i32x8;
extern "C" __device__ int llvm_writelane(int, int, int) __asm("llvm.amdgcn.writelane.i32");

constexpr int COL_F = NWAVE * HD + 4;
constexpr int LDS_TBL = 0;
constexpr int LDS_PW = LDS_TBL + NBLK * NBATCH * 4;
constexpr int CW_PREFIX = LDS_PW;
constexpr int CW_Q = LDS_PW + 16;
constexpr int CW_ORIGIN = LDS_PW + 20;
constexpr int CW_STATS = LDS_PW + 24;
constexpr int CW_ROWS = LDS_PW + 32;
constexpr int CW_LAT = LDS_PW + 40;
constexpr int CW_ADV = LDS_PW + 48;
constexpr int LDS_EP = LDS_PW + 64;
constexpr int LDS_Q = LDS_EP + 256;
constexpr int LDS_ML = LDS_Q + 8192;
constexpr int LDS_O = LDS_ML + NCOL * 40 * 4;
constexpr int LDS_BYTES = LDS_O + NCOL * COL_F * 4;

constexpr int WS_HEAD = 2 << 20, WS_SLOT0 = 1024, SLOT_BYTES = 1152;
constexpr int WS_BYTES = WS_SLOT0 + NCOL * NPART * SLOT_BYTES;

__device__ __forceinline__ f32x4 mfma32(u32x4 a, u32x4 b, f32x4 c) {
  return __builtin_amdgcn_mfma_f32_16x16x32_bf16(__builtin_bit_cast(bf16x8, a), __builtin_bit_cast(bf16x8, b), c, 0, 0, 0);
}
__device__ __forceinline__ unsigned cvtpk(float lo, float hi) {
  return __builtin_bit_cast(unsigned, __builtin_convertvector(f32x2{lo, hi}, bf16x2));
}
__device__ __forceinline__ float fmx(float a, float b) { return __builtin_elementwise_maximum(a, b); }
__device__ __forceinline__ float swap16_max(float x) {
  auto s = __builtin_amdgcn_permlane16_swap(__float_as_uint(x), __float_as_uint(x), false, false);
  return fmx(__uint_as_float(s[0]), __uint_as_float(s[1]));
}
__device__ __forceinline__ float swap32_max(float x) {
  auto s = __builtin_amdgcn_permlane32_swap(__float_as_uint(x), __float_as_uint(x), false, false);
  return fmx(__uint_as_float(s[0]), __uint_as_float(s[1]));
}
template <int CTRL> __device__ __forceinline__ unsigned dppu(unsigned x) {
  return (unsigned)__builtin_amdgcn_mov_dpp((int)x, CTRL, 0xf, 0xf, true);
}
template <int CTRL> __device__ __forceinline__ float dppf(float x) { return __uint_as_float(dppu<CTRL>(__float_as_uint(x))); }
constexpr int XOR1 = 0xB1, XOR2 = 0x4E, XOR3 = 0x1B, XOR7 = 0x141, XOR15 = 0x140;
__device__ __forceinline__ float max8(float x) {
  x = fmx(x, dppf<XOR7>(x));
  return fmx(fmx(fmx(x, dppf<XOR1>(x)), dppf<XOR2>(x)), dppf<XOR3>(x));
}
__device__ __forceinline__ float sum8(float x) {
  x += dppf<XOR1>(x); x += dppf<XOR2>(x); return x + dppf<XOR7>(x);
}
__device__ __forceinline__ float rdlane(float x, int l) { return __int_as_float(__builtin_amdgcn_readlane(__float_as_int(x), l)); }
__device__ __forceinline__ int clamp_len(int l) { return (l <= 0 || l > LK) ? LK : l; }
__device__ __forceinline__ unsigned now() { return (unsigned)__builtin_amdgcn_s_memrealtime(); }
__device__ __forceinline__ f32x2 wsum8(const f32x2 (&o)[8], float f, int l0) {
  float s[8];
#pragma unroll
  for (int w = 0; w < 8; ++w) s[w] = rdlane(f, l0 + w);
  asm volatile("" : "+s"(s[0]), "+s"(s[1]), "+s"(s[2]), "+s"(s[3]), "+s"(s[4]), "+s"(s[5]), "+s"(s[6]), "+s"(s[7]));
  const f32x2 a = o[0] * s[0] + o[2] * s[2] + o[4] * s[4] + o[6] * s[6];
  const f32x2 b = o[1] * s[1] + o[3] * s[3] + o[5] * s[5] + o[7] * s[7];
  return a + b;
}

#define CND4(D, A, B, P)                                                                        \
  "v_cndmask_b32_dpp %[" D "0], %[" A "0], %[" B "0], vcc " P " row_mask:0xf bank_mask:0xf\n\t" \
  "v_cndmask_b32_dpp %[" D "1], %[" A "1], %[" B "1], vcc " P " row_mask:0xf bank_mask:0xf\n\t" \
  "v_cndmask_b32_dpp %[" D "2], %[" A "2], %[" B "2], vcc " P " row_mask:0xf bank_mask:0xf\n\t" \
  "v_cndmask_b32_dpp %[" D "3], %[" A "3], %[" B "3], vcc " P " row_mask:0xf bank_mask:0xf\n\t"
#define SWAP_STAGE(MASK, P)                                                                                              \
  asm("s_mov_b32 vcc_lo, " MASK "\n\ts_mov_b32 vcc_hi, " MASK "\n\t" CND4("x", "b", "a", P) "s_not_b64 vcc, vcc\n\t" CND4("y", "a", "b", P) \
      "s_nop 1"                                                                                                          \
      : [x0] "=&v"(x[0]), [x1] "=&v"(x[1]), [x2] "=&v"(x[2]), [x3] "=&v"(x[3]), [y0] "=&v"(y[0]), [y1] "=&v"(y[1]),      \
        [y2] "=&v"(y[2]), [y3] "=&v"(y[3])                                                                               \
      : [a0] "v"(a[0]), [a1] "v"(a[1]), [a2] "v"(a[2]), [a3] "v"(a[3]), [b0] "v"(b[0]), [b1] "v"(b[1]), [b2] "v"(b[2]),  \
        [b3] "v"(b[3])                                                                                                   \
      : "vcc", "scc")
__device__ __forceinline__ void swap1(const u32x4& a, const u32x4& b, u32x4& ox, u32x4& oy) {
  unsigned x[4], y[4];
  SWAP_STAGE("0x55555555", "quad_perm:[1,0,3,2]");
  ox = u32x4{x[0], x[1], x[2], x[3]};
  oy = u32x4{y[0], y[1], y[2], y[3]};
}
__device__ __forceinline__ void swap2(const u32x4& a, const u32x4& b, u32x4& ox, u32x4& oy) {
  unsigned x[4], y[4];
  SWAP_STAGE("0x33333333", "quad_perm:[2,3,0,1]");
  ox = u32x4{x[0], x[1], x[2], x[3]};
  oy = u32x4{y[0], y[1], y[2], y[3]};
}
__device__ __forceinline__ void quad_transpose(const u32x4 (&in)[4], u32x4 (&out)[4]) {
  u32x4 t[4];
  swap1(in[0], in[1], t[0], t[1]);
  swap1(in[2], in[3], t[2], t[3]);
  swap2(t[0], t[2], out[0], out[2]);
  swap2(t[1], t[3], out[1], out[3]);
}

__global__ __launch_bounds__(NTHR, 2) void attn_fused(const char* __restrict__ q, const char* __restrict__ kc,
                                                      const char* __restrict__ vc, const int* __restrict__ bt,
                                                      const int* __restrict__ lens, char* __restrict__ ws,
                                                      __hip_bfloat16* __restrict__ out, int nphys) {
  extern __shared__ __attribute__((aligned(16))) char smem[];
  const int tid = threadIdx.x, lane = tid & 63, wave = __builtin_amdgcn_readfirstlane(tid >> 6);
  if (tid < 16) ((unsigned*)(smem + LDS_PW))[tid] = 0u;
  unsigned warm0 = 0;
  if (wave == 0) {
    unsigned long long pc;
    unsigned len;
    asm volatile("s_getpc_b64 %0\n.Lcode_begin:\n\ts_mov_b32 %1, .Lcode_end-.Lcode_begin" : "=s"(pc), "=s"(len));
    const auto rc = __builtin_amdgcn_make_buffer_rsrc((void*)pc, 0, max((int)len, 0), 0x00020000);
    warm0 = __builtin_amdgcn_raw_buffer_load_b32(rc, (lane * 8 + ((blockIdx.x >> 3) & 7)) * 128, 0, 0);
  }
  asm volatile("s_waitcnt lgkmcnt(0)" :: "s"(q), "s"(kc), "s"(vc), "s"(bt), "s"(lens), "s"(ws), "s"(out), "s"(nphys) : "memory");
  const i32x8 slen = *(const __attribute__((address_space(4))) i32x8*)(uintptr_t)lens;
  const int g = lane >> 4, c = lane & 15;
  const int kvh = blockIdx.x & 7, part = blockIdx.x >> 3;

  u32x2 y[NBATCH];
  const bool btw = (unsigned)(wave - 2) < 4u;
  const int bcol = (tid - 128) * 8;
  if (btw) {
    const auto rbt = __builtin_amdgcn_make_buffer_rsrc((void*)bt, 0, NBATCH * NBLK * 4, 0x00020000);
#pragma unroll
    for (int b = 0; b < NBATCH; ++b) y[b] = __builtin_amdgcn_raw_buffer_load_b64(rbt, bcol + (b & 1) * 2048, (b >> 1) * 4096, 0);
  }
  asm volatile("s_barrier" ::: "memory");
  const auto rws = __builtin_amdgcn_make_buffer_rsrc((void*)(ws + (size_t)kvh * WS_HEAD), 0, WS_BYTES, 0x00020000);
  const auto rout = __builtin_amdgcn_make_buffer_rsrc((void*)out, 0, NBATCH * NH * HD * 2, 0x00020000);
  const char* kbase = kc + kvh * (HD * 2);
  const char* vbase = vc + kvh * (HD * 2);
  int koff = ((c >> 2) * 4) * TOK_BYTES + (4 * g + (c & 3)) * 16;
  int voff = 4 * g * TOK_BYTES + c * 16;
  asm volatile("" : "+v"(koff), "+v"(voff), "+s"(kbase), "+s"(vbase));
  if (btw) {
#pragma unroll
    for (int b = 0; b < NBATCH; ++b) *(u32x2*)(smem + LDS_TBL + b * (NBLK * 4) + bcol) = y[b];
    unsigned d0 = y[0][0] ^ y[1][0], d1 = y[0][1] ^ y[1][1];
#pragma unroll
    for (int b = 2; b < NBATCH; ++b) {
      d0 = __builtin_amdgcn_bitop3_b32(d0, y[0][0], y[b][0], 0xF6);
      d1 = __builtin_amdgcn_bitop3_b32(d1, y[0][1], y[b][1], 0xF6);
    }
    unsigned f0, f1;
    asm("s_ff1_i32_b64 %0, %1" : "=s"(f0) : "s"(__builtin_amdgcn_ballot_w64(d0 != 0)));
    asm("s_ff1_i32_b64 %0, %1" : "=s"(f1) : "s"(__builtin_amdgcn_ballot_w64(d1 != 0)));
    asm volatile("" ::: "memory");
    ((unsigned*)(smem + CW_PREFIX))[wave - 2] = (((wave - 2) * 128) | min(2 * f0, 2 * f1 + 1)) + 1;
  }
  if (wave == 6) {
    const auto rq = __builtin_amdgcn_make_buffer_rsrc((void*)(q + 4 * kvh * (HD * 2)), 0, NBATCH * NH * HD * 2 - 4 * kvh * (HD * 2), 0x00020000);
    u32x4 qs[NBATCH];
#pragma unroll
    for (int b = 0; b < NBATCH; ++b) qs[b] = __builtin_amdgcn_raw_buffer_load_b128(rq, lane * 16, b * (NH * HD * 2), 0);
    const unsigned e = __builtin_amdgcn_raw_buffer_load_b32(rws, lane < 32 ? lane * 4 : WS_SLOT0 + (part * NPART + lane - 32) * SLOT_BYTES + 1024, 0, AUX_SC1);
#pragma unroll
    for (int b = 0; b < NBATCH; ++b) *(u32x4*)(smem + LDS_Q + (((b >> 2) * 4 + (c & 3)) * 64 + (c >> 2) * 16 + (b & 3) * 4 + g) * 16) = qs[b];
    *(unsigned*)(smem + LDS_EP + lane * 4) = e;
    asm volatile("" ::: "memory");
    *(unsigned*)(smem + CW_Q) = 1u;
  }
  if (wave == 7) {
    static_assert(PF_N == 2, "32 lanes per page");
    const auto rk = __builtin_amdgcn_make_buffer_rsrc((void*)kc, 0, (unsigned)nphys * PAGE_BYTES, 0x00020000);
    const auto rv = __builtin_amdgcn_make_buffer_rsrc((void*)vc, 0, (unsigned)nphys * PAGE_BYTES, 0x00020000);
    const unsigned idx = part * PF_N + (lane >> 5), even = (idx * 2 + 1) * nphys / (2 * NPART * PF_N);
    const unsigned far0 = 0x80000000u >> __builtin_clz(nphys), nfar = (unsigned)nphys - far0;
    const unsigned page = nfar >= 128u ? far0 + (idx * nfar * 5 >> 9) : even;
    const unsigned off = page * PAGE_BYTES + ((lane >> 1) & 15) * TOK_BYTES + kvh * (HD * 2) + (lane & 1) * 128;
    const unsigned toff = (lane & 31) == 0 ? even * PAGE_BYTES + kvh * (HD * 2) : (unsigned)OOB;
    const unsigned tk = __builtin_amdgcn_raw_buffer_load_b32(rk, toff, 0, 0);
    const unsigned wk = __builtin_amdgcn_raw_buffer_load_b32(rk, off, 0, 0), wv = __builtin_amdgcn_raw_buffer_load_b32(rv, off, 0, 0);
    const unsigned tv = __builtin_amdgcn_raw_buffer_load_b32(rv, toff, 0, 0);
    asm volatile("" :: "v"(wk), "v"(wv), "v"(tk), "v"(tv));
  }
  asm volatile("" :: "v"(warm0));
  __builtin_amdgcn_s_waitcnt(0x0F70);
  int lraw = 1;
#pragma unroll
  for (int b = 0; b < NBATCH; ++b) lraw = llvm_writelane(slen[b], b, lraw);
  const int npg8 = (clamp_len(lraw) + PAGE - 1) >> 4;
  int nmax = max(npg8, (int)dppu<XOR1>(npg8));
  nmax = max(nmax, (int)dppu<XOR2>(nmax));
  nmax = __builtin_amdgcn_readfirstlane(max(nmax, (int)dppu<XOR7>(nmax)));
  int npg = lane < NBATCH ? npg8 : (1 << 20);
  unsigned farpage = 0x80000000u >> __builtin_clz(nphys);
  int lastix = LDS_TBL + ((lane & 7) * NBLK + npg8 - 1) * 4;
  int rest = max(32 - lane, 0), soff0 = lane * NBLK, item0 = 16 * (lane >> 1) + 2 * wave + (lane & 1);
  asm volatile("" : "+v"(npg), "+s"(nmax), "+s"(farpage), "+v"(lastix), "+v"(rest), "+v"(soff0), "+v"(item0));

  unsigned p1, last;
  do {
    asm volatile("" ::: "memory");
    const u32x4 a = *(const u32x4*)(smem + CW_PREFIX);
    asm volatile("" ::: "memory");
    last = *(const unsigned*)(smem + lastix);
    asm volatile("" : "+v"(last));
    p1 = __builtin_amdgcn_readfirstlane(min(min(a[0], a[1]), min(a[2], a[3])));
  } while (!p1);
  asm volatile("" ::: "memory");
  int P;
  asm("s_min_u32 %0, %1, %2" : "=s"(P) : "s"(p1 - 1), "s"(nmax) : "scc");
  const int cnt = max(npg - P, 0);
  int send = cnt;
  send += (int)dppu<0x111>(send);
  send += (int)dppu<0x112>(send);
  send += (int)dppu<0x114>(send);
  const int npriv = __builtin_amdgcn_readlane(send, NBATCH - 1);
  int bnd;
  {
    const int f = __builtin_ctz((unsigned)__builtin_amdgcn_ballot_w64(last >= farpage) | 0x100u);
    const int nfar = f < NBATCH ? npriv - __builtin_amdgcn_readlane(send - cnt, f) : 0;
    const unsigned wt = npriv * 16 + P * WSH + nfar * (WFAR - 16);
    const unsigned x = wt * (unsigned)lane;
    bnd = max((int)__umulhi(x, (unsigned)((1ull << 27) / WSH)), ((int)(x >> 5) - P * (WSH - 16)) >> 4);
    bnd = min(bnd, P + npriv - (int)__umulhi(wt * (unsigned)rest, (unsigned)((1ull << 27) / WFAR)));
  }
  const int i0 = __builtin_amdgcn_readlane(bnd, part), i1 = __builtin_amdgcn_readlane(bnd, part + 1);
  const int nitem = i1 - i0, ntile = (nitem + 1) >> 1, nextra = ntile & 7;
  const int nw = (ntile >> 3) + (wave < nextra ? 1 : 0);
  const int soff = soff0 - (send - cnt);
  const int item = i0 + item0;
  int tix = 0;
  for (int cur = max(i0 - P, 0); cur < i1 - P;) {
    const int s = __popcll(__builtin_amdgcn_ballot_w64(send <= cur));
    const int so = __builtin_amdgcn_readlane(soff, s);
    tix = item - P >= cur ? so : tix;
    cur = __builtin_amdgcn_readlane(send, s);
  }
  tix = (tix + item) & (NBATCH * NBLK - 1);
  const unsigned tstart = now();
  const int phys = *(const int*)(smem + LDS_TBL + tix * 4);
  int early = wave < 2 ? nitem - 2 * wave : 0;
  asm volatile("" : "+s"(early));
  unsigned tpace;
  int dpace, nfast, dcut;
  u32x4 buf[2][4];
  auto issue = [&](const char* base, int off, int k) {
#pragma unroll
    for (int s = 0; s < 2; ++s) {
      const bool on = s == 0 || 16 * k + 2 * wave + 1 < nitem;
      const size_t po = (size_t)(unsigned)__builtin_amdgcn_readlane(phys, on ? 2 * k + s : 2 * k) * PAGE_BYTES;
      const auto rp = __builtin_amdgcn_make_buffer_rsrc((void*)(base + po), 0, on ? PAGE_SPAN : 0, 0x00020000);
#pragma unroll
      for (int i = 0; i < 4; ++i) buf[s][i] = __builtin_amdgcn_raw_buffer_load_b128(rp, off + (i & 1) * TOK_BYTES, (i >> 1) * 2 * TOK_BYTES, AUX_NT);
    }
    __builtin_amdgcn_sched_barrier(0);
  };
  unsigned tiss = 0, tarr = 0;
  const int lat8 = 8 * (ADV_LAT + ((((uintptr_t)kbase >> 8) & 3) == 1 ? ADV_SLOW : 0));
  auto pace = [&](int n, bool steer) {
    int adv = 0;
    if (steer) {
      asm volatile("" ::: "memory");
      *(unsigned char*)(smem + CW_LAT + wave) = (unsigned char)min(tarr - tiss, 200u);
      asm volatile("" ::: "memory");
      const u32x2 lt = *(const u32x2*)(smem + CW_LAT);
      adv = __builtin_amdgcn_readfirstlane(*(const int*)(smem + CW_ADV));
      const int sum = __builtin_amdgcn_readfirstlane(__builtin_amdgcn_sad_u8(lt[0], 0, __builtin_amdgcn_sad_u8(lt[1], 0, 0)));
      const int line = max(n - 6, 0) * ((dpace * ADV_MAX) >> 10);
      const int step = min(max((lat8 - sum + (((line - adv) * ADV_PULL) >> 10)) * ADV_GAIN, -ADV_DOWN * 16), ADV_UP * 16);
      adv = min(max(adv + ((dpace * step) >> 14), -n * ((dpace * ADV_MIN) >> 10)), line);
      *(int*)(smem + CW_ADV) = adv;
    }
    const unsigned tg = tpace + (((n - 1) * dpace - max(n - nfast, 0) * dcut - adv) >> 8);
    while ((int)((tiss = now()) - tg) < 0) __builtin_amdgcn_s_sleep(1);
  };
  if (early > 0) issue(kbase, koff, 0);
  if (wave == 0) *(unsigned*)(smem + CW_ORIGIN) = tstart | 1u;

  unsigned contrib;
  {
    const int s = part >> 2;
    const int e = __builtin_amdgcn_readlane(send, s) + P, n = __builtin_amdgcn_readlane(cnt, s), ps = min(P, __builtin_amdgcn_readlane(npg, s));
    const int nxt = __builtin_amdgcn_ds_bpermute((lane + 1) * 4, bnd);
    contrib = (unsigned)__builtin_amdgcn_ballot_w64(nxt > bnd && (bnd < ps || (n > 0 && bnd < e && nxt > e - n)));
  }
  const unsigned um = nitem > 0 ? (unsigned)__builtin_amdgcn_ballot_w64(i0 < min(P, npg) || (cnt > 0 && i0 - P < send && i1 - P > send - cnt)) & 0xffu : 0u;
  const int nseq = __popc(um);
  const bool wide = nseq > 2;
  {
    const float span = (float)(npriv * 16 + P * WSH) * (PACE * (1.f / 32.f)) - (wide ? LEAD * 256.f : nseq == 2 ? LEAD2 * 256.f : 0.f);
    dpace = __builtin_amdgcn_readfirstlane((int)(fmaxf(span, 0.f) * __builtin_amdgcn_rcpf((float)max(2 * ntile, 1))));
    nfast = 2 * ntile - ENDN;
    dcut = wide ? 0 : __builtin_amdgcn_readfirstlane((int)((float)dpace * (float)(P * WSH) * __builtin_amdgcn_rcpf((float)(npriv * 16 + P * WSH))));
  }
  do {
    asm volatile("" ::: "memory");
    tpace = __builtin_amdgcn_readfirstlane(*(const unsigned*)(smem + CW_ORIGIN));
  } while (!tpace);
  const unsigned tend = tpace + (unsigned)(((npriv * 16 + P * WSH) >> 5) * PACE >> 8);
  if (nw > 0 && wave >= 2) {
    pace(wave, false);
    issue(kbase, koff, 0);
  }

  while (__hip_atomic_load((unsigned*)(smem + CW_Q), __ATOMIC_RELAXED, __HIP_MEMORY_SCOPE_WORKGROUP) == 0u) __builtin_amdgcn_s_sleep(1);
  asm volatile("" ::: "memory");
  const unsigned epoch = *(const unsigned*)(smem + LDS_EP + lane * 4);
  int lenv[2];
  u32x4 qf[2][4];
#pragma unroll
  for (int cg = 0; cg < 2; ++cg) {
    const int l = __builtin_amdgcn_ds_bpermute((4 * cg + (c >> 2)) * 4, lraw);
#pragma unroll
    for (int i = 0; i < 4; ++i) qf[cg][i] = *(const u32x4*)(smem + LDS_Q + ((cg * 4 + i) * 64 + lane) * 16);
    if (l <= 0) {
#pragma unroll
      for (int i = 0; i < 4; ++i) qf[cg][i] = u32x4{0, 0, 0, 0};
    }
    lenv[cg] = clamp_len(l) - 4 * g;
  }
  const unsigned meta = item < i1 ? (unsigned)(tix & (NBLK - 1)) * PAGE | (item < P ? 0xff0000u : 0x10000u << (tix >> 9)) : 0u;
  const int rk = (wave - nextra) & 7;
  int ncol = wide ? (nseq + 1 - (rk >> 2)) >> 1 : (unsigned)(rk - 1) < 4u ? nseq : 0;
  int mrd, lrd, pcol, phdr;
  unsigned ctv;
  {
    const int u = (lane >> 3) & 3;
    const int rank = ((um >> (lane & 7)) & 1) ? __popc(um & ((1u << (lane & 7)) - 1)) : -1;
    const unsigned long long hit = __builtin_amdgcn_ballot_w64(rank == (wide ? 2 * u + (rk >> 2) : u));
    const unsigned mine8 = (unsigned)(hit >> (8 * u)) & 0xffu;
    const bool on = mine8 != 0 && lane < 32;
    const int col = 4 * __builtin_ctz(mine8 | 0x80u) + (rk & 3);
    mrd = LDS_ML + (col * 40 + 32 + (lane & 7)) * 4;
    lrd = LDS_ML + (col * 40 + (lane & 7) * 4) * 4;
    pcol = col;
    const int slot = WS_SLOT0 + (col * NPART + part) * SLOT_BYTES;
    phdr = on ? slot + 1024 + (lane & 7) * 16 : OOB;
    ctv = (unsigned)__builtin_amdgcn_ds_bpermute(col * 4, (int)epoch) + 1;
  }
  int phdr0 = wide || lane >= 8 ? OOB : phdr;
  int ord = LDS_O + (__builtin_amdgcn_readfirstlane(pcol) * COL_F + 2 * lane) * 4;
  int prow = WS_SLOT0 + (__builtin_amdgcn_readfirstlane(pcol) * NPART + part) * SLOT_BYTES + lane * 16;
  unsigned ct = __builtin_amdgcn_readfirstlane(ctv);
  asm volatile("" : "+v"(mrd), "+v"(lrd), "+v"(ord), "+v"(prow), "+v"(ct), "+v"(phdr0), "+v"(phdr), "+v"(ctv), "+v"(pcol), "+s"(ncol));
  int wm = LDS_ML + (c * 40 + 32 + rk) * 4, wl = LDS_ML + (c * 40 + rk * 4 + g) * 4, wo[2];
#pragma unroll
  for (int cg = 0; cg < 2; ++cg) wo[cg] = ((um >> (4 * cg + (c >> 2))) & 1) ? LDS_O + ((16 * cg + c) * COL_F + rk * HD + 4 * g) * 4 : -1;
  asm volatile("" : "+v"(wm), "+v"(wl), "+v"(wo[0]), "+v"(wo[1]));

  float m[2] = {-INFINITY, -INFINITY}, ms[2] = {0.f, 0.f}, l[2] = {0.f, 0.f};
  f32x4 acc[2][8];
#pragma unroll
  for (int cg = 0; cg < 2; ++cg)
#pragma unroll
    for (int i = 0; i < 8; ++i) acc[cg][i] = f32x4{0.f, 0.f, 0.f, 0.f};
  __builtin_amdgcn_s_setprio(2);

  auto put_stats = [&]() {
#pragma unroll
    for (int cg = 0; cg < 2; ++cg) {
      *(float*)(smem + wm + cg * (16 * 160)) = m[cg] * SC_LOG2;
      *(float*)(smem + wl + cg * (16 * 160)) = l[cg];
    }
  };
  auto flag_stats = [&]() {
    asm volatile("" ::: "memory");
    *(unsigned char*)(smem + CW_STATS + rk) = 1;
  };
  if (nw == 0) {
    put_stats();
    flag_stats();
  }
  const int sc0 = LDS_O + ((lane >> 5) * COL_F + rk * HD) * 4 + (lane & 31) * 16;
  for (int k0 = 0; k0 < nw; k0 += 4) {
    const int k1 = min(k0 + 4, nw), nround = min(ntile - 8 * k0, 32);
    float mx[2] = {-INFINITY, -INFINITY};
    for (int k = k0; k < k1;) {
      u32x4 kt[2][4];
      quad_transpose(buf[0], kt[0]);
      asm volatile("" :: "v"(kt[0][0]));
      tarr = now();
      quad_transpose(buf[1], kt[1]);
      const unsigned mt[2] = {(unsigned)__builtin_amdgcn_readlane((int)meta, 2 * k), (unsigned)__builtin_amdgcn_readlane((int)meta, 2 * k + 1)};
#pragma unroll
      for (int cg = 0; cg < 2; ++cg) {
        if (((mt[0] | mt[1]) >> (16 + 4 * cg)) & 0xfu) {
#pragma unroll
          for (int s = 0; s < 2; ++s) {
            f32x4 x = {0.f, 0.f, 0.f, 0.f};
#pragma unroll
            for (int i = 0; i < 4; ++i) x = mfma32(kt[s][i], qf[cg][i], x);
            const int lim = ((mt[s] >> (16 + 4 * cg + (c >> 2))) & 1) ? lenv[cg] - (int)(mt[s] & 0xffffu) : 0;
#pragma unroll
            for (int e = 0; e < 4; ++e) x[e] = e < lim ? x[e] : -INFINITY;
            mx[cg] = fmx(fmx(fmx(mx[cg], x[0]), x[1]), fmx(x[2], x[3]));
            *(f32x4*)(smem + sc0 + (((k - k0) * 2 + cg) * 4 + 2 * s) * (COL_F * 4)) = x;
          }
        }
      }
      ++k;
      if (k < k1) {
        pace(16 * k0 + 8 * (k - k0) + wave, true);
        issue(kbase, koff, k);
      }
    }
    pace(16 * k0 + nround + wave, true);
    issue(vbase, voff, k0);
#pragma unroll
    for (int cg = 0; cg < 2; ++cg) {
      m[cg] = fmx(m[cg], swap32_max(swap16_max(mx[cg])));
      const float mn = m[cg] == -INFINITY ? 0.f : m[cg] * SC_LOG2;
      if (k0 > 0) {
        const float alpha = __builtin_amdgcn_exp2f(fminf(ms[cg] - mn, 0.f));
        l[cg] *= alpha;
#pragma unroll
        for (int i = 0; i < 8; ++i) acc[cg][i] *= alpha;
      }
      ms[cg] = mn;
    }
    for (int k = k0; k < k1;) {
      const unsigned mu = (unsigned)__builtin_amdgcn_readlane((int)meta, 2 * k) | (unsigned)__builtin_amdgcn_readlane((int)meta, 2 * k + 1);
      u32x4 pb[2];
#pragma unroll
      for (int cg = 0; cg < 2; ++cg) {
        if ((mu >> (16 + 4 * cg)) & 0xfu) {
#pragma unroll
          for (int s = 0; s < 2; ++s) {
            f32x4 x = *(const f32x4*)(smem + sc0 + (((k - k0) * 2 + cg) * 4 + 2 * s) * (COL_F * 4));
#pragma unroll
            for (int e = 0; e < 4; ++e) x[e] = __builtin_amdgcn_exp2f(__builtin_fmaf(x[e], SC_LOG2, -ms[cg]));
            l[cg] += (x[0] + x[1]) + (x[2] + x[3]);
            pb[cg][2 * s] = cvtpk(x[0], x[1]);
            pb[cg][2 * s + 1] = cvtpk(x[2], x[3]);
          }
        }
      }
      ++k;
      if (k == nw) put_stats();
      u32x4 va[8];
#pragma unroll
      for (int s = 0; s < 2; ++s) {
#pragma unroll
        for (int i = 0; i < 8; ++i) {
          const unsigned sel = (i & 1) ? 0x07060302u : 0x05040100u;
          va[i][2 * s] = __builtin_amdgcn_perm(buf[s][1][i >> 1], buf[s][0][i >> 1], sel);
          va[i][2 * s + 1] = __builtin_amdgcn_perm(buf[s][3][i >> 1], buf[s][2][i >> 1], sel);
        }
        if (s == 0) {
          asm volatile("" :: "v"(va[7][1]));
          tarr = now();
        }
      }
      asm volatile("" :: "v"(va[7][3]));
      if (k == nw) flag_stats();
#pragma unroll
      for (int cg = 0; cg < 2; ++cg) {
        if ((mu >> (16 + 4 * cg)) & 0xfu) {
#pragma unroll
          for (int i = 0; i < 8; ++i) acc[cg][i] = mfma32(va[i], pb[cg], acc[cg][i]);
        }
      }
      if (k < nw) {
        const bool more_v = k < k1;
        pace(more_v ? 16 * k0 + nround + 8 * (k - k0) + wave : 16 * k1 + wave, true);
        issue(more_v ? vbase : kbase, more_v ? voff : koff, k);
      }
    }
  }
  __builtin_amdgcn_s_waitcnt(0x0F70);

#pragma unroll
  for (int cg = 0; cg < 2; ++cg) {
    if (wo[cg] >= 0) {
#pragma unroll
      for (int i = 0; i < 8; ++i) *(f32x4*)(smem + wo[cg] + 64 * i) = acc[cg][i];
    }
  }
  asm volatile("" ::: "memory");
  *(unsigned char*)(smem + CW_ROWS + rk) = 1;
  __builtin_amdgcn_s_setprio(0);

  if (ncol > 0) {
    for (;;) {
      asm volatile("" ::: "memory");
      const u32x2 dn = *(const u32x2*)(smem + CW_ROWS);
      if (__builtin_amdgcn_ballot_w64((dn[0] & (dn[1] | 0x01010100u)) == 0x01010101u)) break;
    }
    asm volatile("" ::: "memory");
    f32x2 o[NWAVE];
#pragma unroll
    for (int w = 0; w < 5; ++w) o[w] = *(const f32x2*)(smem + ord + w * (HD * 4));
    float mw;
    f32x4 lw;
    for (;;) {
      asm volatile("" ::: "memory");
      const unsigned dn = *(const unsigned*)(smem + CW_STATS + 4);
      asm volatile("" ::: "memory");
      mw = *(const float*)(smem + mrd);
      lw = *(const f32x4*)(smem + lrd);
      asm volatile("" : "+v"(mw), "+v"(lw));
      if (__builtin_amdgcn_ballot_w64(dn == 0x01010101u)) break;
    }
    const float M = max8(mw);
    const float f = __builtin_amdgcn_exp2f(mw - M);
    const float L = sum8(f * ((lw[0] + lw[1]) + (lw[2] + lw[3])));
    const u32x4 hdr = {__float_as_uint(M), ctv, __float_as_uint(L), ctv};
    __builtin_amdgcn_raw_buffer_store_b128(hdr, rws, phdr0, 0, AUX_SC1);
    float fw[8];
#pragma unroll
    for (int w = 0; w < 8; ++w) fw[w] = rdlane(f, w);
    asm volatile("" : "+s"(fw[0]), "+s"(fw[1]), "+s"(fw[2]), "+s"(fw[3]), "+s"(fw[4]), "+s"(fw[5]), "+s"(fw[6]), "+s"(fw[7]));
    f32x2 a = (o[0] * fw[0] + o[2] * fw[2] + o[4] * fw[4]) + (o[1] * fw[1] + o[3] * fw[3]);
    asm volatile("" : "+v"(a));
    for (;;) {
      asm volatile("" ::: "memory");
      const unsigned dn = *(const unsigned*)(smem + CW_ROWS + 4);
      asm volatile("" ::: "memory");
#pragma unroll
      for (int w = 5; w < NWAVE; ++w) o[w] = *(const f32x2*)(smem + ord + w * (HD * 4));
      asm volatile("" : "+v"(o[5]), "+v"(o[6]), "+v"(o[7]));
      if (__builtin_amdgcn_ballot_w64(dn == 0x01010101u)) break;
    }
    a = (a + o[5] * fw[5]) + (o[6] * fw[6] + o[7] * fw[7]);
    __builtin_amdgcn_raw_buffer_store_b128(u32x4{__float_as_uint(a[0]), ct, __float_as_uint(a[1]), ct}, rws, prow, 0, AUX_SC1);
    const int phdr1 = !wide && lane < 8 ? OOB : phdr;
#pragma unroll 1
    for (int u = 1; u < ncol; ++u) {
      const int col = __builtin_amdgcn_readlane(pcol, 8 * u);
#pragma unroll
      for (int w = 0; w < NWAVE; ++w) o[w] = *(const f32x2*)(smem + LDS_O + (col * COL_F + 2 * lane) * 4 + w * (HD * 4));
      if (u == ncol - 1) __builtin_amdgcn_raw_buffer_store_b128(hdr, rws, phdr1, 0, AUX_SC1);
      ct = __builtin_amdgcn_readlane(ctv, 8 * u);
      a = wsum8(o, f, 8 * u);
      __builtin_amdgcn_raw_buffer_store_b128(u32x4{__float_as_uint(a[0]), ct, __float_as_uint(a[1]), ct}, rws,
                                             WS_SLOT0 + (col * NPART + part) * SLOT_BYTES + lane * 16, 0, AUX_SC1);
    }
    if (wide && ncol == 1) __builtin_amdgcn_raw_buffer_store_b128(hdr, rws, phdr1, 0, AUX_SC1);
  }

  if (rk == 0) {
    const unsigned tag = (unsigned)__builtin_amdgcn_readlane((int)epoch, part) + 1;
    const bool oddi = (lane >> 3) & 1;
    int orow = (((part >> 2) * NH + 4 * kvh + (part & 3)) * HD + ((lane >> 1) & 3) * 32 + (lane & 1) * 16 + (lane >> 4) * 2 + (oddi ? 8 : 0)) * 2;
    float Mr = -3.0e38f, Lr = 0.f, a0 = 0.f, a1 = 0.f;
    unsigned spin = 0;
    const int ncon = __popc(contrib);
    const int crank = lane < 32 && ((contrib >> lane) & 1) ? __popc(contrib & ((1u << lane) - 1)) : 63;
    const int cpart = __builtin_amdgcn_ds_permute(crank * 4, lane);
    for (int base = 0; base < ncon; base += GS) {
      const int u16 = lane & 15;
      const bool hon = u16 < GS && base + u16 < ncon;
      const int sl = __builtin_amdgcn_ds_bpermute((base + u16) * 4, cpart);
      const int so = hon ? WS_SLOT0 + (part * NPART + sl) * SLOT_BYTES : OOB;
      const unsigned htag = hon ? tag : 0u;
      const float hbias = hon ? 0.f : -INFINITY;
      int doff[GS];
      unsigned etag[GS];
#pragma unroll
      for (int u = 0; u < GS; ++u) {
        doff[u] = __builtin_amdgcn_readlane(so, u) + lane * 16;
        etag[u] = __builtin_amdgcn_readlane(htag, u);
        asm volatile("" : "+v"(doff[u]), "+s"(etag[u]));
      }
      asm volatile("" : "+v"(orow));
      u32x4 h, d[GS];
      const int eoff = lane == 0 && base + GS >= ncon ? part * 4 : OOB;
      for (;;) {
        asm volatile("" ::: "memory");
        const int av = __builtin_amdgcn_readfirstlane(*(const int*)(smem + CW_ADV));
        if (wide || (int)(now() - (tend + POLL_AT - (av >> 8))) >= 0) break;
        __builtin_amdgcn_s_sleep(2);
      }
#pragma unroll 1
      for (; spin < SPIN_CAP; ++spin) {
        asm volatile("" ::: "memory");
        h = __builtin_amdgcn_raw_buffer_load_b128(rws, so + 1024, 0, AUX_SC1);
        if (!__builtin_amdgcn_ballot_w64(__builtin_amdgcn_bitop3_b32(h[1], h[3], htag, 0x7E) != 0)) break;
      }
#pragma unroll
      for (int u = GS - 1; u >= 0; --u) d[u] = __builtin_amdgcn_raw_buffer_load_b128(rws, doff[u], 0, AUX_SC1);
      __builtin_amdgcn_raw_buffer_store_b32(tag, rws, eoff, 0, AUX_SC1);
      __builtin_amdgcn_sched_barrier(0);
      const float mu = __uint_as_float(h[0]) + hbias;
      float M = max8(mu);
      M = fmx(fmx(M, dppf<XOR15>(M)), Mr);
      float f = __builtin_amdgcn_exp2f(mu - M), fr = __builtin_amdgcn_exp2f(Mr - M);
      const float l8 = sum8(f * __uint_as_float(h[2]));
      Lr = (l8 + dppf<XOR15>(l8)) + fr * Lr;
      Mr = M;
      if (base + GS >= ncon) {
        const float inv = __builtin_amdgcn_rcpf(Lr);
        f *= inv;
        fr *= inv;
      }
      a0 *= fr;
      a1 *= fr;
      float fu[GS], b0 = 0.f, b1 = 0.f;
#pragma unroll
      for (int u = 0; u < GS; ++u) {
        fu[u] = rdlane(f, u);
        asm volatile("" : "+s"(fu[u]));
      }
      asm volatile("s_nop 1" : "+v"(a0), "+v"(a1), "+v"(b0), "+v"(b1));
#pragma unroll 1
      for (; spin < SPIN_CAP; ++spin) {
        unsigned bad = 0;
#pragma unroll
        for (int u = GS - 1; u >= 0; --u) bad |= __builtin_amdgcn_bitop3_b32(d[u][1], d[u][3], etag[u], 0x7E);
        if (!__builtin_amdgcn_ballot_w64(bad != 0)) break;
        asm volatile("" ::: "memory");
#pragma unroll
        for (int u = GS - 1; u >= 0; --u) d[u] = __builtin_amdgcn_raw_buffer_load_b128(rws, doff[u], 0, AUX_SC1);
        __builtin_amdgcn_raw_buffer_store_b32(tag, rws, eoff, 0, AUX_SC1);
      }
#pragma unroll
      for (int u = 0; u < GS; u += 2)
        asm("v_fmac_f32 %0, %4, %6\n\tv_fmac_f32 %1, %4, %7\n\tv_fmac_f32 %2, %5, %8\n\tv_fmac_f32 %3, %5, %9"
            : "+v"(a0), "+v"(a1), "+v"(b0), "+v"(b1)
            : "s"(fu[u]), "s"(fu[u + 1]), "v"(d[u][0]), "v"(d[u][2]), "v"(d[u + 1][0]), "v"(d[u + 1][2]));
      a0 += b0;
      a1 += b1;
    }
    const unsigned pk = cvtpk(a0, a1), pp = dppu<0x128>(pk);
    __builtin_amdgcn_raw_buffer_store_b32(__builtin_amdgcn_perm(pk, pp, oddi ? 0x07060302u : 0x01000504u), rout, orow, 0, AUX_SC1);
    asm volatile(".Lcode_end:");
  }
}

}

extern "C" void kernel_launch(void* const* d_in, const int* in_sizes, int n_in, void* d_out, int out_size, void* d_ws,
                              size_t ws_size, hipStream_t stream) {
  (void)n_in; (void)out_size; (void)ws_size;
  (void)hipFuncSetAttribute((const void*)attn_fused, hipFuncAttributeMaxDynamicSharedMemorySize, LDS_BYTES);
  hipLaunchKernelGGL(attn_fused, dim3(NKVH * NPART), dim3(NTHR), LDS_BYTES, stream, (const char*)d_in[0], (const char*)d_in[1],
                     (const char*)d_in[2], (const int*)d_in[3], (const int*)d_in[4], (char*)d_ws, (__hip_bfloat16*)d_out, in_sizes[1] / (PAGE_BYTES / 2));
}
```

```cpp
#include <hip/hip_runtime.h>
#include <hip/hip_bf16.h>
#include <stdint.h>

namespace {

constexpr int NBATCH = 8, NH = 32, NKVH = 8, HD = 128, PAGE = 16, NBLK = 512, LK = NBLK * PAGE;
constexpr int NPART = 32, NWAVE = 8, NTHR = NWAVE * 64, NCOL = 32;
constexpr int WSH = 17, WFAR = 22;
static_assert(WFAR >= WSH && WSH >= 16, "split points are a min / max of three lines");
constexpr int PACE = 480;
constexpr int LEAD = 170;
constexpr int LEAD2 = 25;
constexpr int ENDN = 6;
constexpr int ADV_LAT = 105;
constexpr int ADV_SLOW = 25;
constexpr int ADV_PULL = 6;
constexpr int ADV_GAIN = 8;
constexpr int ADV_UP = 150, ADV_DOWN = 80;
constexpr int ADV_MAX = 115, ADV_MIN = 60;
constexpr int POLL_AT = -20;
constexpr int PF_N = 2;
constexpr int TOK_BYTES = NKVH * HD * 2;
constexpr int PAGE_BYTES = PAGE * TOK_BYTES;
constexpr int PAGE_SPAN = (PAGE - 1) * TOK_BYTES + HD * 2;
constexpr float SC_LOG2 = 0.08838834764831845f * 1.4426950408889634f;
constexpr int AUX_NT = 2, AUX_SC1 = 16;
constexpr unsigned SPIN_CAP = 1u << 20;
constexpr int OOB = 0x70000000;
constexpr int GS = 12;

typedef __attribute__((ext_vector_type(8))) __bf16 bf16x8;
typedef __attribute__((ext_vector_type(2))) __bf16 bf16x2;
typedef __attribute__((ext_vector_type(4))) float f32x4;
typedef __attribute__((ext_vector_type(2))) float f32x2;
typedef __attribute__((ext_vector_type(4))) unsigned u32x4;
typedef __attribute__((ext_vector_type(2))) unsigned u32x2;
typedef __attribute__((ext_vector_type(8))) int i32x8;
extern "C" __device__ int llvm_writelane(int, int, int) __asm("llvm.amdgcn.writelane.i32");

constexpr int COL_F = NWAVE * HD + 4;
constexpr int LDS_TBL = 0;
constexpr int LDS_PW = LDS_TBL + NBLK * NBATCH * 4;
constexpr int CW_PREFIX = LDS_PW;
constexpr int CW_Q = LDS_PW + 16;
constexpr int CW_ORIGIN = LDS_PW + 20;
constexpr int CW_STATS = LDS_PW + 24;
constexpr int CW_ROWS = LDS_PW + 32;
constexpr int CW_LAT = LDS_PW + 40;
constexpr int CW_ADV = LDS_PW + 48;
constexpr int LDS_EP = LDS_PW + 64;
constexpr int LDS_Q = LDS_EP + 256;
constexpr int LDS_ML = LDS_Q + 8192;
constexpr int LDS_O = LDS_ML + NCOL * 40 * 4;
constexpr int LDS_BYTES = LDS_O + NCOL * COL_F * 4;

constexpr int WS_HEAD = 2 << 20, WS_SLOT0 = 1024, SLOT_BYTES = 1152;
constexpr int WS_BYTES = WS_SLOT0 + NCOL * NPART * SLOT_BYTES;

__device__ __forceinline__ f32x4 mfma32(u32x4 a, u32x4 b, f32x4 c) {
  return __builtin_amdgcn_mfma_f32_16x16x32_bf16(__builtin_bit_cast(bf16x8, a), __builtin_bit_cast(bf16x8, b), c, 0, 0, 0);
}
__device__ __forceinline__ unsigned cvtpk(float lo, float hi) {
  return __builtin_bit_cast(unsigned, __builtin_convertvector(f32x2{lo, hi}, bf16x2));
}
__device__ __forceinline__ float fmx(float a, float b) { return __builtin_elementwise_maximum(a, b); }
__device__ __forceinline__ float swap16_max(float x) {
  auto s = __builtin_amdgcn_permlane16_swap(__float_as_uint(x), __float_as_uint(x), false, false);
  return fmx(__uint_as_float(s[0]), __uint_as_float(s[1]));
}
__device__ __forceinline__ float swap32_max(float x) {
  auto s = __builtin_amdgcn_permlane32_swap(__float_as_uint(x), __float_as_uint(x), false, false);
  return fmx(__uint_as_float(s[0]), __uint_as_float(s[1]));
}
template <int CTRL> __device__ __forceinline__ unsigned dppu(unsigned x) {
  return (unsigned)__builtin_amdgcn_mov_dpp((int)x, CTRL, 0xf, 0xf, true);
}
template <int CTRL> __device__ __forceinline__ float dppf(float x) { return __uint_as_float(dppu<CTRL>(__float_as_uint(x))); }
constexpr int XOR1 = 0xB1, XOR2 = 0x4E, XOR3 = 0x1B, XOR7 = 0x141, XOR15 = 0x140;
__device__ __forceinline__ float max8(float x) {
  x = fmx(x, dppf<XOR7>(x));
  return fmx(fmx(fmx(x, dppf<XOR1>(x)), dppf<XOR2>(x)), dppf<XOR3>(x));
}
__device__ __forceinline__ float sum8(float x) {
  x += dppf<XOR1>(x); x += dppf<XOR2>(x); return x + dppf<XOR7>(x);
}
__device__ __forceinline__ float rdlane(float x, int l) { return __int_as_float(__builtin_amdgcn_readlane(__float_as_int(x), l)); }
__device__ __forceinline__ int clamp_len(int l) { return (l <= 0 || l > LK) ? LK : l; }
__device__ __forceinline__ unsigned now() { return (unsigned)__builtin_amdgcn_s_memrealtime(); }
__device__ __forceinline__ f32x2 wsum8(const f32x2 (&o)[8], float f, int l0) {
  float s[8];
#pragma unroll
  for (int w = 0; w < 8; ++w) s[w] = rdlane(f, l0 + w);
  asm volatile("" : "+s"(s[0]), "+s"(s[1]), "+s"(s[2]), "+s"(s[3]), "+s"(s[4]), "+s"(s[5]), "+s"(s[6]), "+s"(s[7]));
  const f32x2 a = o[0] * s[0] + o[2] * s[2] + o[4] * s[4] + o[6] * s[6];
  const f32x2 b = o[1] * s[1] + o[3] * s[3] + o[5] * s[5] + o[7] * s[7];
  return a + b;
}

#define CND4(D, A, B, P)                                                                        \
  "v_cndmask_b32_dpp %[" D "0], %[" A "0], %[" B "0], vcc " P " row_mask:0xf bank_mask:0xf\n\t" \
  "v_cndmask_b32_dpp %[" D "1], %[" A "1], %[" B "1], vcc " P " row_mask:0xf bank_mask:0xf\n\t" \
  "v_cndmask_b32_dpp %[" D "2], %[" A "2], %[" B "2], vcc " P " row_mask:0xf bank_mask:0xf\n\t" \
  "v_cndmask_b32_dpp %[" D "3], %[" A "3], %[" B "3], vcc " P " row_mask:0xf bank_mask:0xf\n\t"
#define SWAP_STAGE(MASK, P)                                                                                              \
  asm("s_mov_b32 vcc_lo, " MASK "\n\ts_mov_b32 vcc_hi, " MASK "\n\t" CND4("x", "b", "a", P) "s_not_b64 vcc, vcc\n\t" CND4("y", "a", "b", P) \
      "s_nop 1"                                                                                                          \
      : [x0] "=&v"(x[0]), [x1] "=&v"(x[1]), [x2] "=&v"(x[2]), [x3] "=&v"(x[3]), [y0] "=&v"(y[0]), [y1] "=&v"(y[1]),      \
        [y2] "=&v"(y[2]), [y3] "=&v"(y[3])                                                                               \
      : [a0] "v"(a[0]), [a1] "v"(a[1]), [a2] "v"(a[2]), [a3] "v"(a[3]), [b0] "v"(b[0]), [b1] "v"(b[1]), [b2] "v"(b[2]),  \
        [b3] "v"(b[3])                                                                                                   \
      : "vcc", "scc")
__device__ __forceinline__ void swap1(const u32x4& a, const u32x4& b, u32x4& ox, u32x4& oy) {
  unsigned x[4], y[4];
  SWAP_STAGE("0x55555555", "quad_perm:[1,0,3,2]");
  ox = u32x4{x[0], x[1], x[2], x[3]};
  oy = u32x4{y[0], y[1], y[2], y[3]};
}
__device__ __forceinline__ void swap2(const u32x4& a, const u32x4& b, u32x4& ox, u32x4& oy) {
  unsigned x[4], y[4];
  SWAP_STAGE("0x33333333", "quad_perm:[2,3,0,1]");
  ox = u32x4{x[0], x[1], x[2], x[3]};
  oy = u32x4{y[0], y[1], y[2], y[3]};
}
__device__ __forceinline__ void quad_transpose(const u32x4 (&in)[4], u32x4 (&out)[4]) {
  u32x4 t[4];
  swap1(in[0], in[1], t[0], t[1]);
  swap1(in[2], in[3], t[2], t[3]);
  swap2(t[0], t[2], out[0], out[2]);
  swap2(t[1], t[3], out[1], out[3]);
}

__global__ __launch_bounds__(NTHR, 2) void attn_fused(const char* __restrict__ q, const char* __restrict__ kc,
                                                      const char* __restrict__ vc, const int* __restrict__ bt,
                                                      const int* __restrict__ lens, char* __restrict__ ws,
                                                      __hip_bfloat16* __restrict__ out, int nphys) {
  extern __shared__ __attribute__((aligned(16))) char smem[];
  const int tid = threadIdx.x, lane = tid & 63, wave = __builtin_amdgcn_readfirstlane(tid >> 6);
  if (tid < 16) ((unsigned*)(smem + LDS_PW))[tid] = 0u;
  unsigned warm0 = 0;
  if (wave == 0) {
    unsigned long long pc;
    unsigned len;
    asm volatile("s_getpc_b64 %0\n.Lcode_begin:\n\ts_mov_b32 %1, .Lcode_end-.Lcode_begin" : "=s"(pc), "=s"(len));
    const auto rc = __builtin_amdgcn_make_buffer_rsrc((void*)pc, 0, max((int)len, 0), 0x00020000);
    warm0 = __builtin_amdgcn_raw_buffer_load_b32(rc, (lane * 8 + ((blockIdx.x >> 3) & 7)) * 128, 0, 0);
  }
  asm volatile("s_waitcnt lgkmcnt(0)" :: "s"(q), "s"(kc), "s"(vc), "s"(bt), "s"(lens), "s"(ws), "s"(out), "s"(nphys) : "memory");
  const i32x8 slen = *(const __attribute__((address_space(4))) i32x8*)(uintptr_t)lens;
  const int g = lane >> 4, c = lane & 15;
  const int kvh = blockIdx.x & 7, part = blockIdx.x >> 3;

  u32x2 y[NBATCH];
  const bool btw = (unsigned)(wave - 2) < 4u;
  const int bcol = (tid - 128) * 8;
  if (btw) {
    const auto rbt = __builtin_amdgcn_make_buffer_rsrc((void*)bt, 0, NBATCH * NBLK * 4, 0x00020000);
#pragma unroll
    for (int b = 0; b < NBATCH; ++b) y[b] = __builtin_amdgcn_raw_buffer_load_b64(rbt, bcol + (b & 1) * 2048, (b >> 1) * 4096, 0);
  }
  asm volatile("s_barrier" ::: "memory");
  const auto rws = __builtin_amdgcn_make_buffer_rsrc((void*)(ws + (size_t)kvh * WS_HEAD), 0, WS_BYTES, 0x00020000);
  const auto rout = __builtin_amdgcn_make_buffer_rsrc((void*)out, 0, NBATCH * NH * HD * 2, 0x00020000);
  const char* kbase = kc + kvh * (HD * 2);
  const char* vbase = vc + kvh * (HD * 2);
  int koff = ((c >> 2) * 4) * TOK_BYTES + (4 * g + (c & 3)) * 16;
  int voff = 4 * g * TOK_BYTES + c * 16;
  asm volatile("" : "+v"(koff), "+v"(voff), "+s"(kbase), "+s"(vbase));
  if (btw) {
#pragma unroll
    for (int b = 0; b < NBATCH; ++b) *(u32x2*)(smem + LDS_TBL + b * (NBLK * 4) + bcol) = y[b];
    unsigned d0 = y[0][0] ^ y[1][0], d1 = y[0][1] ^ y[1][1];
#pragma unroll
    for (int b = 2; b < NBATCH; ++b) {
      d0 = __builtin_amdgcn_bitop3_b32(d0, y[0][0], y[b][0], 0xF6);
      d1 = __builtin_amdgcn_bitop3_b32(d1, y[0][1], y[b][1], 0xF6);
    }
    unsigned f0, f1;
    asm("s_ff1_i32_b64 %0, %1" : "=s"(f0) : "s"(__builtin_amdgcn_ballot_w64(d0 != 0)));
    asm("s_ff1_i32_b64 %0, %1" : "=s"(f1) : "s"(__builtin_amdgcn_ballot_w64(d1 != 0)));
    asm volatile("" ::: "memory");
    ((unsigned*)(smem + CW_PREFIX))[wave - 2] = (((wave - 2) * 128) | min(2 * f0, 2 * f1 + 1)) + 1;
  }
  if (wave == 6) {
    const auto rq = __builtin_amdgcn_make_buffer_rsrc((void*)(q + 4 * kvh * (HD * 2)), 0, NBATCH * NH * HD * 2 - 4 * kvh * (HD * 2), 0x00020000);
    u32x4 qs[NBATCH];
#pragma unroll
    for (int b = 0; b < NBATCH; ++b) qs[b] = __builtin_amdgcn_raw_buffer_load_b128(rq, lane * 16, b * (NH * HD * 2), 0);
    const unsigned e = __builtin_amdgcn_raw_buffer_load_b32(rws, lane < 32 ? lane * 4 : WS_SLOT0 + (part * NPART + lane - 32) * SLOT_BYTES + 1024, 0, AUX_SC1);
#pragma unroll
    for (int b = 0; b < NBATCH; ++b) *(u32x4*)(smem + LDS_Q + (((b >> 2) * 4 + (c & 3)) * 64 + (c >> 2) * 16 + (b & 3) * 4 + g) * 16) = qs[b];
    *(unsigned*)(smem + LDS_EP + lane * 4) = e;
    asm volatile("" ::: "memory");
    *(unsigned*)(smem + CW_Q) = 1u;
  }
  if (wave == 7) {
    static_assert(PF_N == 2, "32 lanes per page");
    const auto rk = __builtin_amdgcn_make_buffer_rsrc((void*)kc, 0, (unsigned)nphys * PAGE_BYTES, 0x00020000);
    const auto rv = __builtin_amdgcn_make_buffer_rsrc((void*)vc, 0, (unsigned)nphys * PAGE_BYTES, 0x00020000);
    const unsigned idx = part * PF_N + (lane >> 5), even = (idx * 2 + 1) * nphys / (2 * NPART * PF_N);
    const unsigned far0 = 0x80000000u >> __builtin_clz(nphys), nfar = (unsigned)nphys - far0;
    const unsigned page = nfar >= 128u ? far0 + (idx * nfar * 5 >> 9) : even;
    const unsigned off = page * PAGE_BYTES + ((lane >> 1) & 15) * TOK_BYTES + kvh * (HD * 2) + (lane & 1) * 128;
    const unsigned toff = (lane & 31) == 0 ? even * PAGE_BYTES + kvh * (HD * 2) : (unsigned)OOB;
    const unsigned tk = __builtin_amdgcn_raw_buffer_load_b32(rk, toff, 0, 0);
    const unsigned wk = __builtin_amdgcn_raw_buffer_load_b32(rk, off, 0, 0), wv = __builtin_amdgcn_raw_buffer_load_b32(rv, off, 0, 0);
    const unsigned tv = __builtin_amdgcn_raw_buffer_load_b32(rv, toff, 0, 0);
    asm volatile("" :: "v"(wk), "v"(wv), "v"(tk), "v"(tv));
  }
  asm volatile("" :: "v"(warm0));
  __builtin_amdgcn_s_waitcnt(0x0F70);
  int lraw = 1;
#pragma unroll
  for (int b = 0; b < NBATCH; ++b) lraw = llvm_writelane(slen[b], b, lraw);
  const int npg8 = (clamp_len(lraw) + PAGE - 1) >> 4;
  int nmax = max(npg8, (int)dppu<XOR1>(npg8));
  nmax = max(nmax, (int)dppu<XOR2>(nmax));
  nmax = __builtin_amdgcn_readfirstlane(max(nmax, (int)dppu<XOR7>(nmax)));
  int npg = lane < NBATCH ? npg8 : (1 << 20);
  unsigned farpage = 0x80000000u >> __builtin_clz(nphys);
  int lastix = LDS_TBL + ((lane & 7) * NBLK + npg8 - 1) * 4;
  int rest = max(32 - lane, 0), soff0 = lane * NBLK, item0 = 16 * (lane >> 1) + 2 * wave + (lane & 1);
  asm volatile("" : "+v"(npg), "+s"(nmax), "+s"(farpage), "+v"(lastix), "+v"(rest), "+v"(soff0), "+v"(item0));

  unsigned p1, last;
  do {
    asm volatile("" ::: "memory");
    const u32x4 a = *(const u32x4*)(smem + CW_PREFIX);
    asm volatile("" ::: "memory");
    last = *(const unsigned*)(smem + lastix);
    asm volatile("" : "+v"(last));
    p1 = __builtin_amdgcn_readfirstlane(min(min(a[0], a[1]), min(a[2], a[3])));
  } while (!p1);
  asm volatile("" ::: "memory");
  int P;
  asm("s_min_u32 %0, %1, %2" : "=s"(P) : "s"(p1 - 1), "s"(nmax) : "scc");
  const int cnt = max(npg - P, 0);
  int send = cnt;
  send += (int)dppu<0x111>(send);
  send += (int)dppu<0x112>(send);
  send += (int)dppu<0x114>(send);
  const int npriv = __builtin_amdgcn_readlane(send, NBATCH - 1);
  int bnd;
  {
    const int f = __builtin_ctz((unsigned)__builtin_amdgcn_ballot_w64(last >= farpage) | 0x100u);
    const int nfar = f < NBATCH ? npriv - __builtin_amdgcn_readlane(send - cnt, f) : 0;
    const unsigned wt = npriv * 16 + P * WSH + nfar * (WFAR - 16);
    const unsigned x = wt * (unsigned)lane;
    bnd = max((int)__umulhi(x, (unsigned)((1ull << 27) / WSH)), ((int)(x >> 5) - P * (WSH - 16)) >> 4);
    bnd = min(bnd, P + npriv - (int)__umulhi(wt * (unsigned)rest, (unsigned)((1ull << 27) / WFAR)));
  }
  const int i0 = __builtin_amdgcn_readlane(bnd, part), i1 = __builtin_amdgcn_readlane(bnd, part + 1);
  const int nitem = i1 - i0, ntile = (nitem + 1) >> 1, nextra = ntile & 7;
  const int nw = (ntile >> 3) + (wave < nextra ? 1 : 0);
  const int soff = soff0 - (send - cnt);
  const int item = i0 + item0;
  int tix = 0;
  for (int cur = max(i0 - P, 0); cur < i1 - P;) {
    const int s = __popcll(__builtin_amdgcn_ballot_w64(send <= cur));
    const int so = __builtin_amdgcn_readlane(soff, s);
    tix = item - P >= cur ? so : tix;
    cur = __builtin_amdgcn_readlane(send, s);
  }
  tix = (tix + item) & (NBATCH * NBLK - 1);
  const unsigned tstart = now();
  const int phys = *(const int*)(smem + LDS_TBL + tix * 4);
  int early = wave < 2 ? nitem - 2 * wave : 0;
  asm volatile("" : "+s"(early));
  unsigned tpace;
  int dpace, nfast, dcut;
  u32x4 buf[2][4];
  auto issue = [&](const char* base, int off, int k) {
#pragma unroll
    for (int s = 0; s < 2; ++s) {
      const bool on = s == 0 || 16 * k + 2 * wave + 1 < nitem;
      const size_t po = (size_t)(unsigned)__builtin_amdgcn_readlane(phys, on ? 2 * k + s : 2 * k) * PAGE_BYTES;
      const auto rp = __builtin_amdgcn_make_buffer_rsrc((void*)(base + po), 0, on ? PAGE_SPAN : 0, 0x00020000);
#pragma unroll
      for (int i = 0; i < 4; ++i) buf[s][i] = __builtin_amdgcn_raw_buffer_load_b128(rp, off + (i & 1) * TOK_BYTES, (i >> 1) * 2 * TOK_BYTES, AUX_NT);
    }
    __builtin_amdgcn_sched_barrier(0);
  };
  unsigned tiss = 0, tarr = 0;
  const int lat8 = 8 * (ADV_LAT + ((((uintptr_t)kbase >> 8) & 3) == 1 ? ADV_SLOW : 0));
  auto pace = [&](int n, bool steer) {
    int adv = 0;
    if (steer) {
      asm volatile("" ::: "memory");
      *(unsigned char*)(smem + CW_LAT + wave) = (unsigned char)min(tarr - tiss, 200u);
      asm volatile("" ::: "memory");
      const u32x2 lt = *(const u32x2*)(smem + CW_LAT);
      adv = __builtin_amdgcn_readfirstlane(*(const int*)(smem + CW_ADV));
      const int sum = __builtin_amdgcn_readfirstlane(__builtin_amdgcn_sad_u8(lt[0], 0, __builtin_amdgcn_sad_u8(lt[1], 0, 0)));
      const int line = max(n - 6, 0) * ((dpace * ADV_MAX) >> 10);
      const int step = min(max((lat8 - sum + (((line - adv) * ADV_PULL) >> 10)) * ADV_GAIN, -ADV_DOWN * 16), ADV_UP * 16);
      adv = min(max(adv + ((dpace * step) >> 14), -n * ((dpace * ADV_MIN) >> 10)), line);
      *(int*)(smem + CW_ADV) = adv;
    }
    const unsigned tg = tpace + (((n - 1) * dpace - max(n - nfast, 0) * dcut - adv) >> 8);
    while ((int)((tiss = now()) - tg) < 0) __builtin_amdgcn_s_sleep(1);
  };
  if (early > 0) issue(kbase, koff, 0);
  if (wave == 0) *(unsigned*)(smem + CW_ORIGIN) = tstart | 1u;

  unsigned contrib;
  {
    const int s = part >> 2;
    const int e = __builtin_amdgcn_readlane(send, s) + P, n = __builtin_amdgcn_readlane(cnt, s), ps = min(P, __builtin_amdgcn_readlane(npg, s));
    const int nxt = __builtin_amdgcn_ds_bpermute((lane + 1) * 4, bnd);
    contrib = (unsigned)__builtin_amdgcn_ballot_w64(nxt > bnd && (bnd < ps || (n > 0 && bnd < e && nxt > e - n)));
  }
  const unsigned um = nitem > 0 ? (unsigned)__builtin_amdgcn_ballot_w64(i0 < min(P, npg) || (cnt > 0 && i0 - P < send && i1 - P > send - cnt)) & 0xffu : 0u;
  const int nseq = __popc(um);
  const bool wide = nseq > 2;
  {
    const float span = (float)(npriv * 16 + P * WSH) * (PACE * (1.f / 32.f)) - (wide ? LEAD * 256.f : nseq == 2 ? LEAD2 * 256.f : 0.f);
    dpace = __builtin_amdgcn_readfirstlane((int)(fmaxf(span, 0.f) * __builtin_amdgcn_rcpf((float)max(2 * ntile, 1))));
    nfast = 2 * ntile - ENDN;
    dcut = wide ? 0 : __builtin_amdgcn_readfirstlane((int)((float)dpace * (float)(P * WSH) * __builtin_amdgcn_rcpf((float)(npriv * 16 + P * WSH))));
  }
  do {
    asm volatile("" ::: "memory");
    tpace = __builtin_amdgcn_readfirstlane(*(const unsigned*)(smem + CW_ORIGIN));
  } while (!tpace);
  const unsigned tend = tpace + (unsigned)(((npriv * 16 + P * WSH) >> 5) * PACE >> 8);
  if (nw > 0 && wave >= 2) {
    pace(wave, false);
    issue(kbase, koff, 0);
  }

  while (__hip_atomic_load((unsigned*)(smem + CW_Q), __ATOMIC_RELAXED, __HIP_MEMORY_SCOPE_WORKGROUP) == 0u) __builtin_amdgcn_s_sleep(1);
  asm volatile("" ::: "memory");
  const unsigned epoch = *(const unsigned*)(smem + LDS_EP + lane * 4);
  int lenv[2];
  u32x4 qf[2][4];
#pragma unroll
  for (int cg = 0; cg < 2; ++cg) {
    const int l = __builtin_amdgcn_ds_bpermute((4 * cg + (c >> 2)) * 4, lraw);
#pragma unroll
    for (int i = 0; i < 4; ++i) qf[cg][i] = *(const u32x4*)(smem + LDS_Q + ((cg * 4 + i) * 64 + lane) * 16);
    if (l <= 0) {
#pragma unroll
      for (int i = 0; i < 4; ++i) qf[cg][i] = u32x4{0, 0, 0, 0};
    }
    lenv[cg] = clamp_len(l) - 4 * g;
  }
  const unsigned meta = item < i1 ? (unsigned)(tix & (NBLK - 1)) * PAGE | (item < P ? 0xff0000u : 0x10000u << (tix >> 9)) : 0u;
  const int rk = (wave - nextra) & 7;
  int ncol = wide ? (nseq + 1 - (rk >> 2)) >> 1 : (unsigned)(rk - 1) < 4u ? nseq : 0;
  int mrd, lrd, pcol, phdr;
  unsigned ctv;
  {
    const int u = (lane >> 3) & 3;
    const int rank = ((um >> (lane & 7)) & 1) ? __popc(um & ((1u << (lane & 7)) - 1)) : -1;
    const unsigned long long hit = __builtin_amdgcn_ballot_w64(rank == (wide ? 2 * u + (rk >> 2) : u));
    const unsigned mine8 = (unsigned)(hit >> (8 * u)) & 0xffu;
    const bool on = mine8 != 0 && lane < 32;
    const int col = 4 * __builtin_ctz(mine8 | 0x80u) + (rk & 3);
    mrd = LDS_ML + (col * 40 + 32 + (lane & 7)) * 4;
    lrd = LDS_ML + (col * 40 + (lane & 7) * 4) * 4;
    pcol = col;
    const int slot = WS_SLOT0 + (col * NPART + part) * SLOT_BYTES;
    phdr = on ? slot + 1024 + (lane & 7) * 16 : OOB;
    ctv = (unsigned)__builtin_amdgcn_ds_bpermute(col * 4, (int)epoch) + 1;
  }
  int phdr0 = wide || lane >= 8 ? OOB : phdr;
  int ord = LDS_O + (__builtin_amdgcn_readfirstlane(pcol) * COL_F + 2 * lane) * 4;
  int prow = WS_SLOT0 + (__builtin_amdgcn_readfirstlane(pcol) * NPART + part) * SLOT_BYTES + lane * 16;
  unsigned ct = __builtin_amdgcn_readfirstlane(ctv);
  asm volatile("" : "+v"(mrd), "+v"(lrd), "+v"(ord), "+v"(prow), "+v"(ct), "+v"(phdr0), "+v"(phdr), "+v"(ctv), "+v"(pcol), "+s"(ncol));
  int wm = LDS_ML + (c * 40 + 32 + rk) * 4, wl = LDS_ML + (c * 40 + rk * 4 + g) * 4;
  int wrow[2] = {LDS_O + (c * COL_F + rk * HD + 4 * g) * 4, LDS_O + ((16 + c) * COL_F + rk * HD + 4 * g) * 4};
  asm volatile("" : "+v"(wm), "+v"(wl), "+v"(wrow[0]), "+v"(wrow[1]));

  float m[2] = {-INFINITY, -INFINITY}, ms[2] = {0.f, 0.f}, l[2] = {0.f, 0.f};
  f32x4 acc[2][8];
#pragma unroll
  for (int cg = 0; cg < 2; ++cg)
#pragma unroll
    for (int i = 0; i < 8; ++i) acc[cg][i] = f32x4{0.f, 0.f, 0.f, 0.f};
  __builtin_amdgcn_s_setprio(2);

  const int sc0 = LDS_O + ((lane >> 5) * COL_F + rk * HD) * 4 + (lane & 31) * 16;
  u32x4 pb[2];
  auto weights = [&](int k) {
    const unsigned mu = (unsigned)__builtin_amdgcn_readlane((int)meta, 2 * k) | (unsigned)__builtin_amdgcn_readlane((int)meta, 2 * k + 1);
#pragma unroll
    for (int cg = 0; cg < 2; ++cg) {
      if ((mu >> (16 + 4 * cg)) & 0xfu) {
#pragma unroll
        for (int s = 0; s < 2; ++s) {
          f32x4 x = *(const f32x4*)(smem + sc0 + (((k & 3) * 2 + cg) * 4 + 2 * s) * (COL_F * 4));
#pragma unroll
          for (int e = 0; e < 4; ++e) x[e] = __builtin_amdgcn_exp2f(__builtin_fmaf(x[e], SC_LOG2, -ms[cg]));
          l[cg] += (x[0] + x[1]) + (x[2] + x[3]);
          pb[cg][2 * s] = cvtpk(x[0], x[1]);
          pb[cg][2 * s + 1] = cvtpk(x[2], x[3]);
        }
      }
    }
    return mu;
  };
  auto operands = [&](u32x4 (&va)[8], int s) {
#pragma unroll
    for (int i = 0; i < 8; ++i) {
      const unsigned sel = (i & 1) ? 0x07060302u : 0x05040100u;
      va[i][2 * s] = __builtin_amdgcn_perm(buf[s][1][i >> 1], buf[s][0][i >> 1], sel);
      va[i][2 * s + 1] = __builtin_amdgcn_perm(buf[s][3][i >> 1], buf[s][2][i >> 1], sel);
    }
  };
  for (int k0 = 0; k0 < nw; k0 += 4) {
    const int k1 = min(k0 + 4, nw), nround = min(ntile - 8 * k0, 32);
    float mx[2] = {-INFINITY, -INFINITY};
    for (int k = k0; k < k1;) {
      u32x4 kt[2][4];
      quad_transpose(buf[0], kt[0]);
      asm volatile("" :: "v"(kt[0][0]));
      tarr = now();
      quad_transpose(buf[1], kt[1]);
      const unsigned mt[2] = {(unsigned)__builtin_amdgcn_readlane((int)meta, 2 * k), (unsigned)__builtin_amdgcn_readlane((int)meta, 2 * k + 1)};
#pragma unroll
      for (int cg = 0; cg < 2; ++cg) {
        if (((mt[0] | mt[1]) >> (16 + 4 * cg)) & 0xfu) {
#pragma unroll
          for (int s = 0; s < 2; ++s) {
            f32x4 x = {0.f, 0.f, 0.f, 0.f};
#pragma unroll
            for (int i = 0; i < 4; ++i) x = mfma32(kt[s][i], qf[cg][i], x);
            const int lim = ((mt[s] >> (16 + 4 * cg + (c >> 2))) & 1) ? lenv[cg] - (int)(mt[s] & 0xffffu) : 0;
#pragma unroll
            for (int e = 0; e < 4; ++e) x[e] = e < lim ? x[e] : -INFINITY;
            mx[cg] = fmx(fmx(fmx(mx[cg], x[0]), x[1]), fmx(x[2], x[3]));
            *(f32x4*)(smem + sc0 + (((k - k0) * 2 + cg) * 4 + 2 * s) * (COL_F * 4)) = x;
          }
        }
      }
      ++k;
      if (k < k1) {
        pace(16 * k0 + 8 * (k - k0) + wave, true);
        issue(kbase, koff, k);
      }
    }
    pace(16 * k0 + nround + wave, true);
    issue(vbase, voff, k0);
#pragma unroll
    for (int cg = 0; cg < 2; ++cg) {
      m[cg] = fmx(m[cg], swap32_max(swap16_max(mx[cg])));
      const float mn = m[cg] == -INFINITY ? 0.f : m[cg] * SC_LOG2;
      if (k0 > 0) {
        const float alpha = __builtin_amdgcn_exp2f(fminf(ms[cg] - mn, 0.f));
        l[cg] *= alpha;
#pragma unroll
        for (int i = 0; i < 8; ++i) acc[cg][i] *= alpha;
      }
      ms[cg] = mn;
    }
    for (int k = k0; k < min(k1, nw - 1);) {
      const unsigned mu = weights(k);
      ++k;
      u32x4 va[8];
      operands(va, 0);
      asm volatile("" :: "v"(va[7][1]));
      tarr = now();
      operands(va, 1);
#pragma unroll
      for (int cg = 0; cg < 2; ++cg) {
        if ((mu >> (16 + 4 * cg)) & 0xfu) {
#pragma unroll
          for (int i = 0; i < 8; ++i) acc[cg][i] = mfma32(va[i], pb[cg], acc[cg][i]);
        }
      }
      const bool more_v = k < k1;
      pace(more_v ? 16 * k0 + nround + 8 * (k - k0) + wave : 16 * k1 + wave, true);
      issue(more_v ? vbase : kbase, more_v ? voff : koff, k);
    }
  }

  {
    const unsigned mu = nw > 0 ? weights(nw - 1) : 0u;
#pragma unroll
    for (int cg = 0; cg < 2; ++cg) {
      *(float*)(smem + wm + cg * (16 * 160)) = m[cg] * SC_LOG2;
      *(float*)(smem + wl + cg * (16 * 160)) = l[cg];
      if (((um >> (4 * cg)) & 0xfu) && !((mu >> (16 + 4 * cg)) & 0xfu)) {
#pragma unroll
        for (int i = 0; i < 8; ++i) *(f32x4*)(smem + wrow[cg] + 64 * i) = acc[cg][i];
      }
    }
    u32x4 va[8];
    operands(va, 0);
    __builtin_amdgcn_sched_barrier(0);
    __builtin_amdgcn_s_waitcnt(0x0F70);
    asm volatile("" ::: "memory");
    *(unsigned char*)(smem + CW_STATS + rk) = 1;
#pragma unroll
    for (int cg = 0; cg < 2; ++cg) {
      if ((mu >> (16 + 4 * cg)) & 0xfu) {
        unsigned lo = 0x05040100u, hi = 0x07060302u;
        asm volatile("" : "+s"(lo), "+s"(hi));
#pragma unroll
        for (int i = 0; i < 8 + 2; ++i) {
          if (i < 8) {
            va[i][2] = __builtin_amdgcn_perm(buf[1][1][i >> 1], buf[1][0][i >> 1], (i & 1) ? hi : lo);
            va[i][3] = __builtin_amdgcn_perm(buf[1][3][i >> 1], buf[1][2][i >> 1], (i & 1) ? hi : lo);
          }
          if (i >= 2) *(f32x4*)(smem + wrow[cg] + 64 * (i - 2)) = acc[cg][i - 2];
          if (i < 8) acc[cg][i] = mfma32(va[i], pb[cg], acc[cg][i]);
          __builtin_amdgcn_sched_barrier(0);
        }
      }
    }
  }
  asm volatile("" ::: "memory");
  *(unsigned char*)(smem + CW_ROWS + rk) = 1;
  __builtin_amdgcn_s_setprio(0);

  if (ncol > 0) {
    for (;;) {
      asm volatile("" ::: "memory");
      const u32x2 dn = *(const u32x2*)(smem + CW_ROWS);
      if (__builtin_amdgcn_ballot_w64((dn[0] & (dn[1] | 0x01010100u)) == 0x01010101u)) break;
    }
    asm volatile("" ::: "memory");
    f32x2 o[NWAVE];
#pragma unroll
    for (int w = 0; w < 5; ++w) o[w] = *(const f32x2*)(smem + ord + w * (HD * 4));
    float mw;
    f32x4 lw;
    for (;;) {
      asm volatile("" ::: "memory");
      const unsigned dn = *(const unsigned*)(smem + CW_STATS + 4);
      asm volatile("" ::: "memory");
      mw = *(const float*)(smem + mrd);
      lw = *(const f32x4*)(smem + lrd);
      asm volatile("" : "+v"(mw), "+v"(lw));
      if (__builtin_amdgcn_ballot_w64(dn == 0x01010101u)) break;
    }
    const float M = max8(mw);
    const float f = __builtin_amdgcn_exp2f(mw - M);
    const float L = sum8(f * ((lw[0] + lw[1]) + (lw[2] + lw[3])));
    const u32x4 hdr = {__float_as_uint(M), ctv, __float_as_uint(L), ctv};
    __builtin_amdgcn_raw_buffer_store_b128(hdr, rws, phdr0, 0, AUX_SC1);
    float fw[8];
#pragma unroll
    for (int w = 0; w < 8; ++w) fw[w] = rdlane(f, w);
    asm volatile("" : "+s"(fw[0]), "+s"(fw[1]), "+s"(fw[2]), "+s"(fw[3]), "+s"(fw[4]), "+s"(fw[5]), "+s"(fw[6]), "+s"(fw[7]));
    f32x2 a = (o[0] * fw[0] + o[2] * fw[2] + o[4] * fw[4]) + (o[1] * fw[1] + o[3] * fw[3]);
    asm volatile("" : "+v"(a));
    for (;;) {
      asm volatile("" ::: "memory");
      const unsigned dn = *(const unsigned*)(smem + CW_ROWS + 4);
      asm volatile("" ::: "memory");
#pragma unroll
      for (int w = 5; w < NWAVE; ++w) o[w] = *(const f32x2*)(smem + ord + w * (HD * 4));
      asm volatile("" : "+v"(o[5]), "+v"(o[6]), "+v"(o[7]));
      if (__builtin_amdgcn_ballot_w64(dn == 0x01010101u)) break;
    }
    a = (a + o[5] * fw[5]) + (o[6] * fw[6] + o[7] * fw[7]);
    __builtin_amdgcn_raw_buffer_store_b128(u32x4{__float_as_uint(a[0]), ct, __float_as_uint(a[1]), ct}, rws, prow, 0, AUX_SC1);
    const int phdr1 = !wide && lane < 8 ? OOB : phdr;
#pragma unroll 1
    for (int u = 1; u < ncol; ++u) {
      const int col = __builtin_amdgcn_readlane(pcol, 8 * u);
#pragma unroll
      for (int w = 0; w < NWAVE; ++w) o[w] = *(const f32x2*)(smem + LDS_O + (col * COL_F + 2 * lane) * 4 + w * (HD * 4));
      if (u == ncol - 1) __builtin_amdgcn_raw_buffer_store_b128(hdr, rws, phdr1, 0, AUX_SC1);
      ct = __builtin_amdgcn_readlane(ctv, 8 * u);
      a = wsum8(o, f, 8 * u);
      __builtin_amdgcn_raw_buffer_store_b128(u32x4{__float_as_uint(a[0]), ct, __float_as_uint(a[1]), ct}, rws,
                                             WS_SLOT0 + (col * NPART + part) * SLOT_BYTES + lane * 16, 0, AUX_SC1);
    }
    if (wide && ncol == 1) __builtin_amdgcn_raw_buffer_store_b128(hdr, rws, phdr1, 0, AUX_SC1);
  }

  if (rk == 0) {
    const unsigned tag = (unsigned)__builtin_amdgcn_readlane((int)epoch, part) + 1;
    const bool oddi = (lane >> 3) & 1;
    int orow = (((part >> 2) * NH + 4 * kvh + (part & 3)) * HD + ((lane >> 1) & 3) * 32 + (lane & 1) * 16 + (lane >> 4) * 2 + (oddi ? 8 : 0)) * 2;
    unsigned osel = oddi ? 0x07060302u : 0x01000504u;
    float Mr = -3.0e38f, Lr = 0.f, a0 = 0.f, a1 = 0.f;
    unsigned spin = 0;
    const int ncon = __popc(contrib);
    const int crank = lane < 32 && ((contrib >> lane) & 1) ? __popc(contrib & ((1u << lane) - 1)) : 63;
    const int cpart = __builtin_amdgcn_ds_permute(crank * 4, lane);
    for (int base = 0; base < ncon; base += GS) {
      const int u16 = lane & 15;
      const bool hon = u16 < GS && base + u16 < ncon;
      const int sl = __builtin_amdgcn_ds_bpermute((base + u16) * 4, cpart);
      const int so = hon ? WS_SLOT0 + (part * NPART + sl) * SLOT_BYTES : OOB;
      const unsigned htag = hon ? tag : 0u;
      const float hbias = hon ? 0.f : -INFINITY;
      int doff[GS];
      unsigned etag[GS];
#pragma unroll
      for (int u = 0; u < GS; ++u) {
        doff[u] = __builtin_amdgcn_readlane(so, u) + lane * 16;
        etag[u] = __builtin_amdgcn_readlane(htag, u);
        asm volatile("" : "+v"(doff[u]), "+s"(etag[u]));
      }
      asm volatile("" : "+v"(orow), "+v"(osel));
      u32x4 h, d[GS];
      const int eoff = lane == 0 && base + GS >= ncon ? part * 4 : OOB;
      for (;;) {
        asm volatile("" ::: "memory");
        const int av = __builtin_amdgcn_readfirstlane(*(const int*)(smem + CW_ADV));
        if (wide || (int)(now() - (tend + POLL_AT - (av >> 8))) >= 0) break;
        __builtin_amdgcn_s_sleep(2);
      }
#pragma unroll 1
      for (; spin < SPIN_CAP; ++spin) {
        asm volatile("" ::: "memory");
        h = __builtin_amdgcn_raw_buffer_load_b128(rws, so + 1024, 0, AUX_SC1);
        if (!__builtin_amdgcn_ballot_w64(__builtin_amdgcn_bitop3_b32(h[1], h[3], htag, 0x7E) != 0)) break;
      }
#pragma unroll
      for (int u = 0; u < GS; ++u) d[u] = __builtin_amdgcn_raw_buffer_load_b128(rws, doff[u], 0, AUX_SC1);
      __builtin_amdgcn_raw_buffer_store_b32(tag, rws, eoff, 0, AUX_SC1);
      __builtin_amdgcn_sched_barrier(0);
      const float mu = __uint_as_float(h[0]) + hbias;
      float M = max8(mu);
      M = fmx(fmx(M, dppf<XOR15>(M)), Mr);
      float f = __builtin_amdgcn_exp2f(mu - M), fr = __builtin_amdgcn_exp2f(Mr - M);
      const float l8 = sum8(f * __uint_as_float(h[2]));
      Lr = (l8 + dppf<XOR15>(l8)) + fr * Lr;
      Mr = M;
      if (base + GS >= ncon) {
        const float inv = __builtin_amdgcn_rcpf(Lr);
        f *= inv;
        fr *= inv;
      }
      a0 *= fr;
      a1 *= fr;
      float fu[GS];
#pragma unroll
      for (int u = 0; u < GS; ++u) {
        fu[u] = rdlane(f, u);
        asm volatile("" : "+s"(fu[u]));
      }
      asm volatile("s_nop 1" : "+v"(a0), "+v"(a1));
      float t0, t1;
#pragma unroll 1
      for (;; ++spin) {
        unsigned bad = 0;
        t0 = a0;
        t1 = a1;
#pragma unroll
        for (int u = 0; u < GS; ++u) {
          bad |= __builtin_amdgcn_bitop3_b32(d[u][1], d[u][3], etag[u], 0x7E);
          asm volatile("v_fmac_f32 %0, %2, %3\n\tv_fmac_f32 %1, %2, %4" : "+v"(t0), "+v"(t1) : "s"(fu[u]), "v"(d[u][0]), "v"(d[u][2]));
          __builtin_amdgcn_sched_barrier(0);
        }
        if (!__builtin_amdgcn_ballot_w64(bad != 0) || spin >= SPIN_CAP) break;
        asm volatile("" ::: "memory");
#pragma unroll
        for (int u = 0; u < GS; ++u) d[u] = __builtin_amdgcn_raw_buffer_load_b128(rws, doff[u], 0, AUX_SC1);
        __builtin_amdgcn_raw_buffer_store_b32(tag, rws, eoff, 0, AUX_SC1);
      }
      a0 = t0;
      a1 = t1;
    }
    const unsigned pk = cvtpk(a0, a1), pp = dppu<0x128>(pk);
    __builtin_amdgcn_raw_buffer_store_b32(__builtin_amdgcn_perm(pk, pp, osel), rout, orow, 0, AUX_SC1);
    asm volatile(".Lcode_end:");
  }
}

}

extern "C" void kernel_launch(void* const* d_in, const int* in_sizes, int n_in, void* d_out, int out_size, void* d_ws,
                              size_t ws_size, hipStream_t stream) {
  (void)n_in; (void)out_size; (void)ws_size;
  (void)hipFuncSetAttribute((const void*)attn_fused, hipFuncAttributeMaxDynamicSharedMemorySize, LDS_BYTES);
  hipLaunchKernelGGL(attn_fused, dim3(NKVH * NPART), dim3(NTHR), LDS_BYTES, stream, (const char*)d_in[0], (const char*)d_in[1],
                     (const char*)d_in[2], (const int*)d_in[3], (const int*)d_in[4], (char*)d_ws, (__hip_bfloat16*)d_out, in_sizes[1] / (PAGE_BYTES / 2));
}
```

```cpp
#include <hip/hip_runtime.h>
#include <hip/hip_bf16.h>
#include <stdint.h>

namespace {

constexpr int NBATCH = 8, NH = 32, NKVH = 8, HD = 128, PAGE = 16, NBLK = 512, LK = NBLK * PAGE;
constexpr int NPART = 32, NWAVE = 8, NTHR = NWAVE * 64, NCOL = 32;
constexpr int WSH = 17, WFAR = 22;
static_assert(WFAR >= WSH && WSH >= 16, "split points are a min / max of three lines");
constexpr int PACE = 480;
constexpr int LEAD = 170;
constexpr int LEAD2 = 25;
constexpr int ENDN = 6;
constexpr int ADV_LAT = 105;
constexpr int ADV_SLOW = 25;
constexpr int ADV_PULL = 6;
constexpr int ADV_GAIN = 8;
constexpr int ADV_UP = 150, ADV_DOWN = 80;
constexpr int ADV_MAX = 115, ADV_MIN = 60;
constexpr int POLL_AT = -20;
constexpr int PF_N = 2;
constexpr int TOK_BYTES = NKVH * HD * 2;
constexpr int PAGE_BYTES = PAGE * TOK_BYTES;
constexpr int PAGE_SPAN = (PAGE - 1) * TOK_BYTES + HD * 2;
constexpr float SC_LOG2 = 0.08838834764831845f * 1.4426950408889634f;
constexpr int AUX_NT = 2, AUX_SC1 = 16;
constexpr unsigned SPIN_CAP = 1u << 20;
constexpr int OOB = 0x70000000;
constexpr int GS = 12;

typedef __attribute__((ext_vector_type(8))) __bf16 bf16x8;
typedef __attribute__((ext_vector_type(2))) __bf16 bf16x2;
typedef __attribute__((ext_vector_type(4))) float f32x4;
typedef __attribute__((ext_vector_type(2))) float f32x2;
typedef __attribute__((ext_vector_type(4))) unsigned u32x4;
typedef __attribute__((ext_vector_type(2))) unsigned u32x2;
typedef __attribute__((ext_vector_type(8))) int i32x8;
extern "C" __device__ int llvm_writelane(int, int, int) __asm("llvm.amdgcn.writelane.i32");

constexpr int COL_F = NWAVE * HD + 4;
constexpr int LDS_TBL = 0;
constexpr int LDS_PW = LDS_TBL + NBLK * NBATCH * 4;
constexpr int CW_PREFIX = LDS_PW;
constexpr int CW_Q = LDS_PW + 16;
constexpr int CW_ORIGIN = LDS_PW + 20;
constexpr int CW_STATS = LDS_PW + 24;
constexpr int CW_ROWS = LDS_PW + 32;
constexpr int CW_LAT = LDS_PW + 40;
constexpr int CW_ADV = LDS_PW + 48;
constexpr int LDS_EP = LDS_PW + 64;
constexpr int LDS_Q = LDS_EP + 256;
constexpr int LDS_ML = LDS_Q + 8192;
constexpr int LDS_O = LDS_ML + NCOL * 40 * 4;
constexpr int LDS_BYTES = LDS_O + NCOL * COL_F * 4;

constexpr int WS_HEAD = 2 << 20, WS_SLOT0 = 1024, SLOT_BYTES = 1152;
constexpr int WS_BYTES = WS_SLOT0 + NCOL * NPART * SLOT_BYTES;

__device__ __forceinline__ f32x4 mfma32(u32x4 a, u32x4 b, f32x4 c) {
  return __builtin_amdgcn_mfma_f32_16x16x32_bf16(__builtin_bit_cast(bf16x8, a), __builtin_bit_cast(bf16x8, b), c, 0, 0, 0);
}
__device__ __forceinline__ unsigned cvtpk(float lo, float hi) {
  return __builtin_bit_cast(unsigned, __builtin_convertvector(f32x2{lo, hi}, bf16x2));
}
__device__ __forceinline__ float fmx(float a, float b) { return __builtin_elementwise_maximum(a, b); }
__device__ __forceinline__ float swap16_max(float x) {
  auto s = __builtin_amdgcn_permlane16_swap(__float_as_uint(x), __float_as_uint(x), false, false);
  return fmx(__uint_as_float(s[0]), __uint_as_float(s[1]));
}
__device__ __forceinline__ float swap32_max(float x) {
  auto s = __builtin_amdgcn_permlane32_swap(__float_as_uint(x), __float_as_uint(x), false, false);
  return fmx(__uint_as_float(s[0]), __uint_as_float(s[1]));
}
template <int CTRL> __device__ __forceinline__ unsigned dppu(unsigned x) {
  return (unsigned)__builtin_amdgcn_mov_dpp((int)x, CTRL, 0xf, 0xf, true);
}
template <int CTRL> __device__ __forceinline__ float dppf(float x) { return __uint_as_float(dppu<CTRL>(__float_as_uint(x))); }
constexpr int XOR1 = 0xB1, XOR2 = 0x4E, XOR3 = 0x1B, XOR7 = 0x141, XOR15 = 0x140;
__device__ __forceinline__ float max8(float x) {
  x = fmx(x, dppf<XOR7>(x));
  return fmx(fmx(fmx(x, dppf<XOR1>(x)), dppf<XOR2>(x)), dppf<XOR3>(x));
}
__device__ __forceinline__ float sum8(float x) {
  x += dppf<XOR1>(x); x += dppf<XOR2>(x); return x + dppf<XOR7>(x);
}
__device__ __forceinline__ float rdlane(float x, int l) { return __int_as_float(__builtin_amdgcn_readlane(__float_as_int(x), l)); }
__device__ __forceinline__ int clamp_len(int l) { return (l <= 0 || l > LK) ? LK : l; }
__device__ __forceinline__ unsigned now() { return (unsigned)__builtin_amdgcn_s_memrealtime(); }
__device__ __forceinline__ f32x2 wsum8(const f32x2 (&o)[8], float f, int l0) {
  float s[8];
#pragma unroll
  for (int w = 0; w < 8; ++w) s[w] = rdlane(f, l0 + w);
  asm volatile("" : "+s"(s[0]), "+s"(s[1]), "+s"(s[2]), "+s"(s[3]), "+s"(s[4]), "+s"(s[5]), "+s"(s[6]), "+s"(s[7]));
  const f32x2 a = o[0] * s[0] + o[2] * s[2] + o[4] * s[4] + o[6] * s[6];
  const f32x2 b = o[1] * s[1] + o[3] * s[3] + o[5] * s[5] + o[7] * s[7];
  return a + b;
}

#define CND4(D, A, B, P)                                                                        \
  "v_cndmask_b32_dpp %[" D "0], %[" A "0], %[" B "0], vcc " P " row_mask:0xf bank_mask:0xf\n\t" \
  "v_cndmask_b32_dpp %[" D "1], %[" A "1], %[" B "1], vcc " P " row_mask:0xf bank_mask:0xf\n\t" \
  "v_cndmask_b32_dpp %[" D "2], %[" A "2], %[" B "2], vcc " P " row_mask:0xf bank_mask:0xf\n\t" \
  "v_cndmask_b32_dpp %[" D "3], %[" A "3], %[" B "3], vcc " P " row_mask:0xf bank_mask:0xf\n\t"
#define SWAP_STAGE(MASK, P)                                                                                              \
  asm("s_mov_b32 vcc_lo, " MASK "\n\ts_mov_b32 vcc_hi, " MASK "\n\t" CND4("x", "b", "a", P) "s_not_b64 vcc, vcc\n\t" CND4("y", "a", "b", P) \
      "s_nop 1"                                                                                                          \
      : [x0] "=&v"(x[0]), [x1] "=&v"(x[1]), [x2] "=&v"(x[2]), [x3] "=&v"(x[3]), [y0] "=&v"(y[0]), [y1] "=&v"(y[1]),      \
        [y2] "=&v"(y[2]), [y3] "=&v"(y[3])                                                                               \
      : [a0] "v"(a[0]), [a1] "v"(a[1]), [a2] "v"(a[2]), [a3] "v"(a[3]), [b0] "v"(b[0]), [b1] "v"(b[1]), [b2] "v"(b[2]),  \
        [b3] "v"(b[3])                                                                                                   \
      : "vcc", "scc")
__device__ __forceinline__ void swap1(const u32x4& a, const u32x4& b, u32x4& ox, u32x4& oy) {
  unsigned x[4], y[4];
  SWAP_STAGE("0x55555555", "quad_perm:[1,0,3,2]");
  ox = u32x4{x[0], x[1], x[2], x[3]};
  oy = u32x4{y[0], y[1], y[2], y[3]};
}
__device__ __forceinline__ void swap2(const u32x4& a, const u32x4& b, u32x4& ox, u32x4& oy) {
  unsigned x[4], y[4];
  SWAP_STAGE("0x33333333", "quad_perm:[2,3,0,1]");
  ox = u32x4{x[0], x[1], x[2], x[3]};
  oy = u32x4{y[0], y[1], y[2], y[3]};
}
__device__ __forceinline__ void quad_transpose(const u32x4 (&in)[4], u32x4 (&out)[4]) {
  u32x4 t[4];
  swap1(in[0], in[1], t[0], t[1]);
  swap1(in[2], in[3], t[2], t[3]);
  swap2(t[0], t[2], out[0], out[2]);
  swap2(t[1], t[3], out[1], out[3]);
}

__global__ __launch_bounds__(NTHR, 2) void attn_fused(const char* __restrict__ q, const char* __restrict__ kc,
                                                      const char* __restrict__ vc, const int* __restrict__ bt,
                                                      const int* __restrict__ lens, char* __restrict__ ws,
                                                      __hip_bfloat16* __restrict__ out, int nphys) {
  extern __shared__ __attribute__((aligned(16))) char smem[];
  const int tid = threadIdx.x, lane = tid & 63, wave = __builtin_amdgcn_readfirstlane(tid >> 6);
  if (tid < 16) ((unsigned*)(smem + LDS_PW))[tid] = 0u;
  unsigned warm0 = 0;
  if (wave == 0) {
    unsigned long long pc;
    unsigned len;
    asm volatile("s_getpc_b64 %0\n.Lcode_begin:\n\ts_mov_b32 %1, .Lcode_end-.Lcode_begin" : "=s"(pc), "=s"(len));
    const auto rc = __builtin_amdgcn_make_buffer_rsrc((void*)pc, 0, max((int)len, 0), 0x00020000);
    warm0 = __builtin_amdgcn_raw_buffer_load_b32(rc, (lane * 8 + ((blockIdx.x >> 3) & 7)) * 128, 0, 0);
  }
  asm volatile("s_waitcnt lgkmcnt(0)" :: "s"(q), "s"(kc), "s"(vc), "s"(bt), "s"(lens), "s"(ws), "s"(out), "s"(nphys) : "memory");
  const i32x8 slen = *(const __attribute__((address_space(4))) i32x8*)(uintptr_t)lens;
  const int g = lane >> 4, c = lane & 15;
  const int kvh = blockIdx.x & 7, part = blockIdx.x >> 3;

  u32x2 y[NBATCH];
  const bool btw = (unsigned)(wave - 2) < 4u;
  const int bcol = (tid - 128) * 8;
  if (btw) {
    const auto rbt = __builtin_amdgcn_make_buffer_rsrc((void*)bt, 0, NBATCH * NBLK * 4, 0x00020000);
#pragma unroll
    for (int b = 0; b < NBATCH; ++b) y[b] = __builtin_amdgcn_raw_buffer_load_b64(rbt, bcol + (b & 1) * 2048, (b >> 1) * 4096, 0);
  }
  static_assert(PF_N == 2, "32 lanes per page");
  unsigned poff = 0, toff = 0;
  if (wave == 7) {
    const unsigned idx = part * PF_N + (lane >> 5), even = (idx * 2 + 1) * nphys / (2 * NPART * PF_N);
    const unsigned far0 = 0x80000000u >> __builtin_clz(nphys), nfar = (unsigned)nphys - far0;
    const unsigned page = nfar >= 128u ? far0 + (idx * nfar * 5 >> 9) : even;
    poff = page * PAGE_BYTES + ((lane >> 1) & 15) * TOK_BYTES + kvh * (HD * 2) + (lane & 1) * 128;
    toff = (lane & 31) == 0 ? even * PAGE_BYTES + kvh * (HD * 2) : (unsigned)OOB;
    asm volatile("" : "+v"(poff), "+v"(toff));
  }
  asm volatile("s_barrier" ::: "memory");
  if (wave == 7) {
    const auto rk = __builtin_amdgcn_make_buffer_rsrc((void*)kc, 0, (unsigned)nphys * PAGE_BYTES, 0x00020000);
    const auto rv = __builtin_amdgcn_make_buffer_rsrc((void*)vc, 0, (unsigned)nphys * PAGE_BYTES, 0x00020000);
    const unsigned wk = __builtin_amdgcn_raw_buffer_load_b32(rk, poff, 0, 0), wv = __builtin_amdgcn_raw_buffer_load_b32(rv, poff, 0, 0);
    __builtin_amdgcn_sched_barrier(0);
    const unsigned tk = __builtin_amdgcn_raw_buffer_load_b32(rk, toff, 0, 0);
    const unsigned tv = __builtin_amdgcn_raw_buffer_load_b32(rv, toff, 0, 0);
    asm volatile("" :: "v"(wk), "v"(wv), "v"(tk), "v"(tv));
  }
  const auto rws = __builtin_amdgcn_make_buffer_rsrc((void*)(ws + (size_t)kvh * WS_HEAD), 0, WS_BYTES, 0x00020000);
  const auto rout = __builtin_amdgcn_make_buffer_rsrc((void*)out, 0, NBATCH * NH * HD * 2, 0x00020000);
  const char* kbase = kc + kvh * (HD * 2);
  const char* vbase = vc + kvh * (HD * 2);
  int koff = ((c >> 2) * 4) * TOK_BYTES + (4 * g + (c & 3)) * 16;
  int voff = 4 * g * TOK_BYTES + c * 16;
  asm volatile("" : "+v"(koff), "+v"(voff), "+s"(kbase), "+s"(vbase));
  if (btw) {
#pragma unroll
    for (int b = 0; b < NBATCH; ++b) *(u32x2*)(smem + LDS_TBL + b * (NBLK * 4) + bcol) = y[b];
    unsigned d0 = y[0][0] ^ y[1][0], d1 = y[0][1] ^ y[1][1];
#pragma unroll
    for (int b = 2; b < NBATCH; ++b) {
      d0 = __builtin_amdgcn_bitop3_b32(d0, y[0][0], y[b][0], 0xF6);
      d1 = __builtin_amdgcn_bitop3_b32(d1, y[0][1], y[b][1], 0xF6);
    }
    unsigned f0, f1;
    asm("s_ff1_i32_b64 %0, %1" : "=s"(f0) : "s"(__builtin_amdgcn_ballot_w64(d0 != 0)));
    asm("s_ff1_i32_b64 %0, %1" : "=s"(f1) : "s"(__builtin_amdgcn_ballot_w64(d1 != 0)));
    asm volatile("" ::: "memory");
    ((unsigned*)(smem + CW_PREFIX))[wave - 2] = (((wave - 2) * 128) | min(2 * f0, 2 * f1 + 1)) + 1;
  }
  if (wave == 6) {
    const auto rq = __builtin_amdgcn_make_buffer_rsrc((void*)(q + 4 * kvh * (HD * 2)), 0, NBATCH * NH * HD * 2 - 4 * kvh * (HD * 2), 0x00020000);
    u32x4 qs[NBATCH];
#pragma unroll
    for (int b = 0; b < NBATCH; ++b) qs[b] = __builtin_amdgcn_raw_buffer_load_b128(rq, lane * 16, b * (NH * HD * 2), 0);
    const unsigned e = __builtin_amdgcn_raw_buffer_load_b32(rws, lane < 32 ? lane * 4 : WS_SLOT0 + (part * NPART + lane - 32) * SLOT_BYTES + 1024, 0, AUX_SC1);
#pragma unroll
    for (int b = 0; b < NBATCH; ++b) *(u32x4*)(smem + LDS_Q + (((b >> 2) * 4 + (c & 3)) * 64 + (c >> 2) * 16 + (b & 3) * 4 + g) * 16) = qs[b];
    *(unsigned*)(smem + LDS_EP + lane * 4) = e;
    asm volatile("" ::: "memory");
    *(unsigned*)(smem + CW_Q) = 1u;
  }
  asm volatile("" :: "v"(warm0));
  __builtin_amdgcn_s_waitcnt(0x0F70);
  int lraw = 1;
#pragma unroll
  for (int b = 0; b < NBATCH; ++b) lraw = llvm_writelane(slen[b], b, lraw);
  const int npg8 = (clamp_len(lraw) + PAGE - 1) >> 4;
  int nmax = max(npg8, (int)dppu<XOR1>(npg8));
  nmax = max(nmax, (int)dppu<XOR2>(nmax));
  nmax = __builtin_amdgcn_readfirstlane(max(nmax, (int)dppu<XOR7>(nmax)));
  int npg = lane < NBATCH ? npg8 : (1 << 20);
  unsigned farpage = 0x80000000u >> __builtin_clz(nphys);
  int lastix = LDS_TBL + ((lane & 7) * NBLK + npg8 - 1) * 4;
  int rest = max(32 - lane, 0), soff0 = lane * NBLK, item0 = 16 * (lane >> 1) + 2 * wave + (lane & 1);
  asm volatile("" : "+v"(npg), "+s"(nmax), "+s"(farpage), "+v"(lastix), "+v"(rest), "+v"(soff0), "+v"(item0));

  unsigned p1, last;
  do {
    asm volatile("" ::: "memory");
    const u32x4 a = *(const u32x4*)(smem + CW_PREFIX);
    asm volatile("" ::: "memory");
    last = *(const unsigned*)(smem + lastix);
    asm volatile("" : "+v"(last));
    p1 = __builtin_amdgcn_readfirstlane(min(min(a[0], a[1]), min(a[2], a[3])));
  } while (!p1);
  asm volatile("" ::: "memory");
  int P;
  asm("s_min_u32 %0, %1, %2" : "=s"(P) : "s"(p1 - 1), "s"(nmax) : "scc");
  const int cnt = max(npg - P, 0);
  int send = cnt;
  send += (int)dppu<0x111>(send);
  send += (int)dppu<0x112>(send);
  send += (int)dppu<0x114>(send);
  const int npriv = __builtin_amdgcn_readlane(send, NBATCH - 1);
  int bnd;
  {
    const int f = __builtin_ctz((unsigned)__builtin_amdgcn_ballot_w64(last >= farpage) | 0x100u);
    const int nfar = f < NBATCH ? npriv - __builtin_amdgcn_readlane(send - cnt, f) : 0;
    const unsigned wt = npriv * 16 + P * WSH + nfar * (WFAR - 16);
    const unsigned x = wt * (unsigned)lane;
    bnd = max((int)__umulhi(x, (unsigned)((1ull << 27) / WSH)), ((int)(x >> 5) - P * (WSH - 16)) >> 4);
    bnd = min(bnd, P + npriv - (int)__umulhi(wt * (unsigned)rest, (unsigned)((1ull << 27) / WFAR)));
  }
  const int i0 = __builtin_amdgcn_readlane(bnd, part), i1 = __builtin_amdgcn_readlane(bnd, part + 1);
  const int nitem = i1 - i0, ntile = (nitem + 1) >> 1, nextra = ntile & 7;
  const int nw = (ntile >> 3) + (wave < nextra ? 1 : 0);
  const int soff = soff0 - (send - cnt);
  const int item = i0 + item0;
  int tix = 0;
  for (int cur = max(i0 - P, 0); cur < i1 - P;) {
    const int s = __popcll(__builtin_amdgcn_ballot_w64(send <= cur));
    const int so = __builtin_amdgcn_readlane(soff, s);
    tix = item - P >= cur ? so : tix;
    cur = __builtin_amdgcn_readlane(send, s);
  }
  tix = (tix + item) & (NBATCH * NBLK - 1);
  const unsigned tstart = now();
  const int phys = *(const int*)(smem + LDS_TBL + tix * 4);
  int early = wave < 2 ? nitem - 2 * wave : 0;
  asm volatile("" : "+s"(early));
  unsigned tpace;
  int dpace, nfast, dcut;
  u32x4 buf[2][4];
  auto issue = [&](const char* base, int off, int k) {
#pragma unroll
    for (int s = 0; s < 2; ++s) {
      const bool on = s == 0 || 16 * k + 2 * wave + 1 < nitem;
      const size_t po = (size_t)(unsigned)__builtin_amdgcn_readlane(phys, on ? 2 * k + s : 2 * k) * PAGE_BYTES;
      const auto rp = __builtin_amdgcn_make_buffer_rsrc((void*)(base + po), 0, on ? PAGE_SPAN : 0, 0x00020000);
#pragma unroll
      for (int i = 0; i < 4; ++i) buf[s][i] = __builtin_amdgcn_raw_buffer_load_b128(rp, off + (i & 1) * TOK_BYTES, (i >> 1) * 2 * TOK_BYTES, AUX_NT);
    }
    __builtin_amdgcn_sched_barrier(0);
  };
  unsigned tiss = 0, tarr = 0;
  const int lat8 = 8 * (ADV_LAT + ((((uintptr_t)kbase >> 8) & 3) == 1 ? ADV_SLOW : 0));
  auto pace = [&](int n, bool steer) {
    int adv = 0;
    if (steer) {
      asm volatile("" ::: "memory");
      *(unsigned char*)(smem + CW_LAT + wave) = (unsigned char)min(tarr - tiss, 200u);
      asm volatile("" ::: "memory");
      const u32x2 lt = *(const u32x2*)(smem + CW_LAT);
      adv = __builtin_amdgcn_readfirstlane(*(const int*)(smem + CW_ADV));
      const int sum = __builtin_amdgcn_readfirstlane(__builtin_amdgcn_sad_u8(lt[0], 0, __builtin_amdgcn_sad_u8(lt[1], 0, 0)));
      const int line = max(n - 6, 0) * ((dpace * ADV_MAX) >> 10);
      const int step = min(max((lat8 - sum + (((line - adv) * ADV_PULL) >> 10)) * ADV_GAIN, -ADV_DOWN * 16), ADV_UP * 16);
      adv = min(max(adv + ((dpace * step) >> 14), -n * ((dpace * ADV_MIN) >> 10)), line);
      *(int*)(smem + CW_ADV) = adv;
    }
    const unsigned tg = tpace + (((n - 1) * dpace - max(n - nfast, 0) * dcut - adv) >> 8);
    while ((int)((tiss = now()) - tg) < 0) __builtin_amdgcn_s_sleep(1);
  };
  if (early > 0) issue(kbase, koff, 0);
  if (wave == 0) *(unsigned*)(smem + CW_ORIGIN) = tstart | 1u;

  unsigned contrib;
  {
    const int s = part >> 2;
    const int e = __builtin_amdgcn_readlane(send, s) + P, n = __builtin_amdgcn_readlane(cnt, s), ps = min(P, __builtin_amdgcn_readlane(npg, s));
    const int nxt = __builtin_amdgcn_ds_bpermute((lane + 1) * 4, bnd);
    contrib = (unsigned)__builtin_amdgcn_ballot_w64(nxt > bnd && (bnd < ps || (n > 0 && bnd < e && nxt > e - n)));
  }
  const unsigned um = nitem > 0 ? (unsigned)__builtin_amdgcn_ballot_w64(i0 < min(P, npg) || (cnt > 0 && i0 - P < send && i1 - P > send - cnt)) & 0xffu : 0u;
  const int nseq = __popc(um);
  const bool wide = nseq > 2;
  {
    const float span = (float)(npriv * 16 + P * WSH) * (PACE * (1.f / 32.f)) - (wide ? LEAD * 256.f : nseq == 2 ? LEAD2 * 256.f : 0.f);
    dpace = __builtin_amdgcn_readfirstlane((int)(fmaxf(span, 0.f) * __builtin_amdgcn_rcpf((float)max(2 * ntile, 1))));
    nfast = 2 * ntile - ENDN;
    dcut = wide ? 0 : __builtin_amdgcn_readfirstlane((int)((float)dpace * (float)(P * WSH) * __builtin_amdgcn_rcpf((float)(npriv * 16 + P * WSH))));
  }
  do {
    asm volatile("" ::: "memory");
    tpace = __builtin_amdgcn_readfirstlane(*(const unsigned*)(smem + CW_ORIGIN));
  } while (!tpace);
  const unsigned tend = tpace + (unsigned)(((npriv * 16 + P * WSH) >> 5) * PACE >> 8);
  if (nw > 0 && wave >= 2) {
    pace(wave, false);
    issue(kbase, koff, 0);
  }

  while (__hip_atomic_load((unsigned*)(smem + CW_Q), __ATOMIC_RELAXED, __HIP_MEMORY_SCOPE_WORKGROUP) == 0u) __builtin_amdgcn_s_sleep(1);
  asm volatile("" ::: "memory");
  const unsigned epoch = *(const unsigned*)(smem + LDS_EP + lane * 4);
  int lenv[2];
  u32x4 qf[2][4];
#pragma unroll
  for (int cg = 0; cg < 2; ++cg) {
    const int l = __builtin_amdgcn_ds_bpermute((4 * cg + (c >> 2)) * 4, lraw);
#pragma unroll
    for (int i = 0; i < 4; ++i) qf[cg][i] = *(const u32x4*)(smem + LDS_Q + ((cg * 4 + i) * 64 + lane) * 16);
    if (l <= 0) {
#pragma unroll
      for (int i = 0; i < 4; ++i) qf[cg][i] = u32x4{0, 0, 0, 0};
    }
    lenv[cg] = clamp_len(l) - 4 * g;
  }
  const unsigned meta = item < i1 ? (unsigned)(tix & (NBLK - 1)) * PAGE | (item < P ? 0xff0000u : 0x10000u << (tix >> 9)) : 0u;
  const int rk = (wave - nextra) & 7;
  int ncol = wide ? (nseq + 1 - (rk >> 2)) >> 1 : (unsigned)(rk - 1) < 4u ? nseq : 0;
  int mrd, lrd, pcol, phdr;
  unsigned ctv;
  {
    const int u = (lane >> 3) & 3;
    const int rank = ((um >> (lane & 7)) & 1) ? __popc(um & ((1u << (lane & 7)) - 1)) : -1;
    const unsigned long long hit = __builtin_amdgcn_ballot_w64(rank == (wide ? 2 * u + (rk >> 2) : u));
    const unsigned mine8 = (unsigned)(hit >> (8 * u)) & 0xffu;
    const bool on = mine8 != 0 && lane < 32;
    const int col = 4 * __builtin_ctz(mine8 | 0x80u) + (rk & 3);
    mrd = LDS_ML + (col * 40 + 32 + (lane & 7)) * 4;
    lrd = LDS_ML + (col * 40 + (lane & 7) * 4) * 4;
    pcol = col;
    const int slot = WS_SLOT0 + (col * NPART + part) * SLOT_BYTES;
    phdr = on ? slot + 1024 + (lane & 7) * 16 : OOB;
    ctv = (unsigned)__builtin_amdgcn_ds_bpermute(col * 4, (int)epoch) + 1;
  }
  int phdr0 = wide || lane >= 8 ? OOB : phdr;
  int ord = LDS_O + (__builtin_amdgcn_readfirstlane(pcol) * COL_F + 2 * lane) * 4;
  int prow = WS_SLOT0 + (__builtin_amdgcn_readfirstlane(pcol) * NPART + part) * SLOT_BYTES + lane * 16;
  unsigned ct = __builtin_amdgcn_readfirstlane(ctv);
  asm volatile("" : "+v"(mrd), "+v"(lrd), "+v"(ord), "+v"(prow), "+v"(ct), "+v"(phdr0), "+v"(phdr), "+v"(ctv), "+v"(pcol), "+s"(ncol));
  int wm = LDS_ML + (c * 40 + 32 + rk) * 4, wl = LDS_ML + (c * 40 + rk * 4 + g) * 4;
  int wrow[2] = {LDS_O + (c * COL_F + rk * HD + 4 * g) * 4, LDS_O + ((16 + c) * COL_F + rk * HD + 4 * g) * 4};
  asm volatile("" : "+v"(wm), "+v"(wl), "+v"(wrow[0]), "+v"(wrow[1]));

  float m[2] = {-INFINITY, -INFINITY}, ms[2] = {0.f, 0.f}, l[2] = {0.f, 0.f};
  f32x4 acc[2][8];
#pragma unroll
  for (int cg = 0; cg < 2; ++cg)
#pragma unroll
    for (int i = 0; i < 8; ++i) acc[cg][i] = f32x4{0.f, 0.f, 0.f, 0.f};
  __builtin_amdgcn_s_setprio(2);

  const int sc0 = LDS_O + ((lane >> 5) * COL_F + rk * HD) * 4 + (lane & 31) * 16;
  u32x4 pb[2];
  auto weights = [&](int k) {
    const unsigned mu = (unsigned)__builtin_amdgcn_readlane((int)meta, 2 * k) | (unsigned)__builtin_amdgcn_readlane((int)meta, 2 * k + 1);
#pragma unroll
    for (int cg = 0; cg < 2; ++cg) {
      if ((mu >> (16 + 4 * cg)) & 0xfu) {
#pragma unroll
        for (int s = 0; s < 2; ++s) {
          f32x4 x = *(const f32x4*)(smem + sc0 + (((k & 3) * 2 + cg) * 4 + 2 * s) * (COL_F * 4));
#pragma unroll
          for (int e = 0; e < 4; ++e) x[e] = __builtin_amdgcn_exp2f(__builtin_fmaf(x[e], SC_LOG2, -ms[cg]));
          l[cg] += (x[0] + x[1]) + (x[2] + x[3]);
          pb[cg][2 * s] = cvtpk(x[0], x[1]);
          pb[cg][2 * s + 1] = cvtpk(x[2], x[3]);
        }
      }
    }
    return mu;
  };
  auto operands = [&](u32x4 (&va)[8], int s) {
#pragma unroll
    for (int i = 0; i < 8; ++i) {
      const unsigned sel = (i & 1) ? 0x07060302u : 0x05040100u;
      va[i][2 * s] = __builtin_amdgcn_perm(buf[s][1][i >> 1], buf[s][0][i >> 1], sel);
      va[i][2 * s + 1] = __builtin_amdgcn_perm(buf[s][3][i >> 1], buf[s][2][i >> 1], sel);
    }
  };
  for (int k0 = 0; k0 < nw; k0 += 4) {
    const int k1 = min(k0 + 4, nw), nround = min(ntile - 8 * k0, 32);
    float mx[2] = {-INFINITY, -INFINITY};
    for (int k = k0; k < k1;) {
      u32x4 kt[2][4];
      quad_transpose(buf[0], kt[0]);
      asm volatile("" :: "v"(kt[0][0]));
      tarr = now();
      quad_transpose(buf[1], kt[1]);
      const unsigned mt[2] = {(unsigned)__builtin_amdgcn_readlane((int)meta, 2 * k), (unsigned)__builtin_amdgcn_readlane((int)meta, 2 * k + 1)};
#pragma unroll
      for (int cg = 0; cg < 2; ++cg) {
        if (((mt[0] | mt[1]) >> (16 + 4 * cg)) & 0xfu) {
#pragma unroll
          for (int s = 0; s < 2; ++s) {
            f32x4 x = {0.f, 0.f, 0.f, 0.f};
#pragma unroll
            for (int i = 0; i < 4; ++i) x = mfma32(kt[s][i], qf[cg][i], x);
            const int lim = ((mt[s] >> (16 + 4 * cg + (c >> 2))) & 1) ? lenv[cg] - (int)(mt[s] & 0xffffu) : 0;
#pragma unroll
            for (int e = 0; e < 4; ++e) x[e] = e < lim ? x[e] : -INFINITY;
            mx[cg] = fmx(fmx(fmx(mx[cg], x[0]), x[1]), fmx(x[2], x[3]));
            *(f32x4*)(smem + sc0 + (((k - k0) * 2 + cg) * 4 + 2 * s) * (COL_F * 4)) = x;
          }
        }
      }
      ++k;
      if (k < k1) {
        pace(16 * k0 + 8 * (k - k0) + wave, true);
        issue(kbase, koff, k);
      }
    }
    pace(16 * k0 + nround + wave, true);
    issue(vbase, voff, k0);
#pragma unroll
    for (int cg = 0; cg < 2; ++cg) {
      m[cg] = fmx(m[cg], swap32_max(swap16_max(mx[cg])));
      const float mn = m[cg] == -INFINITY ? 0.f : m[cg] * SC_LOG2;
      if (k0 > 0) {
        const float alpha = __builtin_amdgcn_exp2f(fminf(ms[cg] - mn, 0.f));
        l[cg] *= alpha;
#pragma unroll
        for (int i = 0; i < 8; ++i) acc[cg][i] *= alpha;
      }
      ms[cg] = mn;
    }
    for (int k = k0; k < min(k1, nw - 1);) {
      const unsigned mu = weights(k);
      ++k;
      u32x4 va[8];
      operands(va, 0);
      asm volatile("" :: "v"(va[7][1]));
      tarr = now();
      operands(va, 1);
#pragma unroll
      for (int cg = 0; cg < 2; ++cg) {
        if ((mu >> (16 + 4 * cg)) & 0xfu) {
#pragma unroll
          for (int i = 0; i < 8; ++i) acc[cg][i] = mfma32(va[i], pb[cg], acc[cg][i]);
        }
      }
      const bool more_v = k < k1;
      pace(more_v ? 16 * k0 + nround + 8 * (k - k0) + wave : 16 * k1 + wave, true);
      issue(more_v ? vbase : kbase, more_v ? voff : koff, k);
    }
  }

  {
    const unsigned mu = nw > 0 ? weights(nw - 1) : 0u;
#pragma unroll
    for (int cg = 0; cg < 2; ++cg) {
      *(float*)(smem + wm + cg * (16 * 160)) = m[cg] * SC_LOG2;
      *(float*)(smem + wl + cg * (16 * 160)) = l[cg];
      if (((um >> (4 * cg)) & 0xfu) && !((mu >> (16 + 4 * cg)) & 0xfu)) {
#pragma unroll
        for (int i = 0; i < 8; ++i) *(f32x4*)(smem + wrow[cg] + 64 * i) = acc[cg][i];
      }
    }
    u32x4 va[8];
    operands(va, 0);
    __builtin_amdgcn_sched_barrier(0);
    __builtin_amdgcn_s_waitcnt(0x0F70);
    asm volatile("" ::: "memory");
    *(unsigned char*)(smem + CW_STATS + rk) = 1;
#pragma unroll
    for (int cg = 0; cg < 2; ++cg) {
      if ((mu >> (16 + 4 * cg)) & 0xfu) {
        unsigned lo = 0x05040100u, hi = 0x07060302u;
        asm volatile("" : "+s"(lo), "+s"(hi));
#pragma unroll
        for (int i = 0; i < 8 + 2; ++i) {
          if (i < 8) {
            va[i][2] = __builtin_amdgcn_perm(buf[1][1][i >> 1], buf[1][0][i >> 1], (i & 1) ? hi : lo);
            va[i][3] = __builtin_amdgcn_perm(buf[1][3][i >> 1], buf[1][2][i >> 1], (i & 1) ? hi : lo);
          }
          if (i >= 2) *(f32x4*)(smem + wrow[cg] + 64 * (i - 2)) = acc[cg][i - 2];
          if (i < 8) acc[cg][i] = mfma32(va[i], pb[cg], acc[cg][i]);
          __builtin_amdgcn_sched_barrier(0);
        }
      }
    }
  }
  asm volatile("" ::: "memory");
  *(unsigned char*)(smem + CW_ROWS + rk) = 1;
  __builtin_amdgcn_s_setprio(0);

  if (ncol > 0) {
    for (;;) {
      asm volatile("" ::: "memory");
      const u32x2 dn = *(const u32x2*)(smem + CW_ROWS);
      if (__builtin_amdgcn_ballot_w64((dn[0] & (dn[1] | 0x01010100u)) == 0x01010101u)) break;
    }
    asm volatile("" ::: "memory");
    f32x2 o[NWAVE];
#pragma unroll
    for (int w = 0; w < 5; ++w) o[w] = *(const f32x2*)(smem + ord + w * (HD * 4));
    float mw;
    f32x4 lw;
    for (;;) {
      asm volatile("" ::: "memory");
      const unsigned dn = *(const unsigned*)(smem + CW_STATS + 4);
      asm volatile("" ::: "memory");
      mw = *(const float*)(smem + mrd);
      lw = *(const f32x4*)(smem + lrd);
      asm volatile("" : "+v"(mw), "+v"(lw));
      if (__builtin_amdgcn_ballot_w64(dn == 0x01010101u)) break;
    }
    const float M = max8(mw);
    const float f = __builtin_amdgcn_exp2f(mw - M);
    const float L = sum8(f * ((lw[0] + lw[1]) + (lw[2] + lw[3])));
    const u32x4 hdr = {__float_as_uint(M), ctv, __float_as_uint(L), ctv};
    __builtin_amdgcn_raw_buffer_store_b128(hdr, rws, phdr0, 0, AUX_SC1);
    float fw[8];
#pragma unroll
    for (int w = 0; w < 8; ++w) fw[w] = rdlane(f, w);
    asm volatile("" : "+s"(fw[0]), "+s"(fw[1]), "+s"(fw[2]), "+s"(fw[3]), "+s"(fw[4]), "+s"(fw[5]), "+s"(fw[6]), "+s"(fw[7]));
    f32x2 a = (o[0] * fw[0] + o[2] * fw[2] + o[4] * fw[4]) + (o[1] * fw[1] + o[3] * fw[3]);
    asm volatile("" : "+v"(a));
    for (;;) {
      asm volatile("" ::: "memory");
      const unsigned dn = *(const unsigned*)(smem + CW_ROWS + 4);
      asm volatile("" ::: "memory");
#pragma unroll
      for (int w = 5; w < NWAVE; ++w) o[w] = *(const f32x2*)(smem + ord + w * (HD * 4));
      asm volatile("" : "+v"(o[5]), "+v"(o[6]), "+v"(o[7]));
      if (__builtin_amdgcn_ballot_w64(dn == 0x01010101u)) break;
    }
    a = (a + o[5] * fw[5]) + (o[6] * fw[6] + o[7] * fw[7]);
    __builtin_amdgcn_raw_buffer_store_b128(u32x4{__float_as_uint(a[0]), ct, __float_as_uint(a[1]), ct}, rws, prow, 0, AUX_SC1);
    const int phdr1 = !wide && lane < 8 ? OOB : phdr;
#pragma unroll 1
    for (int u = 1; u < ncol; ++u) {
      const int col = __builtin_amdgcn_readlane(pcol, 8 * u);
#pragma unroll
      for (int w = 0; w < NWAVE; ++w) o[w] = *(const f32x2*)(smem + LDS_O + (col * COL_F + 2 * lane) * 4 + w * (HD * 4));
      if (u == ncol - 1) __builtin_amdgcn_raw_buffer_store_b128(hdr, rws, phdr1, 0, AUX_SC1);
      ct = __builtin_amdgcn_readlane(ctv, 8 * u);
      a = wsum8(o, f, 8 * u);
      __builtin_amdgcn_raw_buffer_store_b128(u32x4{__float_as_uint(a[0]), ct, __float_as_uint(a[1]), ct}, rws,
                                             WS_SLOT0 + (col * NPART + part) * SLOT_BYTES + lane * 16, 0, AUX_SC1);
    }
    if (wide && ncol == 1) __builtin_amdgcn_raw_buffer_store_b128(hdr, rws, phdr1, 0, AUX_SC1);
  }

  if (rk == 0) {
    const unsigned tag = (unsigned)__builtin_amdgcn_readlane((int)epoch, part) + 1;
    const bool oddi = (lane >> 3) & 1;
    int orow = (((part >> 2) * NH + 4 * kvh + (part & 3)) * HD + ((lane >> 1) & 3) * 32 + (lane & 1) * 16 + (lane >> 4) * 2 + (oddi ? 8 : 0)) * 2;
    unsigned osel = oddi ? 0x07060302u : 0x01000504u;
    float Mr = -3.0e38f, Lr = 0.f, a0 = 0.f, a1 = 0.f;
    unsigned spin = 0;
    const int ncon = __popc(contrib);
    const int crank = lane < 32 && ((contrib >> lane) & 1) ? __popc(contrib & ((1u << lane) - 1)) : 63;
    const int cpart = __builtin_amdgcn_ds_permute(crank * 4, lane);
    for (int base = 0; base < ncon; base += GS) {
      const int u16 = lane & 15;
      const bool hon = u16 < GS && base + u16 < ncon;
      const int sl = __builtin_amdgcn_ds_bpermute((base + u16) * 4, cpart);
      const int so = hon ? WS_SLOT0 + (part * NPART + sl) * SLOT_BYTES : OOB;
      const unsigned htag = hon ? tag : 0u;
      const float hbias = hon ? 0.f : -INFINITY;
      int doff[GS];
      unsigned etag[GS];
#pragma unroll
      for (int u = 0; u < GS; ++u) {
        doff[u] = __builtin_amdgcn_readlane(so, u) + lane * 16;
        etag[u] = __builtin_amdgcn_readlane(htag, u);
        asm volatile("" : "+v"(doff[u]), "+s"(etag[u]));
      }
      asm volatile("" : "+v"(orow), "+v"(osel));
      u32x4 h, d[GS];
      const int eoff = lane == 0 && base + GS >= ncon ? part * 4 : OOB;
      for (;;) {
        asm volatile("" ::: "memory");
        const int av = __builtin_amdgcn_readfirstlane(*(const int*)(smem + CW_ADV));
        if (wide || (int)(now() - (tend + POLL_AT - (av >> 8))) >= 0) break;
        __builtin_amdgcn_s_sleep(2);
      }
#pragma unroll 1
      for (; spin < SPIN_CAP; ++spin) {
        asm volatile("" ::: "memory");
        h = __builtin_amdgcn_raw_buffer_load_b128(rws, so + 1024, 0, AUX_SC1);
        if (!__builtin_amdgcn_ballot_w64(__builtin_amdgcn_bitop3_b32(h[1], h[3], htag, 0x7E) != 0)) break;
      }
#pragma unroll
      for (int u = 0; u < GS; ++u) d[u] = __builtin_amdgcn_raw_buffer_load_b128(rws, doff[u], 0, AUX_SC1);
      __builtin_amdgcn_raw_buffer_store_b32(tag, rws, eoff, 0, AUX_SC1);
      __builtin_amdgcn_sched_barrier(0);
      const float mu = __uint_as_float(h[0]) + hbias;
      float M = max8(mu);
      M = fmx(fmx(M, dppf<XOR15>(M)), Mr);
      float f = __builtin_amdgcn_exp2f(mu - M), fr = __builtin_amdgcn_exp2f(Mr - M);
      const float l8 = sum8(f * __uint_as_float(h[2]));
      Lr = (l8 + dppf<XOR15>(l8)) + fr * Lr;
      Mr = M;
      if (base + GS >= ncon) {
        const float inv = __builtin_amdgcn_rcpf(Lr);
        f *= inv;
        fr *= inv;
      }
      a0 *= fr;
      a1 *= fr;
      float fu[GS];
#pragma unroll
      for (int u = 0; u < GS; ++u) {
        fu[u] = rdlane(f, u);
        asm volatile("" : "+s"(fu[u]));
      }
      asm volatile("s_nop 1" : "+v"(a0), "+v"(a1));
      float t0, t1;
#pragma unroll 1
      for (;; ++spin) {
        unsigned bad = 0;
        t0 = a0;
        t1 = a1;
#pragma unroll
        for (int u = 0; u < GS; ++u) {
          bad |= __builtin_amdgcn_bitop3_b32(d[u][1], d[u][3], etag[u], 0x7E);
          asm volatile("v_fmac_f32 %0, %2, %3\n\tv_fmac_f32 %1, %2, %4" : "+v"(t0), "+v"(t1) : "s"(fu[u]), "v"(d[u][0]), "v"(d[u][2]));
          __builtin_amdgcn_sched_barrier(0);
        }
        if (!__builtin_amdgcn_ballot_w64(bad != 0) || spin >= SPIN_CAP) break;
        asm volatile("" ::: "memory");
#pragma unroll
        for (int u = 0; u < GS; ++u) d[u] = __builtin_amdgcn_raw_buffer_load_b128(rws, doff[u], 0, AUX_SC1);
        __builtin_amdgcn_raw_buffer_store_b32(tag, rws, eoff, 0, AUX_SC1);
      }
      a0 = t0;
      a1 = t1;
    }
    const unsigned pk = cvtpk(a0, a1), pp = dppu<0x128>(pk);
    __builtin_amdgcn_raw_buffer_store_b32(__builtin_amdgcn_perm(pk, pp, osel), rout, orow, 0, AUX_SC1);
    asm volatile(".Lcode_end:");
  }
}

}

extern "C" void kernel_launch(void* const* d_in, const int* in_sizes, int n_in, void* d_out, int out_size, void* d_ws,
                              size_t ws_size, hipStream_t stream) {
  (void)n_in; (void)out_size; (void)ws_size;
  (void)hipFuncSetAttribute((const void*)attn_fused, hipFuncAttributeMaxDynamicSharedMemorySize, LDS_BYTES);
  hipLaunchKernelGGL(attn_fused, dim3(NKVH * NPART), dim3(NTHR), LDS_BYTES, stream, (const char*)d_in[0], (const char*)d_in[1],
                     (const char*)d_in[2], (const int*)d_in[3], (const int*)d_in[4], (char*)d_ws, (__hip_bfloat16*)d_out, in_sizes[1] / (PAGE_BYTES / 2));
}
```
